```python
import torch

def _softmax(x, axis=-1):
    return torch.softmax(x, dim=axis)
def _gelu(x):
    return torch.nn.functional.gelu(x, approximate="tanh")
def _silu(x):
    return torch.nn.functional.silu(x)
def _sigmoid(x):
    return torch.sigmoid(x)
def _layernorm(x, axis=-1, eps=1e-5):
    m = x.mean(dim=axis, keepdim=True)
    v = x.var(dim=axis, keepdim=True, unbiased=False)
    return (x - m) / torch.sqrt(v + eps)
def _scatter_add(tgt, idx, src, axis=0):
    return tgt.clone().index_add_(axis, idx, src)
def _segment_sum(src, seg, n):
    out = torch.zeros((n,) + src.shape[1:], dtype=src.dtype)
    return out.index_add_(0, seg, src)
def _pad(x, pw):
    flat = tuple(v for pair in reversed(pw) for v in pair)
    return torch.nn.functional.pad(x, flat)

kernel_name = 'recurrent_scan__gqa_block_cached__mixer_channel_mlp__prompt'

def setup_inputs():

    _w = (lambda a: (lambda t: t.to(torch.bfloat16) if t.is_floating_point() else t)(torch.as_tensor(a)))
    def _A_setup():
        B, Sq, D = 1, 2048, 640
        Hq, Hkv, Sk, Dh = 32, 32, 2048, 192
        assert Hq % Hkv == 0, (Hq, Hkv)
        return {
            "x": _w((lambda *s: torch.randn(*s, dtype=torch.bfloat16) * 0.05)(B, Sq, D)),
            "k_cache": _w((lambda *s: torch.randn(*s, dtype=torch.bfloat16) * 1.6)(B, Hkv, Sk, Dh)),
            "v_cache": _w((lambda *s: torch.randn(*s, dtype=torch.bfloat16) * 0.8)(B, Hkv, Sk, Dh)),
            "wq": _w((lambda *s: torch.randn(*s, dtype=torch.bfloat16) * 1.6)(D, Hq * Dh) * (D ** -0.5)),
            "wo": _w((lambda *s: torch.randn(*s, dtype=torch.bfloat16) * 0.8)(Hq * Dh, D) * ((Hq * Dh) ** -0.5)),
        }
    _A_in = _A_setup()

    def _B_setup():
        B, S, D, F = 1, 2048, 640, 1536
        return {
            "x": _w((lambda *s: torch.randn(*s, dtype=torch.bfloat16) * 0.05)(B, S, D)),
            "w1": _w((lambda *s: torch.randn(*s, dtype=torch.bfloat16) * 3.2)(D, F) * (D ** -0.5)),
            "b1": _w((lambda *s: torch.randn(*s, dtype=torch.bfloat16) * 1.6)(F)),
            "w2": _w((lambda *s: torch.randn(*s, dtype=torch.bfloat16) * 0.2)(F, D) * (F ** -0.5)),
            "b2": _w((lambda *s: torch.randn(*s, dtype=torch.bfloat16) * 0.2)(D)),
        }
    _B_in = _B_setup()

    return {
        "A_x": _A_in["x"],
        "A_k_cache": _A_in["k_cache"],
        "A_v_cache": _A_in["v_cache"],
        "A_wq": _A_in["wq"],
        "A_wo": _A_in["wo"],
        "B_x": _B_in["x"],
        "B_w1": _B_in["w1"],
        "B_b1": _B_in["b1"],
        "B_w2": _B_in["w2"],
        "B_b2": _B_in["b2"],
    }

def reference(A_x, A_k_cache, A_v_cache, A_wq, A_wo, B_x, B_w1, B_b1, B_w2, B_b2):
    def _A_ref(x, k_cache, v_cache, wq, wo):
        B, Sq, D = x.shape
        Hq, Dh = 32, 192
        Hkv = k_cache.shape[1]
        rep = Hq // Hkv
        q = (lambda x, *d: x.permute(*d))((x @ wq).reshape(B, Sq, Hq, Dh), 0, 2, 1, 3)
        k = (lambda x, n, axis: torch.repeat_interleave(x, n, dim=axis))(k_cache, rep, axis=1)
        v = (lambda x, n, axis: torch.repeat_interleave(x, n, dim=axis))(v_cache, rep, axis=1)
        scores = torch.einsum("bhqd,bhkd->bhqk", q, k) * (Dh ** -0.5)
        p = _softmax(scores, axis=-1)
        o = torch.einsum("bhqk,bhkd->bhqd", p, v)
        return (lambda x, *d: x.permute(*d))(o, 0, 2, 1, 3).reshape(B, Sq, Hq * Dh) @ wo

    def _B_ref(x, w1, b1, w2, b2):
        y = _layernorm(x, axis=-1)
        h = _gelu(torch.einsum("bsd,df->bsf", y, w1) + b1)
        return torch.einsum("bsf,fd->bsd", h, w2) + b2

    x = A_x
    xn = _layernorm(x, axis=-1)
    s = _A_ref(xn, A_k_cache, A_v_cache, A_wq, A_wo)
    g = _silu(_B_ref(xn, B_w1, B_b1, B_w2, B_b2))
    return x + s * g
```

```cpp
#include <hip/hip_runtime.h>
#include <hip/hip_bf16.h>
#include <stdint.h>

typedef __attribute__((ext_vector_type(8))) short bf16x8;
typedef __attribute__((ext_vector_type(8))) __bf16 bf16x8b;
typedef __attribute__((ext_vector_type(4))) short s16x4;
typedef __attribute__((ext_vector_type(16))) float f32x16;
typedef __attribute__((ext_vector_type(4))) float f32x4;
typedef __attribute__((ext_vector_type(2))) float f32x2;
typedef __attribute__((ext_vector_type(2))) __bf16 bf16x2b;
typedef __attribute__((ext_vector_type(4))) unsigned u32x4;
typedef __attribute__((ext_vector_type(2))) unsigned u32x2;
typedef __attribute__((address_space(3))) char* lds_ptr;
typedef unsigned short u16;

#define DI __device__ __forceinline__

constexpr int SEQ = 2048, DM = 640, NH = 32, DH = 192, FF = 1536, QD = NH * DH;

DI f32x16 MFMA(bf16x8 a, bf16x8 b, f32x16 c) {
  return __builtin_amdgcn_mfma_f32_32x32x16_bf16(__builtin_bit_cast(bf16x8b, a), __builtin_bit_cast(bf16x8b, b), c, 0, 0, 0);
}
DI unsigned cvtpk(float lo, float hi) {
  f32x2 v = {lo, hi};
  bf16x2b b = __builtin_convertvector(v, bf16x2b);
  return __builtin_bit_cast(unsigned, b);
}
DI float bf2f(u16 v) { return __uint_as_float(((unsigned)v) << 16); }
DI float rbf(float v) { return __uint_as_float(cvtpk(v, 0.f) << 16); }
DI s16x4 trread(lds_ptr p) {
  return __builtin_amdgcn_ds_read_tr16_b64_v4i16((__attribute__((address_space(3))) s16x4*)p);
}
DI bf16x8 ldsread(lds_ptr p) { return *(__attribute__((address_space(3))) bf16x8*)p; }
DI bf16x8 cat(s16x4 a, s16x4 b) { return __builtin_shufflevector(a, b, 0, 1, 2, 3, 4, 5, 6, 7); }
DI void glds16(const void* g, lds_ptr l) {
  __builtin_amdgcn_global_load_lds((const __attribute__((address_space(1))) unsigned*)g,
                                   (__attribute__((address_space(3))) unsigned*)l, 16, 0, 0);
}

DI float wave_sum(float v) {
#pragma unroll
  for (int o = 32; o > 0; o >>= 1) v += __shfl_xor(v, o, 64);
  return v;
}
DI void ln_row(float (&v)[10]) {
  float s = 0.f;
#pragma unroll
  for (int i = 0; i < 10; ++i) s += v[i];
  s = wave_sum(s);
  const float mean = s * (1.0f / DM);
  float q = 0.f;
#pragma unroll
  for (int i = 0; i < 10; ++i) { float d = v[i] - mean; q += d * d; }
  q = wave_sum(q);
  const float mb = rbf(mean), vb = rbf(q * (1.0f / DM));
  const float den = rbf(sqrtf(rbf(vb + 1e-5f)));
#pragma unroll
  for (int i = 0; i < 10; ++i) v[i] = rbf(rbf(v[i] - mb) / den);
}
__global__ __launch_bounds__(256) void k_ln(const u16* __restrict__ x, u16* __restrict__ xnp, u16* __restrict__ y) {
  const int row = blockIdx.x * 4 + (threadIdx.x >> 6), lane = threadIdx.x & 63;
  const unsigned* xr = (const unsigned*)(x + (size_t)row * DM);
  float v[10];
#pragma unroll
  for (int i = 0; i < 5; ++i) {
    unsigned w = xr[lane + 64 * i];
    v[2 * i] = __uint_as_float(w << 16);
    v[2 * i + 1] = __uint_as_float(w & 0xffff0000u);
  }
  ln_row(v);
  unsigned* xo = (unsigned*)(xnp + (size_t)row * DM);
#pragma unroll
  for (int i = 0; i < 5; ++i) {
    int e = lane + 64 * i;
    int ep = (e & ~6) | ((e & 2) << 1) | ((e & 4) >> 1);
    xo[ep] = (__float_as_uint(v[2 * i]) >> 16) | (__float_as_uint(v[2 * i + 1]) & 0xffff0000u);
  }
  ln_row(v);
  unsigned* yo = (unsigned*)(y + (size_t)row * DM);
#pragma unroll
  for (int i = 0; i < 5; ++i)
    yo[lane + 64 * i] = (__float_as_uint(v[2 * i]) >> 16) | (__float_as_uint(v[2 * i + 1]) & 0xffff0000u);
}

DI void gemm_tile_128(const u16* __restrict__ A, int lda, const u16* __restrict__ B, int ldb, int kiters,
                      lds_ptr lds, f32x16 (&acc)[2][2]) {
  const int tid = threadIdx.x, lane = tid & 63, wid = __builtin_amdgcn_readfirstlane(tid >> 6);
  const int r32 = lane & 31, hi = lane >> 5;
  const int wm = wid >> 1, wn = wid & 1;
  const int arow = 32 * wid + (lane >> 3);
  const u16* asrc[4];
#pragma unroll
  for (int i = 0; i < 4; ++i) {
    int row = arow + 8 * i;
    asrc[i] = A + (size_t)row * lda + (((lane & 7) ^ ((row >> 1) & 7)) << 3);
  }
  const u16* bsrc[4];
#pragma unroll
  for (int i = 0; i < 4; ++i) {
    int ii = 4 * wid + i, kg = ii >> 1, nb = 2 * (ii & 1) + (lane >> 5);
    bsrc[i] = B + (size_t)(8 * kg + ((lane >> 2) & 7)) * ldb + nb * 32 + (lane & 3) * 8;
  }
  auto stage = [&](int kt, int buf) {
    lds_ptr la = lds + buf * 32768 + wid * 4096;
    lds_ptr lb = lds + buf * 32768 + 16384 + wid * 4096;
#pragma unroll
    for (int i = 0; i < 4; ++i) glds16(asrc[i] + kt * 64, la + i * 1024);
#pragma unroll
    for (int i = 0; i < 4; ++i) glds16(bsrc[i] + (size_t)kt * 64 * ldb, lb + i * 1024);
  };
  int aoff[2][4];
#pragma unroll
  for (int mb = 0; mb < 2; ++mb)
#pragma unroll
    for (int ks = 0; ks < 4; ++ks) {
      int row = wm * 64 + mb * 32 + r32, c = 2 * ks + hi;
      aoff[mb][ks] = row * 128 + ((c ^ ((row >> 1) & 7)) << 4);
    }
  const int boff = 16384 + hi * 2048 + ((lane >> 2) & 3) * 64 + ((lane >> 4) & 1) * 32 + (lane & 3) * 8 + wn * 1024;
#pragma unroll
  for (int a = 0; a < 2; ++a)
#pragma unroll
    for (int b = 0; b < 2; ++b) acc[a][b] = f32x16{};
  stage(0, 0);
  for (int kt = 0; kt < kiters; ++kt) {
    __syncthreads();
    const int buf = kt & 1;
    if (kt + 1 < kiters) stage(kt + 1, buf ^ 1);
    lds_ptr base = lds + buf * 32768;
#pragma unroll
    for (int ks = 0; ks < 4; ++ks) {
      bf16x8 af[2], bfr[2];
#pragma unroll
      for (int mb = 0; mb < 2; ++mb) af[mb] = ldsread(base + aoff[mb][ks]);
#pragma unroll
      for (int nb = 0; nb < 2; ++nb) {
        lds_ptr p = base + boff + ks * 4096 + nb * 512;
        bfr[nb] = cat(trread(p), trread(p + 256));
      }
#pragma unroll
      for (int mb = 0; mb < 2; ++mb)
#pragma unroll
        for (int nb = 0; nb < 2; ++nb) acc[mb][nb] = MFMA(af[mb], bfr[nb], acc[mb][nb]);
    }
  }
}
DI int crow(int r, int hi) { return (r & 3) + 8 * (r >> 2) + 4 * hi; }

DI float gelu_tanh(float v) {
  const float u = 0.7978845608028654f * (v + 0.044715f * v * v * v);
  const float e = __expf(2.f * u);
  const float t = 1.f - 2.f / (e + 1.f);
  return 0.5f * v * (1.f + t);
}

__global__ __launch_bounds__(256) void k_gemm1(const u16* __restrict__ y, const u16* __restrict__ w1,
                                               const u16* __restrict__ b1, u16* __restrict__ h) {
  extern __shared__ __attribute__((aligned(16))) char smem[];
  const int n0 = blockIdx.x * 128, m0 = blockIdx.y * 128;
  f32x16 acc[2][2];
  gemm_tile_128(y + (size_t)m0 * DM, DM, w1 + n0, FF, DM / 64, (lds_ptr)smem, acc);
  const int lane = threadIdx.x & 63, wid = threadIdx.x >> 6, r32 = lane & 31, hi = lane >> 5;
  const int wm = wid >> 1, wn = wid & 1;
#pragma unroll
  for (int nb = 0; nb < 2; ++nb) {
    const int n = n0 + wn * 64 + nb * 32 + r32;
    const float bias = bf2f(b1[n]);
#pragma unroll
    for (int mb = 0; mb < 2; ++mb)
#pragma unroll
      for (int r = 0; r < 16; ++r) {
        const int m = m0 + wm * 64 + mb * 32 + crow(r, hi);
        float v = rbf(rbf(acc[mb][nb][r]) + bias);
        h[(size_t)m * FF + n] = (u16)(cvtpk(gelu_tanh(v), 0.f) & 0xffff);
      }
  }
}

__global__ __launch_bounds__(256) void k_gemm2(const u16* __restrict__ O, const u16* __restrict__ wo,
                                               const u16* __restrict__ h, const u16* __restrict__ w2,
                                               float* __restrict__ part) {
  extern __shared__ __attribute__((aligned(16))) char smem[];
  const int n0 = blockIdx.x * 128, m0 = blockIdx.y * 128, c = blockIdx.z;
  const u16* A; const u16* B; int lda;
  if (c < 4) { A = O + (size_t)m0 * QD + c * 1536; lda = QD; B = wo + (size_t)c * 1536 * DM + n0; }
  else { A = h + (size_t)m0 * FF; lda = FF; B = w2 + n0; }
  f32x16 acc[2][2];
  gemm_tile_128(A, lda, B, DM, 1536 / 64, (lds_ptr)smem, acc);
  const int lane = threadIdx.x & 63, wid = threadIdx.x >> 6, r32 = lane & 31, hi = lane >> 5;
  const int wm = wid >> 1, wn = wid & 1;
  float* P = part + (size_t)c * SEQ * DM;
#pragma unroll
  for (int nb = 0; nb < 2; ++nb) {
    const int n = n0 + wn * 64 + nb * 32 + r32;
#pragma unroll
    for (int mb = 0; mb < 2; ++mb)
#pragma unroll
      for (int r = 0; r < 16; ++r) {
        const int m = m0 + wm * 64 + mb * 32 + crow(r, hi);
        P[(size_t)m * DM + n] = acc[mb][nb][r];
      }
  }
}

__global__ __launch_bounds__(256) void k_final(const u16* __restrict__ x, const float* __restrict__ part,
                                               const u16* __restrict__ b2, u16* __restrict__ out) {
  const int idx = (blockIdx.x * 256 + threadIdx.x) * 4;
  constexpr size_t PS = (size_t)SEQ * DM;
  const f32x4 p0 = *(const f32x4*)(part + idx), p1 = *(const f32x4*)(part + PS + idx),
              p2 = *(const f32x4*)(part + 2 * PS + idx), p3 = *(const f32x4*)(part + 3 * PS + idx),
              pm = *(const f32x4*)(part + 4 * PS + idx);
  const u32x2 xw = *(const u32x2*)(x + idx);
  const int n = idx % DM;
  const u32x2 bw = *(const u32x2*)(b2 + n);
  float xo[4] = {__uint_as_float(xw[0] << 16), __uint_as_float(xw[0] & 0xffff0000u), __uint_as_float(xw[1] << 16),
                 __uint_as_float(xw[1] & 0xffff0000u)};
  float bo[4] = {__uint_as_float(bw[0] << 16), __uint_as_float(bw[0] & 0xffff0000u), __uint_as_float(bw[1] << 16),
                 __uint_as_float(bw[1] & 0xffff0000u)};
  float r[4];
#pragma unroll
  for (int i = 0; i < 4; ++i) {
    const float s = rbf((p0[i] + p1[i]) + (p2[i] + p3[i]));
    const float m = rbf(rbf(pm[i]) + bo[i]);
    const float g = rbf(m / (1.f + __expf(-m)));
    r[i] = xo[i] + rbf(s * g);
  }
  u32x2 o = {cvtpk(r[0], r[1]), cvtpk(r[2], r[3])};
  *(u32x2*)(out + idx) = o;
}

constexpr int TB = 24576;
constexpr float QSCALE = 0.07216878364870322f * 1.4426950408889634f;
constexpr float THR = 8.0f;

__global__ __launch_bounds__(512, 2) void k_attn(const u16* __restrict__ xnp, const u16* __restrict__ wq,
                                                 const u16* __restrict__ Kc, const u16* __restrict__ Vc,
                                                 u16* __restrict__ O) {
  extern __shared__ __attribute__((aligned(16))) char smem[];
  lds_ptr lds = (lds_ptr)smem;
  const int tid = threadIdx.x, lane = tid & 63, wid = __builtin_amdgcn_readfirstlane(tid >> 6);
  const int r32 = lane & 31, hi = lane >> 5;
  const int bid = blockIdx.x, xcd = bid & 7, idx = bid >> 3;
  const int head = xcd * 4 + (idx >> 3), qb = idx & 7;
  const int q0 = qb * 256 + wid * 32;

  const int krow = 8 * wid + (lane >> 3);
  const u16* ksrc = Kc + (size_t)head * SEQ * DH + (size_t)krow * DH + (((lane & 7) ^ ((krow >> 1) & 7)) << 3);
  const int vrow = 8 * wid + ((lane >> 2) & 7), vcol = (lane >> 5) * 32 + (lane & 3) * 8;
  const u16* vsrc = Vc + (size_t)head * SEQ * DH + (size_t)vrow * DH + vcol;
  const u16* wsrc = wq + (size_t)vrow * QD + head * DH + vcol;
  auto stageK = [&](int t, int buf) {
    lds_ptr d = lds + buf * TB + wid * 1024;
    const u16* s = ksrc + (size_t)t * 64 * DH;
#pragma unroll
    for (int cb = 0; cb < 3; ++cb) glds16(s + cb * 64, d + cb * 8192);
  };
  auto stageV = [&](int t, int buf) {
    lds_ptr d = lds + 2 * TB + buf * TB + wid * 3072;
    const u16* s = vsrc + (size_t)t * 64 * DH;
#pragma unroll
    for (int p = 0; p < 3; ++p) glds16(s + p * 64, d + p * 1024);
  };
  auto stageW = [&](int t) {
    lds_ptr d = lds + 2 * TB + (t & 1) * TB + wid * 3072;
    const u16* s = wsrc + (size_t)t * 64 * QD;
#pragma unroll
    for (int p = 0; p < 3; ++p) glds16(s + p * 64, d + p * 1024);
  };
  int koff[4];
#pragma unroll
  for (int j = 0; j < 4; ++j) koff[j] = r32 * 128 + (((2 * j + hi) ^ ((r32 >> 1) & 7)) << 4);
  const int voff = 2 * TB + (hi * 4 + ((lane >> 2) & 3)) * 64 + ((lane >> 4) & 1) * 32 + (lane & 3) * 8;

  f32x16 acc[6];
#pragma unroll
  for (int d = 0; d < 6; ++d) acc[d] = f32x16{};

  const u16* xsrc = xnp + (size_t)(q0 + r32) * DM + hi * 8;
  bf16x8 xf[4];
  stageK(0, 0);
  stageW(0);
#pragma unroll
  for (int ks = 0; ks < 4; ++ks) xf[ks] = *(const bf16x8*)(xsrc + ks * 16);
  for (int t = 0; t < 10; ++t) {
    __syncthreads();
    if (t + 1 < 10) stageW(t + 1); else stageV(0, 0);
    bf16x8 xc[4];
#pragma unroll
    for (int ks = 0; ks < 4; ++ks) xc[ks] = xf[ks];
    if (t + 1 < 10) {
#pragma unroll
      for (int ks = 0; ks < 4; ++ks) xf[ks] = *(const bf16x8*)(xsrc + (t + 1) * 64 + ks * 16);
    }
    lds_ptr vb = lds + voff + (t & 1) * TB;
#pragma unroll
    for (int ks = 0; ks < 4; ++ks)
#pragma unroll
      for (int db = 0; db < 6; ++db) {
        lds_ptr p = vb + ((2 * ks) * 6 + db) * 512;
        bf16x8 a = cat(trread(p), trread(p + 3072));
        acc[db] = MFMA(a, xc[ks], acc[db]);
      }
  }
  bf16x8 qf[12];
#pragma unroll
  for (int db = 0; db < 6; ++db)
#pragma unroll
    for (int s = 0; s < 2; ++s) {
      unsigned w0 = cvtpk(acc[db][8 * s + 0] * QSCALE, acc[db][8 * s + 1] * QSCALE);
      unsigned w1 = cvtpk(acc[db][8 * s + 2] * QSCALE, acc[db][8 * s + 3] * QSCALE);
      unsigned w2 = cvtpk(acc[db][8 * s + 4] * QSCALE, acc[db][8 * s + 5] * QSCALE);
      unsigned w3 = cvtpk(acc[db][8 * s + 6] * QSCALE, acc[db][8 * s + 7] * QSCALE);
      auto r0 = __builtin_amdgcn_permlane32_swap(w0, w2, false, false);
      auto r1 = __builtin_amdgcn_permlane32_swap(w1, w3, false, false);
      u32x4 w = {r0[0], r1[0], r0[1], r1[1]};
      qf[2 * db + s] = __builtin_bit_cast(bf16x8, w);
    }
#pragma unroll
  for (int d = 0; d < 6; ++d) acc[d] = f32x16{};

  float m_run = -1e30f, l_run = 0.f;
#pragma unroll 1
  for (int t = 0; t < 32; ++t) {
    __syncthreads();
    { const int tn = t + 1 < 32 ? t + 1 : 31; stageK(tn, (t + 1) & 1); stageV(tn, (t + 1) & 1); }
    lds_ptr kb = lds + (t & 1) * TB;
    f32x16 s0 = f32x16{}, s1 = f32x16{};
#pragma unroll
    for (int d0 = 0; d0 < 12; ++d0) {
      lds_ptr p = kb + koff[d0 & 3] + (d0 >> 2) * 8192;
      bf16x8 a0 = ldsread(p), a1 = ldsread(p + 4096);
      s0 = MFMA(a0, qf[d0], s0);
      s1 = MFMA(a1, qf[d0], s1);
    }
    float pm = s0[0];
#pragma unroll
    for (int r = 1; r < 16; ++r) pm = fmaxf(pm, s0[r]);
#pragma unroll
    for (int r = 0; r < 16; ++r) pm = fmaxf(pm, s1[r]);
    {
      auto rr = __builtin_amdgcn_permlane32_swap(__float_as_uint(pm), __float_as_uint(pm), false, false);
      pm = fmaxf(__uint_as_float(rr[0]), __uint_as_float(rr[1]));
    }
    if (__builtin_expect(__any(pm - m_run > THR), 0)) {
      const float mn = fmaxf(m_run, pm);
      const float alpha = __builtin_amdgcn_exp2f(m_run - mn);
      m_run = mn;
      l_run *= alpha;
#pragma unroll
      for (int d = 0; d < 6; ++d)
#pragma unroll
        for (int r = 0; r < 16; ++r) acc[d][r] *= alpha;
    }
    float ls = 0.f;
#pragma unroll
    for (int r = 0; r < 16; ++r) { s0[r] = __builtin_amdgcn_exp2f(s0[r] - m_run); ls += s0[r]; }
#pragma unroll
    for (int r = 0; r < 16; ++r) { s1[r] = __builtin_amdgcn_exp2f(s1[r] - m_run); ls += s1[r]; }
    l_run += ls;
    bf16x8 pf[4];
#pragma unroll
    for (int s = 0; s < 2; ++s) {
      u32x4 w = {cvtpk(s0[8 * s], s0[8 * s + 1]), cvtpk(s0[8 * s + 2], s0[8 * s + 3]), cvtpk(s0[8 * s + 4], s0[8 * s + 5]),
                 cvtpk(s0[8 * s + 6], s0[8 * s + 7])};
      pf[s] = __builtin_bit_cast(bf16x8, w);
      u32x4 w2 = {cvtpk(s1[8 * s], s1[8 * s + 1]), cvtpk(s1[8 * s + 2], s1[8 * s + 3]),
                  cvtpk(s1[8 * s + 4], s1[8 * s + 5]), cvtpk(s1[8 * s + 6], s1[8 * s + 7])};
      pf[2 + s] = __builtin_bit_cast(bf16x8, w2);
    }
    lds_ptr vb = lds + voff + (t & 1) * TB;
#pragma unroll
    for (int ks = 0; ks < 4; ++ks)
#pragma unroll
      for (int db = 0; db < 6; ++db) {
        lds_ptr p = vb + ((2 * ks) * 6 + db) * 512;
        bf16x8 a = cat(trread(p), trread(p + 3072));
        acc[db] = MFMA(a, pf[ks], acc[db]);
      }
  }
  {
    auto rr = __builtin_amdgcn_permlane32_swap(__float_as_uint(l_run), __float_as_uint(l_run), false, false);
    l_run = __uint_as_float(rr[0]) + __uint_as_float(rr[1]);
  }
  const float rl = 1.0f / l_run;
  int tz = threadIdx.x;
  asm volatile("" : "+v"(tz));
  u16* orow = O + (size_t)(qb * 256 + (tz >> 6) * 32 + (tz & 31)) * QD + head * DH + ((tz >> 5) & 1) * 8;
#pragma unroll
  for (int db = 0; db < 6; ++db)
#pragma unroll
    for (int s = 0; s < 2; ++s) {
      unsigned w0 = cvtpk(acc[db][8 * s + 0] * rl, acc[db][8 * s + 1] * rl);
      unsigned w1 = cvtpk(acc[db][8 * s + 2] * rl, acc[db][8 * s + 3] * rl);
      unsigned w2 = cvtpk(acc[db][8 * s + 4] * rl, acc[db][8 * s + 5] * rl);
      unsigned w3 = cvtpk(acc[db][8 * s + 6] * rl, acc[db][8 * s + 7] * rl);
      auto r0 = __builtin_amdgcn_permlane32_swap(w0, w2, false, false);
      auto r1 = __builtin_amdgcn_permlane32_swap(w1, w3, false, false);
      u32x4 w = {r0[0], r1[0], r0[1], r1[1]};
      *(u32x4*)(orow + db * 32 + s * 16) = w;
    }
}

extern "C" void kernel_launch(void* const* d_in, const int* in_sizes, int n_in, void* d_out, int out_size,
                              void* d_ws, size_t ws_size, hipStream_t stream) {
  const u16* x = (const u16*)d_in[0];
  const u16* kc = (const u16*)d_in[1];
  const u16* vc = (const u16*)d_in[2];
  const u16* wq = (const u16*)d_in[3];
  const u16* wo = (const u16*)d_in[4];
  const u16* w1 = (const u16*)d_in[6];
  const u16* b1 = (const u16*)d_in[7];
  const u16* w2 = (const u16*)d_in[8];
  const u16* b2 = (const u16*)d_in[9];
  char* ws = (char*)d_ws;
  u16* xnp = (u16*)(ws);
  u16* y = (u16*)(ws + (4u << 20));
  u16* h = (u16*)(ws + (8u << 20));
  u16* O = (u16*)(ws + (16u << 20));
  float* part = (float*)(ws + (48u << 20));

  hipFuncSetAttribute((const void*)k_attn, hipFuncAttributeMaxDynamicSharedMemorySize, 4 * TB);
  hipFuncSetAttribute((const void*)k_gemm1, hipFuncAttributeMaxDynamicSharedMemorySize, 65536);
  hipFuncSetAttribute((const void*)k_gemm2, hipFuncAttributeMaxDynamicSharedMemorySize, 65536);

  k_ln<<<SEQ / 4, 256, 0, stream>>>(x, xnp, y);
  k_gemm1<<<dim3(FF / 128, SEQ / 128), 256, 65536, stream>>>(y, w1, b1, h);
  k_attn<<<256, 512, 4 * TB, stream>>>(xnp, wq, kc, vc, O);
  k_gemm2<<<dim3(DM / 128, SEQ / 128, 5), 256, 65536, stream>>>(O, wo, h, w2, part);
  k_final<<<SEQ * DM / 1024, 256, 0, stream>>>(x, part, b2, (u16*)d_out);
}
```

```cpp
#include <hip/hip_runtime.h>
#include <hip/hip_bf16.h>
#include <stdint.h>

typedef __attribute__((ext_vector_type(8))) short bf16x8;
typedef __attribute__((ext_vector_type(8))) __bf16 bf16x8b;
typedef __attribute__((ext_vector_type(4))) short s16x4;
typedef __attribute__((ext_vector_type(16))) float f32x16;
typedef __attribute__((ext_vector_type(4))) float f32x4;
typedef __attribute__((ext_vector_type(2))) float f32x2;
typedef __attribute__((ext_vector_type(2))) __bf16 bf16x2b;
typedef __attribute__((ext_vector_type(4))) unsigned u32x4;
typedef __attribute__((ext_vector_type(2))) unsigned u32x2;
typedef __attribute__((address_space(3))) char* lds_ptr;
typedef unsigned short u16;

#define DI __device__ __forceinline__

constexpr int SEQ = 2048, DM = 640, NH = 32, DH = 192, FF = 1536, QD = NH * DH;

DI f32x16 MFMA(bf16x8 a, bf16x8 b, f32x16 c) {
  return __builtin_amdgcn_mfma_f32_32x32x16_bf16(__builtin_bit_cast(bf16x8b, a), __builtin_bit_cast(bf16x8b, b), c, 0, 0, 0);
}
DI unsigned cvtpk(float lo, float hi) {
  f32x2 v = {lo, hi};
  bf16x2b b = __builtin_convertvector(v, bf16x2b);
  return __builtin_bit_cast(unsigned, b);
}
DI float bf2f(u16 v) { return __uint_as_float(((unsigned)v) << 16); }
DI float rbf(float v) { return __uint_as_float(cvtpk(v, 0.f) << 16); }
DI s16x4 trread(lds_ptr p) {
  return __builtin_amdgcn_ds_read_tr16_b64_v4i16((__attribute__((address_space(3))) s16x4*)p);
}
DI bf16x8 ldsread(lds_ptr p) { return *(__attribute__((address_space(3))) bf16x8*)p; }
DI bf16x8 cat(s16x4 a, s16x4 b) { return __builtin_shufflevector(a, b, 0, 1, 2, 3, 4, 5, 6, 7); }
DI void glds16(const void* g, lds_ptr l) {
  __builtin_amdgcn_global_load_lds((const __attribute__((address_space(1))) unsigned*)g,
                                   (__attribute__((address_space(3))) unsigned*)l, 16, 0, 0);
}

DI float wave_sum(float v) {
#pragma unroll
  for (int o = 32; o > 0; o >>= 1) v += __shfl_xor(v, o, 64);
  return v;
}
DI void ln_row(float (&v)[10]) {
  float s = 0.f;
#pragma unroll
  for (int i = 0; i < 10; ++i) s += v[i];
  s = wave_sum(s);
  const float mean = s * (1.0f / DM);
  float q = 0.f;
#pragma unroll
  for (int i = 0; i < 10; ++i) { float d = v[i] - mean; q += d * d; }
  q = wave_sum(q);
  const float mb = rbf(mean), vb = rbf(q * (1.0f / DM));
  const float den = rbf(sqrtf(rbf(vb + 1e-5f)));
#pragma unroll
  for (int i = 0; i < 10; ++i) v[i] = rbf(rbf(v[i] - mb) / den);
}
__global__ __launch_bounds__(256) void k_ln(const u16* __restrict__ x, u16* __restrict__ xnp, u16* __restrict__ y) {
  const int row = blockIdx.x * 4 + (threadIdx.x >> 6), lane = threadIdx.x & 63;
  const unsigned* xr = (const unsigned*)(x + (size_t)row * DM);
  float v[10];
#pragma unroll
  for (int i = 0; i < 5; ++i) {
    unsigned w = xr[lane + 64 * i];
    v[2 * i] = __uint_as_float(w << 16);
    v[2 * i + 1] = __uint_as_float(w & 0xffff0000u);
  }
  ln_row(v);
  unsigned* xo = (unsigned*)(xnp + (size_t)row * DM);
#pragma unroll
  for (int i = 0; i < 5; ++i) {
    int e = lane + 64 * i;
    int ep = (e & ~6) | ((e & 2) << 1) | ((e & 4) >> 1);
    xo[ep] = (__float_as_uint(v[2 * i]) >> 16) | (__float_as_uint(v[2 * i + 1]) & 0xffff0000u);
  }
  ln_row(v);
  unsigned* yo = (unsigned*)(y + (size_t)row * DM);
#pragma unroll
  for (int i = 0; i < 5; ++i)
    yo[lane + 64 * i] = (__float_as_uint(v[2 * i]) >> 16) | (__float_as_uint(v[2 * i + 1]) & 0xffff0000u);
}

DI void glds_g(const void* sbase, unsigned voff, unsigned ldsaddr) {
  asm volatile("s_mov_b32 m0, %2\n\ts_nop 0\n\tglobal_load_lds_dwordx4 %0, %1"
               :: "v"(voff), "s"(sbase), "s"(ldsaddr) : "memory");
}
template <int BM>
DI void gemm_tile(const u16* __restrict__ A, int lda, const u16* __restrict__ B, int ldb, int kiters,
                  unsigned lds0, f32x16 (&acc)[BM / 128][2]) {
  constexpr int MB = BM / 128;
  constexpr int ABYTES = BM * 128;
  constexpr int STAGE = ABYTES + 16384;
  constexpr int NA = BM / 64;
  const int tid = threadIdx.x, lane = tid & 63, wid = __builtin_amdgcn_readfirstlane(tid >> 6);
  const int r32 = lane & 31, hi = lane >> 5;
  const int wm = wid >> 1, wn = wid & 1;
  unsigned avo[NA];
#pragma unroll
  for (int i = 0; i < NA; ++i) {
    const int row = 8 * (NA * wid + i) + (lane >> 3);
    avo[i] = (unsigned)(row * lda * 2 + (((lane & 7) ^ ((row >> 1) & 7)) << 4));
  }
  const unsigned bvo = (unsigned)((8 * wid + ((lane >> 2) & 7)) * ldb * 2 + (lane >> 5) * 64 + (lane & 3) * 16);
  auto stage = [&](int kt, unsigned slot) {
    const char* ap = (const char*)A + (size_t)kt * 128;
    const char* bp = (const char*)B + (size_t)kt * 64 * ldb * 2;
#pragma unroll
    for (int i = 0; i < NA; ++i) glds_g(ap, avo[i], lds0 + slot + (NA * wid + i) * 1024);
    glds_g(bp, bvo, lds0 + slot + ABYTES + wid * 2048);
    glds_g(bp + 128, bvo, lds0 + slot + ABYTES + wid * 2048 + 1024);
  };
  unsigned aoff[MB][4];
#pragma unroll
  for (int mb = 0; mb < MB; ++mb)
#pragma unroll
    for (int ks = 0; ks < 4; ++ks) {
      const int row = wm * (32 * MB) + mb * 32 + r32, c = 2 * ks + hi;
      aoff[mb][ks] = lds0 + row * 128 + ((c ^ ((row >> 1) & 7)) << 4);
    }
  const unsigned boff = lds0 + ABYTES + hi * 2048 + ((lane >> 2) & 3) * 64 + ((lane >> 4) & 1) * 32 + (lane & 3) * 8 + wn * 1024;
#pragma unroll
  for (int a = 0; a < MB; ++a)
#pragma unroll
    for (int b = 0; b < 2; ++b) acc[a][b] = f32x16{};
  stage(0, 0);
  stage(kiters > 1 ? 1 : 0, STAGE);
  unsigned srd = 0, swr = 2 * STAGE;
#pragma unroll 1
  for (int kt = 0; kt < kiters; ++kt) {
    asm volatile("s_waitcnt vmcnt(%0)\n\ts_barrier" :: "i"(NA + 2) : "memory");
    stage(kt + 2 < kiters ? kt + 2 : kiters - 1, swr);
#pragma unroll
    for (int ks = 0; ks < 4; ++ks) {
      bf16x8 af[MB], bfr[2];
#pragma unroll
      for (int mb = 0; mb < MB; ++mb) af[mb] = ldsread((lds_ptr)(uintptr_t)(aoff[mb][ks] + srd));
#pragma unroll
      for (int nb = 0; nb < 2; ++nb) {
        const unsigned q = boff + srd + ks * 4096 + nb * 512;
        bfr[nb] = cat(trread((lds_ptr)(uintptr_t)q), trread((lds_ptr)(uintptr_t)(q + 256)));
      }
#pragma unroll
      for (int mb = 0; mb < MB; ++mb)
#pragma unroll
        for (int nb = 0; nb < 2; ++nb) acc[mb][nb] = MFMA(af[mb], bfr[nb], acc[mb][nb]);
    }
    srd = srd == 2 * STAGE ? 0 : srd + STAGE;
    swr = swr == 2 * STAGE ? 0 : swr + STAGE;
  }
  asm volatile("s_waitcnt vmcnt(0)" ::: "memory");
}
DI int crow(int r, int hi) { return (r & 3) + 8 * (r >> 2) + 4 * hi; }

DI float gelu_tanh(float v) {
  const float u = 0.7978845608028654f * (v + 0.044715f * v * v * v);
  const float e = __expf(2.f * u);
  const float t = 1.f - 2.f / (e + 1.f);
  return 0.5f * v * (1.f + t);
}

__global__ __launch_bounds__(512, 2) void k_gemm1(const u16* __restrict__ y, const u16* __restrict__ w1,
                                                  const u16* __restrict__ b1, u16* __restrict__ h) {
  extern __shared__ __attribute__((aligned(16))) char smem[];
  const int bid = blockIdx.x, lid = (bid & 7) * 24 + (bid >> 3);
  const int n0 = (lid % 12) * 128, m0 = (lid / 12) * 128;
  f32x16 acc[1][2];
  gemm_tile<128>(y + (size_t)m0 * DM, DM, w1 + n0, FF, DM / 64, (unsigned)(uintptr_t)(lds_ptr)smem, acc);
  const int lane = threadIdx.x & 63, wid = threadIdx.x >> 6, r32 = lane & 31, hi = lane >> 5;
  const int wm = wid >> 1, wn = wid & 1;
#pragma unroll
  for (int nb = 0; nb < 2; ++nb) {
    const int n = n0 + wn * 64 + nb * 32 + r32;
    const float bias = bf2f(b1[n]);
#pragma unroll
    for (int r = 0; r < 16; ++r) {
      const int m = m0 + wm * 32 + crow(r, hi);
      float v = rbf(rbf(acc[0][nb][r]) + bias);
      h[(size_t)m * FF + n] = (u16)(cvtpk(gelu_tanh(v), 0.f) & 0xffff);
    }
  }
}

__global__ __launch_bounds__(512, 2) void k_gemm2(const u16* __restrict__ O, const u16* __restrict__ wo,
                                                  const u16* __restrict__ h, const u16* __restrict__ w2,
                                                  float* __restrict__ part) {
  extern __shared__ __attribute__((aligned(16))) char smem[];
  const int bid = blockIdx.x, mt = bid & 7, r = bid >> 3;
  const int n0 = (r % 5) * 128, c = r / 5, m0 = mt * 256;
  const u16* A; const u16* B; int lda, kit;
  if (c < 5) {
    const int kt0 = c == 0 ? 0 : 1 + 19 * c;
    kit = c == 0 ? 20 : 19;
    A = O + (size_t)m0 * QD + kt0 * 64; lda = QD; B = wo + (size_t)kt0 * 64 * DM + n0;
  } else { A = h + (size_t)m0 * FF; lda = FF; B = w2 + n0; kit = 24; }
  f32x16 acc[2][2];
  gemm_tile<256>(A, lda, B, DM, kit, (unsigned)(uintptr_t)(lds_ptr)smem, acc);
  const int lane = threadIdx.x & 63, wid = threadIdx.x >> 6, r32 = lane & 31, hi = lane >> 5;
  const int wm = wid >> 1, wn = wid & 1;
  float* P = part + (size_t)c * SEQ * DM;
#pragma unroll
  for (int nb = 0; nb < 2; ++nb) {
    const int n = n0 + wn * 64 + nb * 32 + r32;
#pragma unroll
    for (int mb = 0; mb < 2; ++mb)
#pragma unroll
      for (int r = 0; r < 16; ++r) {
        const int m = m0 + wm * 64 + mb * 32 + crow(r, hi);
        P[(size_t)m * DM + n] = acc[mb][nb][r];
      }
  }
}

__global__ __launch_bounds__(256) void k_final(const u16* __restrict__ x, const float* __restrict__ part,
                                               const u16* __restrict__ b2, u16* __restrict__ out) {
  const int idx = (blockIdx.x * 256 + threadIdx.x) * 4;
  constexpr size_t PS = (size_t)SEQ * DM;
  const f32x4 p0 = *(const f32x4*)(part + idx), p1 = *(const f32x4*)(part + PS + idx),
              p2 = *(const f32x4*)(part + 2 * PS + idx), p3 = *(const f32x4*)(part + 3 * PS + idx),
              p4 = *(const f32x4*)(part + 4 * PS + idx), pm = *(const f32x4*)(part + 5 * PS + idx);
  const u32x2 xw = *(const u32x2*)(x + idx);
  const int n = idx % DM;
  const u32x2 bw = *(const u32x2*)(b2 + n);
  float xo[4] = {__uint_as_float(xw[0] << 16), __uint_as_float(xw[0] & 0xffff0000u), __uint_as_float(xw[1] << 16),
                 __uint_as_float(xw[1] & 0xffff0000u)};
  float bo[4] = {__uint_as_float(bw[0] << 16), __uint_as_float(bw[0] & 0xffff0000u), __uint_as_float(bw[1] << 16),
                 __uint_as_float(bw[1] & 0xffff0000u)};
  float r[4];
#pragma unroll
  for (int i = 0; i < 4; ++i) {
    const float s = rbf(((p0[i] + p1[i]) + (p2[i] + p3[i])) + p4[i]);
    const float m = rbf(rbf(pm[i]) + bo[i]);
    const float g = rbf(m / (1.f + __expf(-m)));
    r[i] = xo[i] + rbf(s * g);
  }
  u32x2 o = {cvtpk(r[0], r[1]), cvtpk(r[2], r[3])};
  *(u32x2*)(out + idx) = o;
}

constexpr int TB = 24576;
constexpr float QSCALE = 0.07216878364870322f * 1.4426950408889634f;
constexpr float THR = 20.0f;
#define SB() __builtin_amdgcn_sched_barrier(0)
#define PIN(x) asm volatile("" : "+v"(x))

DI void glds_s(const void* sbase, unsigned voff, unsigned ldsaddr) {
  asm volatile("s_mov_b32 m0, %2\n\ts_nop 0\n\tglobal_load_lds_dwordx4 %0, %1"
               :: "v"(voff), "s"(sbase), "s"(ldsaddr) : "memory");
}
template <int IMM> DI void glds_i(const void* sbase, unsigned voff, unsigned ldsbase) {
  asm volatile("s_add_i32 m0, %2, %3\n\ts_nop 0\n\tglobal_load_lds_dwordx4 %0, %1"
               :: "v"(voff), "s"(sbase), "s"(ldsbase), "i"(IMM) : "memory", "scc");
}
DI bf16x8 rdK(unsigned a) { return ldsread((lds_ptr)(uintptr_t)a); }
DI bf16x8 rdV(unsigned a) { return cat(trread((lds_ptr)(uintptr_t)a), trread((lds_ptr)(uintptr_t)(a + 3072))); }
DI unsigned nxt(unsigned s) { return s == 2 * TB ? 0 : s + TB; }
#define ACLOB "a0","a1","a2","a3","a4","a5","a6","a7","a8","a9","a10","a11","a12","a13","a14","a15","a16","a17","a18","a19","a20","a21","a22","a23","a24","a25","a26","a27","a28","a29","a30","a31","a32","a33","a34","a35","a36","a37","a38","a39","a40","a41","a42","a43","a44","a45","a46","a47","a48","a49","a50","a51","a52","a53","a54","a55","a56","a57","a58","a59","a60","a61","a62","a63","a64","a65","a66","a67","a68","a69","a70","a71","a72","a73","a74","a75","a76","a77","a78","a79","a80","a81","a82","a83","a84","a85","a86","a87","a88","a89","a90","a91","a92","a93","a94","a95","a96","a97","a98","a99","a100","a101","a102","a103","a104","a105","a106","a107","a108","a109","a110","a111","a112","a113","a114","a115","a116","a117","a118","a119","a120","a121","a122","a123","a124","a125","a126","a127","a128","a129","a130","a131","a132","a133","a134","a135","a136","a137","a138","a139","a140","a141","a142","a143","a144","a145","a146","a147","a148","a149","a150","a151","a152","a153","a154","a155","a156","a157","a158","a159","a160","a161","a162","a163","a164","a165","a166","a167","a168","a169","a170","a171","a172","a173","a174","a175","a176","a177","a178","a179","a180","a181","a182","a183","a184","a185","a186","a187","a188","a189","a190","a191","a192","a193","a194","a195","a196","a197","a198","a199","a200","a201","a202","a203","a204","a205","a206","a207","a208","a209","a210","a211","a212","a213","a214","a215","a216","a217","a218","a219","a220","a221","a222","a223","a224","a225","a226","a227","a228","a229","a230","a231","a232","a233","a234","a235","a236","a237","a238","a239","a240","a241","a242","a243","a244","a245","a246","a247","a248","a249","a250","a251","a252","a253","a254","a255"
constexpr int AO(int b, int db) { return 16 * (6 * b + db); }
constexpr int AQ(int b, int i) { return 192 + 32 * b + 4 * i; }
template <int R> DI void mfma_o(const bf16x8& a, const bf16x8& b) {
  asm volatile("v_mfma_f32_32x32x16_bf16 a[%c2:%c3], %0, %1, a[%c2:%c3]" :: "v"(a), "v"(b), "i"(R), "i"(R + 15) : ACLOB);
}
template <int R> DI void mfma_first(f32x16& d, const bf16x8& a, const f32x16& c) {
  asm volatile("v_mfma_f32_32x32x16_bf16 %0, %1, a[%c3:%c4], %2" : "=&v"(d) : "v"(a), "v"(c), "i"(R), "i"(R + 3) : ACLOB);
}
template <int R> DI void mfma_qa(f32x16& d, const bf16x8& a) {
  asm volatile("v_mfma_f32_32x32x16_bf16 %0, %1, a[%c2:%c3], %0" : "+v"(d) : "v"(a), "i"(R), "i"(R + 3) : ACLOB);
}
DI void mfma_qv(f32x16& d, const bf16x8& a, const bf16x8& b) {
  asm volatile("v_mfma_f32_32x32x16_bf16 %0, %1, %2, %0" : "+v"(d) : "v"(a), "v"(b));
}
template <int R> DI float acc_rd() { float r; asm volatile("v_accvgpr_read_b32 %0, a[%c1]" : "=v"(r) : "i"(R)); return r; }
template <int R> DI void acc_wr(unsigned v) { asm volatile("v_accvgpr_write_b32 a[%c1], %0" :: "v"(v), "i"(R) : ACLOB); }
template <int R> DI void acc_zero() { asm volatile("v_accvgpr_write_b32 a[%c0], 0" :: "i"(R) : ACLOB); }
template <int R> DI void acc_scale(float f) {
  float t;
  asm volatile("v_accvgpr_read_b32 %0, a[%c2]\n\tv_mul_f32 %0, %1, %0\n\tv_accvgpr_write_b32 a[%c2], %0" : "=&v"(t) : "v"(f), "i"(R) : ACLOB);
}
template <int I> struct IC { static constexpr int v = I; };
template <int N, class F> DI void sfor(F&& f) {
  if constexpr (N > 0) { sfor<N - 1>(f); f(IC<N - 1>{}); }
}

__global__ __launch_bounds__(256, 1) void k_attn(const u16* __restrict__ xnp, const u16* __restrict__ wq,
                                                 const u16* __restrict__ Kc, const u16* __restrict__ Vc,
                                                 u16* __restrict__ O) {
  extern __shared__ __attribute__((aligned(16))) char smem[];
  const unsigned lds0 = (unsigned)(uintptr_t)(lds_ptr)smem;
  const int tid = threadIdx.x, lane = tid & 63, wid = __builtin_amdgcn_readfirstlane(tid >> 6);
  const int r32 = lane & 31, hi = lane >> 5;
  const int bid = blockIdx.x, xcd = bid & 7, idx = bid >> 3;
  const int head = xcd * 4 + (idx >> 3), qb = idx & 7;
  const int q0 = qb * 256 + wid * 64;

  const int krl = lane >> 3;
  const unsigned kvo0 = (unsigned)(krl * DH * 2 + (((lane & 7) ^ (krl >> 1)) << 4));
  const unsigned kvo1 = (unsigned)(krl * DH * 2 + (((lane & 7) ^ (4 + (krl >> 1))) << 4));
  const unsigned vvo = (unsigned)(((lane >> 2) & 7) * DH * 2 + (lane >> 5) * 64 + (lane & 3) * 16);
  const unsigned wvo = (unsigned)(((lane >> 2) & 7) * QD * 2 + (lane >> 5) * 64 + (lane & 3) * 16);
  const char* kbase = (const char*)(Kc + (size_t)head * SEQ * DH) + (size_t)(16 * wid) * DH * 2;
  const char* vbase = (const char*)(Vc + (size_t)head * SEQ * DH) + (size_t)(16 * wid) * DH * 2;
  const char* wbase = (const char*)(wq + head * DH) + (size_t)(16 * wid) * QD * 2;
  const unsigned kdst = lds0 + wid * 2048, vdst = lds0 + 3 * TB + wid * 6144;
  auto pieceK = [&](int key0, unsigned slot, int pc) {
    glds_s(kbase + (size_t)key0 * (DH * 2) + (pc & 1) * (8 * DH * 2) + (pc >> 1) * 128, (pc & 1) ? kvo1 : kvo0,
           kdst + slot + (pc & 1) * 1024 + (pc >> 1) * 8192);
  };
  auto pieceV = [&](int key0, unsigned slot, int pc) {
    glds_s(vbase + (size_t)key0 * (DH * 2) + (pc & 1) * (8 * DH * 2) + (pc >> 1) * 128, vvo,
           vdst + slot + (pc & 1) * 3072 + (pc >> 1) * 1024);
  };
  auto pieceW = [&](int t, unsigned slot, int pc) {
    glds_s(wbase + (size_t)t * (64 * QD * 2) + (size_t)(pc & 1) * (8 * QD * 2) + (pc >> 1) * 128, wvo,
           vdst + slot + (pc & 1) * 3072 + (pc >> 1) * 1024);
  };
  unsigned kpo[6], vpo[6];
#pragma unroll
  for (int pc = 0; pc < 6; ++pc) {
    kpo[pc] = ((pc & 1) ? kvo1 : kvo0) + (pc & 1) * (8 * DH * 2) + (pc >> 1) * 128;
    vpo[pc] = vvo + (pc & 1) * (8 * DH * 2) + (pc >> 1) * 128;
  }
  unsigned koff[4];
#pragma unroll
  for (int j = 0; j < 4; ++j) koff[j] = lds0 + r32 * 128 + (((2 * j + hi) ^ ((r32 >> 1) & 7)) << 4);
  const unsigned voff = lds0 + 3 * TB + (hi * 4 + ((lane >> 2) & 3)) * 64 + ((lane >> 4) & 1) * 32 + (lane & 3) * 8;

  sfor<192>([&](auto I) { acc_zero<decltype(I)::v>(); });
  bf16x8 fr[4];

  const u16* xsrc0 = xnp + (size_t)(q0 + r32) * DM + hi * 8;
  const u16* xsrc1 = xsrc0 + 32 * DM;
  bf16x8 xa[2][4], xb[2][4];
  auto xload = [&](bf16x8 (&x)[2][4], int t) {
    const u16* p0 = xsrc0 + t * 64;
    const u16* p1 = xsrc1 + t * 64;
    asm volatile("global_load_dwordx4 %0, %8, off\n\tglobal_load_dwordx4 %1, %8, off offset:32\n\t"
                 "global_load_dwordx4 %2, %8, off offset:64\n\tglobal_load_dwordx4 %3, %8, off offset:96\n\t"
                 "global_load_dwordx4 %4, %9, off\n\tglobal_load_dwordx4 %5, %9, off offset:32\n\t"
                 "global_load_dwordx4 %6, %9, off offset:64\n\tglobal_load_dwordx4 %7, %9, off offset:96"
                 : "=&v"(x[0][0]), "=&v"(x[0][1]), "=&v"(x[0][2]), "=&v"(x[0][3]), "=&v"(x[1][0]), "=&v"(x[1][1]),
                   "=&v"(x[1][2]), "=&v"(x[1][3])
                 : "v"(p0), "v"(p1) : "memory");
  };
#pragma unroll
  for (int pc = 0; pc < 6; ++pc) pieceW(0, 2 * TB, pc);
#pragma unroll
  for (int pc = 0; pc < 6; ++pc) pieceW(1, 0, pc);
  xload(xa, 0);
  if (wid >= 2) {
#pragma unroll
    for (int pc = 0; pc < 6; ++pc) pieceK(-32, 2 * TB, pc);
  }
#pragma unroll
  for (int pc = 0; pc < 6; ++pc) pieceK(32, 0, pc);
#pragma unroll
  for (int pc = 0; pc < 6; ++pc) pieceK(96, TB, pc);
  {
    unsigned srd = 2 * TB, swr = TB;
    auto qstep = [&](bf16x8 (&xc)[2][4], bf16x8 (&xn)[2][4], const int t) {
      asm volatile("s_waitcnt vmcnt(6)\n\ts_barrier" : "+v"(xc[0][0]), "+v"(xc[0][1]), "+v"(xc[0][2]), "+v"(xc[0][3]), "+v"(xc[1][0]), "+v"(xc[1][1]), "+v"(xc[1][2]), "+v"(xc[1][3]) :: "memory");
      const unsigned vb = voff + srd;
#pragma unroll
      for (int i = 0; i < 4; ++i) fr[i] = rdV(vb + i * 512);
      xload(xn, t + 1 < 10 ? t + 1 : 9);
      SB();
      const bool isw = t + 2 < 10;
      const char* ssrc = isw ? wbase + (size_t)(t + 2) * (64 * QD * 2) : vbase + (size_t)(t - 8) * (64 * DH * 2);
      const unsigned sgs = isw ? 8 * QD * 2 : 8 * DH * 2;
      const unsigned svo = isw ? wvo : vvo;
      sfor<24>([&](auto I) {
        constexpr int i = decltype(I)::v;
        mfma_o<AO(0, i % 6)>(fr[i & 3], xc[0][i / 6]);
        mfma_o<AO(1, i % 6)>(fr[i & 3], xc[1][i / 6]);
        if constexpr (i + 4 < 24) fr[i & 3] = rdV(vb + ((2 * ((i + 4) / 6)) * 6 + ((i + 4) % 6)) * 512);
        if constexpr (i % 2 == 1 && i < 12) {
          constexpr int pc = i / 2;
          glds_s(ssrc + (size_t)(pc & 1) * sgs + (pc >> 1) * 128, svo, vdst + swr + (pc & 1) * 3072 + (pc >> 1) * 1024);
        }
        SB();
      });
      srd = nxt(srd);
      swr = nxt(swr);
    };
    asm volatile("s_waitcnt vmcnt(0)" : "+v"(xa[0][0]), "+v"(xa[0][1]), "+v"(xa[0][2]), "+v"(xa[0][3]), "+v"(xa[1][0]), "+v"(xa[1][1]), "+v"(xa[1][2]), "+v"(xa[1][3]) :: "memory");
#pragma unroll 1
    for (int t = 0; t < 10; t += 2) { qstep(xa, xb, t); qstep(xb, xa, t + 1); }
    asm volatile("s_waitcnt vmcnt(6)" : "+v"(xa[0][0]), "+v"(xa[0][1]), "+v"(xa[0][2]), "+v"(xa[0][3]), "+v"(xa[1][0]), "+v"(xa[1][1]), "+v"(xa[1][2]), "+v"(xa[1][3]) :: "memory");
  }
  asm volatile("s_nop 15\n\ts_nop 7" ::: "memory");
  SB();
  bf16x8 qf[2][4];
  sfor<24>([&](auto I) {
    constexpr int n = decltype(I)::v, b = n / 12, f = n % 12, R = AO(b, f >> 1) + 8 * (f & 1);
    unsigned w0 = cvtpk(acc_rd<R + 0>() * QSCALE, acc_rd<R + 1>() * QSCALE);
    unsigned w1 = cvtpk(acc_rd<R + 2>() * QSCALE, acc_rd<R + 3>() * QSCALE);
    unsigned w2 = cvtpk(acc_rd<R + 4>() * QSCALE, acc_rd<R + 5>() * QSCALE);
    unsigned w3 = cvtpk(acc_rd<R + 6>() * QSCALE, acc_rd<R + 7>() * QSCALE);
    auto r0 = __builtin_amdgcn_permlane32_swap(w0, w2, false, false);
    auto r1 = __builtin_amdgcn_permlane32_swap(w1, w3, false, false);
    if constexpr (f < 8) {
      acc_wr<AQ(b, f) + 0>(r0[0]); acc_wr<AQ(b, f) + 1>(r1[0]); acc_wr<AQ(b, f) + 2>(r0[1]); acc_wr<AQ(b, f) + 3>(r1[1]);
    } else {
      u32x4 w = {r0[0], r1[0], r0[1], r1[1]};
      qf[b][f - 8] = __builtin_bit_cast(bf16x8, w);
    }
  });
  sfor<192>([&](auto I) { acc_zero<decltype(I)::v>(); });

  float m_run[2], l_run[2] = {0.f, 0.f}, alpha[2];
  bool pend = false;
  f32x16 S[2][2], nm[2];
  u32x4 pw[2][2];
  unsigned kb[4], vb;
  nm[0] = f32x16{}; nm[1] = f32x16{};
  PIN(nm[0]); PIN(nm[1]);
#pragma unroll
  for (int i = 0; i < 4; ++i) { PIN(qf[0][i]); PIN(qf[1][i]); }
#pragma unroll
  for (int i = 0; i < 4; ++i) fr[i] = rdK(koff[i] + 2 * TB + 4096);
  SB();
  asm volatile("s_nop 3" ::: "memory");
  SB();

  sfor<12>([&](auto I) {
    constexpr int i = decltype(I)::v;
    if constexpr (i == 0) { mfma_first<AQ(0, 0)>(S[0][0], fr[0], nm[0]); mfma_first<AQ(1, 0)>(S[0][1], fr[0], nm[1]); }
    else if constexpr (i < 8) { mfma_qa<AQ(0, i)>(S[0][0], fr[i & 3]); mfma_qa<AQ(1, i)>(S[0][1], fr[i & 3]); }
    else { mfma_qv(S[0][0], fr[i & 3], qf[0][i - 8]); mfma_qv(S[0][1], fr[i & 3], qf[1][i - 8]); }
    if constexpr (i + 4 < 12) fr[i & 3] = rdK(koff[i & 3] + 2 * TB + ((i + 4) >> 2) * 8192 + 4096);
    SB();
  });
  asm volatile("s_nop 15\n\ts_nop 7" :: "v"(nm[0]), "v"(nm[1]) : "memory");
  SB();
#pragma unroll
  for (int b = 0; b < 2; ++b) {
    float mx = S[0][b][0];
#pragma unroll
    for (int r = 1; r < 16; ++r) mx = fmaxf(mx, S[0][b][r]);
    auto rr = __builtin_amdgcn_permlane32_swap(__float_as_uint(mx), __float_as_uint(mx), false, false);
    mx = fmaxf(__uint_as_float(rr[0]), __uint_as_float(rr[1]));
    m_run[b] = mx;
#pragma unroll
    for (int r = 0; r < 16; ++r) { nm[b][r] = -mx; S[0][b][r] -= mx; }
  }
#pragma unroll
  for (int j = 0; j < 4; ++j) kb[j] = koff[j];
  vb = voff;
#pragma unroll
  for (int i = 0; i < 4; ++i) fr[i] = rdK(kb[i]);

  auto phaseA = [&](auto XX, auto HH, const char* ssrc, const unsigned sdst) {
    constexpr int X = decltype(XX)::v, HB = decltype(HH)::v;
    sfor<12>([&](auto I) {
      constexpr int i = decltype(I)::v;
      auto ex = [&](auto N) {
        constexpr int n = decltype(N)::v;
        if constexpr (n < 32) {
          constexpr int b = (n & 15) >> 3, r = (n >> 4) * 8 + (n & 7);
          S[X][b][r] = __builtin_amdgcn_exp2f(S[X][b][r]);
        }
      };
      auto pk = [&](auto N) {
        constexpr int n = decltype(N)::v;
        if constexpr (n >= 0 && n < 16) {
          constexpr int b = (n >> 2) & 1, ks = n >> 3, w = n & 3;
          pw[b][ks][w] = cvtpk(S[X][b][8 * ks + 2 * w], S[X][b][8 * ks + 2 * w + 1]);
        }
      };
      if constexpr (i == 0) mfma_first<AQ(0, 0)>(S[X ^ 1][0], fr[0], nm[0]);
      else if constexpr (i < 8) mfma_qa<AQ(0, i)>(S[X ^ 1][0], fr[i & 3]);
      else mfma_qv(S[X ^ 1][0], fr[i & 3], qf[0][i - 8]);
      ex(IC<3 * i>{}); ex(IC<3 * i + 1>{});
      pk(IC<2 * i - 8>{});
      PIN(S[X][0]); PIN(S[X][1]);
      SB();
      if constexpr (i == 0) mfma_first<AQ(1, 0)>(S[X ^ 1][1], fr[0], nm[1]);
      else if constexpr (i < 8) mfma_qa<AQ(1, i)>(S[X ^ 1][1], fr[i & 3]);
      else mfma_qv(S[X ^ 1][1], fr[i & 3], qf[1][i - 8]);
      if constexpr (i + 4 < 12) fr[i & 3] = rdK(kb[i & 3] + ((i + 4) >> 2) * 8192 + HB * 4096);
      else fr[i & 3] = rdV(vb + ((4 * HB) * 6 + (i - 8)) * 512);
      ex(IC<3 * i + 2>{});
      pk(IC<2 * i - 7>{});
      if constexpr (i == 2) asm volatile("" :: "v"(nm[0]), "v"(nm[1]));
      if constexpr (i == 1 || i == 5 || i == 9) {
        constexpr int pc = (i - 1) / 4;
        if constexpr (HB == 0) glds_i<(pc & 1) * 1024 + (pc >> 1) * 8192>(ssrc, kpo[pc], sdst);
        else glds_i<(pc & 1) * 3072 + (pc >> 1) * 1024>(ssrc, vpo[pc], sdst);
      }
      PIN(S[X][0]); PIN(S[X][1]); PIN(pw[0][0]); PIN(pw[0][1]); PIN(pw[1][0]); PIN(pw[1][1]);
      SB();
    });
  };
  auto phaseB = [&](auto XX, auto HH, auto HK, const char* ssrc, const unsigned sdst) {
    constexpr int X = decltype(XX)::v, HB = decltype(HH)::v, HBKN = decltype(HK)::v;
    float ls[2], mx[2];
    sfor<12>([&](auto I) {
      constexpr int i = decltype(I)::v;
      mfma_o<AO(0, i % 6)>(fr[i & 3], __builtin_bit_cast(bf16x8, pw[0][i / 6]));
      if constexpr (i < 8) {
        constexpr int b = i >> 2, r = (i & 3) * 4;
        if constexpr ((i & 3) == 0) ls[b] = S[X][b][r] + S[X][b][r + 1]; else { ls[b] += S[X][b][r]; ls[b] += S[X][b][r + 1]; }
        ls[b] += S[X][b][r + 2]; ls[b] += S[X][b][r + 3];
        PIN(ls[b]);
      }
      if constexpr (i == 8) { l_run[0] += ls[0]; l_run[1] += ls[1]; PIN(l_run[0]); PIN(l_run[1]); }
      if constexpr (i == 8 || i == 9 || i == 11) {
        constexpr int pc = i == 8 ? 3 : (i == 9 ? 4 : 5);
        if constexpr (HB == 0) glds_i<(pc & 1) * 1024 + (pc >> 1) * 8192>(ssrc, kpo[pc], sdst);
        else glds_i<(pc & 1) * 3072 + (pc >> 1) * 1024>(ssrc, vpo[pc], sdst);
      }
      SB();
      mfma_o<AO(1, i % 6)>(fr[i & 3], __builtin_bit_cast(bf16x8, pw[1][i / 6]));
      if constexpr (i + 4 < 12) fr[i & 3] = rdV(vb + ((4 * HB + 2 * ((i + 4) / 6)) * 6 + ((i + 4) % 6)) * 512);
      else fr[i & 3] = rdK(kb[i - 8] + HBKN * 4096);
      if constexpr (i >= 2 && i < 10) {
        constexpr int b = (i - 2) >> 2, r = ((i - 2) & 3) * 4;
        if constexpr (((i - 2) & 3) == 0) mx[b] = fmaxf(fmaxf(S[X ^ 1][b][r], S[X ^ 1][b][r + 1]), S[X ^ 1][b][r + 2]);
        else mx[b] = fmaxf(fmaxf(mx[b], S[X ^ 1][b][r]), S[X ^ 1][b][r + 1]), mx[b] = fmaxf(mx[b], S[X ^ 1][b][r + 2]);
        mx[b] = fmaxf(mx[b], S[X ^ 1][b][r + 3]);
        PIN(mx[b]);
      }
      if constexpr (i == 10) {
#pragma unroll
        for (int b = 0; b < 2; ++b) {
          auto rr = __builtin_amdgcn_permlane32_swap(__float_as_uint(mx[b]), __float_as_uint(mx[b]), false, false);
          mx[b] = fmaxf(__uint_as_float(rr[0]), __uint_as_float(rr[1]));
        }
        if (__builtin_expect(__any(fmaxf(mx[0], mx[1]) > THR), 0)) {
#pragma unroll
          for (int b = 0; b < 2; ++b) {
            const float dl = fmaxf(mx[b], 0.f);
            m_run[b] += dl;
            alpha[b] = __builtin_amdgcn_exp2f(-dl);
#pragma unroll
            for (int r = 0; r < 16; ++r) { nm[b][r] = -m_run[b]; S[X ^ 1][b][r] -= dl; }
          }
          pend = true;
        }
      }
      SB();
    });
    if (__builtin_expect(pend, 0)) {
      asm volatile("s_nop 15\n\ts_nop 7" ::: "memory");
      l_run[0] *= alpha[0]; l_run[1] *= alpha[1];
      sfor<192>([&](auto I) { constexpr int n = decltype(I)::v; acc_scale<n>(alpha[n / 96]); });
      asm volatile("s_nop 3" ::: "memory");
      pend = false;
    }
  };

  asm volatile("s_waitcnt vmcnt(0)" ::: "memory");
  unsigned scur = 0;
#pragma unroll 1
  for (int t = 0; t < 32; ++t) {
    const unsigned snxt = nxt(scur), swr = nxt(snxt);
    asm volatile("s_waitcnt vmcnt(6)\n\ts_barrier" ::: "memory");
    int tk = t + 2 < 32 ? t + 2 : 31;
    const int tv = tk;
    if (tk == 31 && wid >= 2) tk = 30;
    const char* ks_ = kbase + (size_t)(tk * 64 + 32) * (DH * 2);
    const char* vs_ = vbase + (size_t)(tv * 64) * (DH * 2);
    SB();
    phaseA(IC<0>{}, IC<0>{}, ks_, kdst + swr);
    phaseB(IC<0>{}, IC<0>{}, IC<1>{}, ks_, kdst + swr);
    phaseA(IC<1>{}, IC<1>{}, vs_, vdst + swr);
#pragma unroll
    for (int j = 0; j < 4; ++j) kb[j] = koff[j] + snxt;
    phaseB(IC<1>{}, IC<1>{}, IC<0>{}, vs_, vdst + swr);
    vb = voff + snxt;
    scur = snxt;
  }
  asm volatile("s_waitcnt vmcnt(0)\n\ts_nop 15\n\ts_nop 7" ::: "memory");
  int tz = threadIdx.x;
  asm volatile("" : "+v"(tz));
  float rls[2]; u16* orows[2];
#pragma unroll
  for (int b = 0; b < 2; ++b) {
    auto rr = __builtin_amdgcn_permlane32_swap(__float_as_uint(l_run[b]), __float_as_uint(l_run[b]), false, false);
    const float rl = 1.0f / (__uint_as_float(rr[0]) + __uint_as_float(rr[1]));
    u16* orow = O + (size_t)(qb * 256 + (tz >> 6) * 64 + b * 32 + (tz & 31)) * QD + head * DH + ((tz >> 5) & 1) * 8;
    rls[b] = rl; orows[b] = orow;
  }
  sfor<24>([&](auto I) {
    constexpr int n = decltype(I)::v, b = n / 12, f = n % 12, R = AO(b, f >> 1) + 8 * (f & 1);
    const float rl = rls[b];
    unsigned w0 = cvtpk(acc_rd<R + 0>() * rl, acc_rd<R + 1>() * rl);
    unsigned w1 = cvtpk(acc_rd<R + 2>() * rl, acc_rd<R + 3>() * rl);
    unsigned w2 = cvtpk(acc_rd<R + 4>() * rl, acc_rd<R + 5>() * rl);
    unsigned w3 = cvtpk(acc_rd<R + 6>() * rl, acc_rd<R + 7>() * rl);
    auto r0 = __builtin_amdgcn_permlane32_swap(w0, w2, false, false);
    auto r1 = __builtin_amdgcn_permlane32_swap(w1, w3, false, false);
    u32x4 w = {r0[0], r1[0], r0[1], r1[1]};
    *(u32x4*)(orows[b] + f * 16) = w;
  });
}

extern "C" void kernel_launch(void* const* d_in, const int* in_sizes, int n_in, void* d_out, int out_size,
                              void* d_ws, size_t ws_size, hipStream_t stream) {
  const u16* x = (const u16*)d_in[0];
  const u16* kc = (const u16*)d_in[1];
  const u16* vc = (const u16*)d_in[2];
  const u16* wq = (const u16*)d_in[3];
  const u16* wo = (const u16*)d_in[4];
  const u16* w1 = (const u16*)d_in[6];
  const u16* b1 = (const u16*)d_in[7];
  const u16* w2 = (const u16*)d_in[8];
  const u16* b2 = (const u16*)d_in[9];
  char* ws = (char*)d_ws;
  u16* xnp = (u16*)(ws);
  u16* y = (u16*)(ws + (4u << 20));
  u16* h = (u16*)(ws + (8u << 20));
  u16* O = (u16*)(ws + (16u << 20));
  float* part = (float*)(ws + (48u << 20));

  hipFuncSetAttribute((const void*)k_attn, hipFuncAttributeMaxDynamicSharedMemorySize, 6 * TB);
  hipFuncSetAttribute((const void*)k_gemm1, hipFuncAttributeMaxDynamicSharedMemorySize, 3 * 32768);
  hipFuncSetAttribute((const void*)k_gemm2, hipFuncAttributeMaxDynamicSharedMemorySize, 3 * 49152);

  k_ln<<<SEQ / 4, 256, 0, stream>>>(x, xnp, y);
  k_gemm1<<<192, 512, 3 * 32768, stream>>>(y, w1, b1, h);
  k_attn<<<256, 256, 6 * TB, stream>>>(xnp, wq, kc, vc, O);
  k_gemm2<<<240, 512, 3 * 49152, stream>>>(O, wo, h, w2, part);
  k_final<<<SEQ * DM / 1024, 256, 0, stream>>>(x, part, b2, (u16*)d_out);
}
```

```cpp
#include <hip/hip_runtime.h>
#include <hip/hip_bf16.h>
#include <stdint.h>

typedef __attribute__((ext_vector_type(8))) short bf16x8;
typedef __attribute__((ext_vector_type(8))) __bf16 bf16x8b;
typedef __attribute__((ext_vector_type(4))) short s16x4;
typedef __attribute__((ext_vector_type(16))) float f32x16;
typedef __attribute__((ext_vector_type(4))) float f32x4;
typedef __attribute__((ext_vector_type(2))) float f32x2;
typedef __attribute__((ext_vector_type(2))) __bf16 bf16x2b;
typedef __attribute__((ext_vector_type(4))) unsigned u32x4;
typedef __attribute__((ext_vector_type(2))) unsigned u32x2;
typedef __attribute__((address_space(3))) char* lds_ptr;
typedef unsigned short u16;

#define DI __device__ __forceinline__

constexpr int SEQ = 2048, DM = 640, NH = 32, DH = 192, FF = 1536, QD = NH * DH;

DI f32x16 MFMA(bf16x8 a, bf16x8 b, f32x16 c) {
  return __builtin_amdgcn_mfma_f32_32x32x16_bf16(__builtin_bit_cast(bf16x8b, a), __builtin_bit_cast(bf16x8b, b), c, 0, 0, 0);
}
DI unsigned cvtpk(float lo, float hi) {
  f32x2 v = {lo, hi};
  bf16x2b b = __builtin_convertvector(v, bf16x2b);
  return __builtin_bit_cast(unsigned, b);
}
DI float bf2f(u16 v) { return __uint_as_float(((unsigned)v) << 16); }
DI float rbf(float v) { return __uint_as_float(cvtpk(v, 0.f) << 16); }
DI s16x4 trread(lds_ptr p) {
  return __builtin_amdgcn_ds_read_tr16_b64_v4i16((__attribute__((address_space(3))) s16x4*)p);
}
DI bf16x8 ldsread(lds_ptr p) { return *(__attribute__((address_space(3))) bf16x8*)p; }
DI bf16x8 cat(s16x4 a, s16x4 b) { return __builtin_shufflevector(a, b, 0, 1, 2, 3, 4, 5, 6, 7); }
DI void glds16(const void* g, lds_ptr l) {
  __builtin_amdgcn_global_load_lds((const __attribute__((address_space(1))) unsigned*)g,
                                   (__attribute__((address_space(3))) unsigned*)l, 16, 0, 0);
}

DI float wave_sum(float v) {
#pragma unroll
  for (int o = 32; o > 0; o >>= 1) v += __shfl_xor(v, o, 64);
  return v;
}
DI void ln_row(float (&v)[10]) {
  float s = 0.f;
#pragma unroll
  for (int i = 0; i < 10; ++i) s += v[i];
  s = wave_sum(s);
  const float mean = s * (1.0f / DM);
  float q = 0.f;
#pragma unroll
  for (int i = 0; i < 10; ++i) { float d = v[i] - mean; q += d * d; }
  q = wave_sum(q);
  const float mb = rbf(mean), vb = rbf(q * (1.0f / DM));
  const float den = rbf(sqrtf(rbf(vb + 1e-5f)));
#pragma unroll
  for (int i = 0; i < 10; ++i) v[i] = rbf(rbf(v[i] - mb) / den);
}
__global__ __launch_bounds__(256) void k_ln(const u16* __restrict__ x, u16* __restrict__ xnp, u16* __restrict__ y) {
  const int row = blockIdx.x * 4 + (threadIdx.x >> 6), lane = threadIdx.x & 63;
  const unsigned* xr = (const unsigned*)(x + (size_t)row * DM);
  float v[10];
#pragma unroll
  for (int i = 0; i < 5; ++i) {
    unsigned w = xr[lane + 64 * i];
    v[2 * i] = __uint_as_float(w << 16);
    v[2 * i + 1] = __uint_as_float(w & 0xffff0000u);
  }
  ln_row(v);
  unsigned* xo = (unsigned*)xnp + (size_t)(row >> 5) * (40 * 64 * 4) + (row & 31) * 4;
#pragma unroll
  for (int i = 0; i < 5; ++i) {
    int e = lane + 64 * i;
    int ep = (e & ~6) | ((e & 2) << 1) | ((e & 4) >> 1);
    xo[(ep >> 3) * 256 + ((ep >> 2) & 1) * 128 + (ep & 3)] =
        (__float_as_uint(v[2 * i]) >> 16) | (__float_as_uint(v[2 * i + 1]) & 0xffff0000u);
  }
  ln_row(v);
  unsigned* yo = (unsigned*)y + (size_t)(row >> 5) * (40 * 64 * 4) + (row & 31) * 4;
#pragma unroll
  for (int i = 0; i < 5; ++i) {
    int e = lane + 64 * i;
    int ep = (e & ~6) | ((e & 2) << 1) | ((e & 4) >> 1);
    yo[(ep >> 3) * 256 + ((ep >> 2) & 1) * 128 + (ep & 3)] =
        (__float_as_uint(v[2 * i]) >> 16) | (__float_as_uint(v[2 * i + 1]) & 0xffff0000u);
  }
}

#define SB() __builtin_amdgcn_sched_barrier(0)
#define PIN(x) asm volatile("" : "+v"(x))
template <int I> struct IC { static constexpr int v = I; };
template <int N, class F> DI void sfor(F&& f) {
  if constexpr (N > 0) { sfor<N - 1>(f); f(IC<N - 1>{}); }
}
template <int IMM> DI void glds_i(const void* sbase, unsigned voff, unsigned ldsbase) {
  asm volatile("s_add_i32 m0, %2, %3\n\ts_nop 0\n\tglobal_load_lds_dwordx4 %0, %1"
               :: "v"(voff), "s"(sbase), "s"(ldsbase), "i"(IMM) : "memory", "scc");
}
template <int OFF> DI void gload16(bf16x8& d, const char* p) {
  asm volatile("global_load_dwordx4 %0, %1, off offset:%2" : "=&v"(d) : "v"(p), "i"(OFF) : "memory");
}
template <int RB> DI void xpin(bf16x8 (&x)[RB][4]);
template <> DI void xpin<1>(bf16x8 (&x)[1][4]) { asm volatile("" : "+v"(x[0][0]), "+v"(x[0][1]), "+v"(x[0][2]), "+v"(x[0][3])); }
template <> DI void xpin<2>(bf16x8 (&x)[2][4]) {
  asm volatile("" : "+v"(x[0][0]), "+v"(x[0][1]), "+v"(x[0][2]), "+v"(x[0][3]), "+v"(x[1][0]), "+v"(x[1][1]), "+v"(x[1][2]), "+v"(x[1][3]));
}
template <int RB, int NB> DI void xwait(bf16x8 (&x)[RB][4]);
template <> DI void xwait<1, 3>(bf16x8 (&x)[1][4]) {
  asm volatile("s_waitcnt vmcnt(3)\n\ts_barrier" : "+v"(x[0][0]), "+v"(x[0][1]), "+v"(x[0][2]), "+v"(x[0][3]) :: "memory");
}
template <> DI void xwait<2, 4>(bf16x8 (&x)[2][4]) {
  asm volatile("s_waitcnt vmcnt(4)\n\ts_barrier" : "+v"(x[0][0]), "+v"(x[0][1]), "+v"(x[0][2]), "+v"(x[0][3]), "+v"(x[1][0]), "+v"(x[1][1]), "+v"(x[1][2]), "+v"(x[1][3]) :: "memory");
}

template <int RB, int NB>
DI void wgemm(const char* wbase, unsigned ldwb, const char* xp, unsigned xrbs, int ktiles, unsigned lds0,
              f32x16 (&acc)[RB][NB]) {
  constexpr unsigned TBY = 4096 * NB;
  const int tid = threadIdx.x, lane = tid & 63, wid = __builtin_amdgcn_readfirstlane(tid >> 6);
  const int hi = lane >> 5;
  unsigned wpo[NB];
#pragma unroll
  for (int i = 0; i < NB; ++i) {
    const int sub = 2 * (wid * NB + i) + hi, kg = sub / NB, nb = sub % NB;
    wpo[i] = (unsigned)(8 * kg + ((lane >> 2) & 7)) * ldwb + nb * 64 + (lane & 3) * 16;
  }
  const unsigned wdst = lds0 + wid * NB * 1024;
  const unsigned voff = lds0 + (hi * 4 + ((lane >> 2) & 3)) * 64 + ((lane >> 4) & 1) * 32 + (lane & 3) * 8;
  auto rdW = [&](unsigned a) { return cat(trread((lds_ptr)(uintptr_t)a), trread((lds_ptr)(uintptr_t)(a + NB * 512))); };
#pragma unroll
  for (int r = 0; r < RB; ++r)
#pragma unroll
    for (int n = 0; n < NB; ++n) acc[r][n] = f32x16{};
  bf16x8 xa[RB][4], xb[RB][4], fr[4];
  const size_t wts = (size_t)64 * ldwb;
  sfor<NB>([&](auto I) { glds_i<decltype(I)::v * 1024>(wbase, wpo[decltype(I)::v], wdst); });
  sfor<NB>([&](auto I) { glds_i<decltype(I)::v * 1024>(wbase + wts, wpo[decltype(I)::v], wdst + TBY); });
  sfor<4 * RB>([&](auto I) { constexpr int i = decltype(I)::v; gload16<(i & 3) * 1024>(xa[i >> 2][i & 3], xp + (i >> 2) * xrbs); });
  asm volatile("s_waitcnt vmcnt(0)" ::: "memory");
  xpin<RB>(xa);
  unsigned srd = 0, swr = 2 * TBY;
  auto step = [&](bf16x8 (&xc)[RB][4], bf16x8 (&xn)[RB][4], const int t) {
    xwait<RB, NB>(xc);
    const unsigned vb = voff + srd;
#pragma unroll
    for (int i = 0; i < 4; ++i) fr[i] = rdW(vb + ((2 * (i / NB)) * NB + (i % NB)) * 512);
    const int tn = t + 1 < ktiles ? t + 1 : ktiles - 1, tw = t + 2 < ktiles ? t + 2 : ktiles - 1;
    const char* xq = xp + (size_t)tn * 4096;
    const char* wq_ = wbase + (size_t)tw * wts;
    SB();
    sfor<4 * NB>([&](auto I) {
      constexpr int i = decltype(I)::v;
#pragma unroll
      for (int r = 0; r < RB; ++r) acc[r][i % NB] = MFMA(fr[i & 3], xc[r][i / NB], acc[r][i % NB]);
      SB();
      if constexpr (i + 4 < 4 * NB) fr[i & 3] = rdW(vb + ((2 * ((i + 4) / NB)) * NB + ((i + 4) % NB)) * 512);
      if constexpr (i < 4 * RB) gload16<(i & 3) * 1024>(xn[i >> 2][i & 3], xq + (i >> 2) * xrbs);
      if constexpr (i >= 4 * RB && i < 4 * RB + NB) glds_i<(i - 4 * RB) * 1024>(wq_, wpo[i - 4 * RB], wdst + swr);
      SB();
    });
    srd = srd == 2 * TBY ? 0 : srd + TBY;
    swr = swr == 2 * TBY ? 0 : swr + TBY;
  };
#pragma unroll 1
  for (int t = 0; t < ktiles; t += 2) { step(xa, xb, t); step(xb, xa, t + 1); }
  asm volatile("s_waitcnt vmcnt(0)" ::: "memory");
  xpin<RB>(xa);
}
DI int crow(int r, int hi) { return (r & 3) + 8 * (r >> 2) + 4 * hi; }

DI float gelu_tanh(float v) {
  const float u = 0.7978845608028654f * (v + 0.044715f * v * v * v);
  const float e = __expf(2.f * u);
  const float t = 1.f - 2.f / (e + 1.f);
  return 0.5f * v * (1.f + t);
}

__global__ __launch_bounds__(256, 2) void k_gemm1(const u16* __restrict__ y, const u16* __restrict__ w1,
                                                  const u16* __restrict__ b1, u16* __restrict__ h) {
  extern __shared__ __attribute__((aligned(16))) char smem[];
  const int bid = blockIdx.x, lid = (bid & 7) * 32 + (bid >> 3);
  const int nt = lid & 15, mt = lid >> 4, n0 = nt * 96;
  const int lane = threadIdx.x & 63, wid = threadIdx.x >> 6, hi = lane >> 5;
  const int rb = mt * 4 + wid;
  f32x16 acc[1][3];
  wgemm<1, 3>((const char*)(w1 + n0), FF * 2, (const char*)y + ((size_t)rb * 40 * 64 + lane) * 16, 0, DM / 64,
              (unsigned)(uintptr_t)(lds_ptr)smem, acc);
  u32x4* ho = (u32x4*)h + ((size_t)rb * 96 + nt * 6) * 64 + lane;
#pragma unroll
  for (int nb = 0; nb < 3; ++nb) {
    float g[16];
#pragma unroll
    for (int q = 0; q < 4; ++q) {
      const u32x2 bw = *(const u32x2*)(b1 + n0 + nb * 32 + 8 * q + 4 * hi);
      const float bs[4] = {__uint_as_float(bw[0] << 16), __uint_as_float(bw[0] & 0xffff0000u), __uint_as_float(bw[1] << 16),
                           __uint_as_float(bw[1] & 0xffff0000u)};
#pragma unroll
      for (int e = 0; e < 4; ++e) g[4 * q + e] = gelu_tanh(rbf(rbf(acc[0][nb][4 * q + e]) + bs[e]));
    }
#pragma unroll
    for (int sidx = 0; sidx < 2; ++sidx)
      ho[(nb * 2 + sidx) * 64] = u32x4{cvtpk(g[8 * sidx], g[8 * sidx + 1]), cvtpk(g[8 * sidx + 2], g[8 * sidx + 3]),
                                       cvtpk(g[8 * sidx + 4], g[8 * sidx + 5]), cvtpk(g[8 * sidx + 6], g[8 * sidx + 7])};
  }
}

__global__ __launch_bounds__(256, 1) void k_gemm2(const u16* __restrict__ O, const u16* __restrict__ wo,
                                                  const u16* __restrict__ h, const u16* __restrict__ w2,
                                                  float* __restrict__ part) {
  extern __shared__ __attribute__((aligned(16))) char smem[];
  const int bid = blockIdx.x, x = bid & 7, r = bid >> 3;
  const int nt = r % 5, mt = (x >> 1) * 2 + ((r / 5) & 1), c = (x & 1) * 3 + r / 10;
  const int lane = threadIdx.x & 63, wid = threadIdx.x >> 6;
  const int rb0 = mt * 8 + wid * 2;
  const char* W; const char* X; unsigned xrbs; int kit;
  if (c < 5) {
    const int kt0 = c < 3 ? 20 * c : 60 + 18 * (c - 3);
    kit = c < 3 ? 20 : 18;
    W = (const char*)(wo + (size_t)kt0 * 64 * DM + nt * 128);
    xrbs = 384 * 1024;
    X = (const char*)O + ((size_t)rb0 * 384 + kt0 * 4) * 1024 + lane * 16;
  } else {
    kit = 24;
    W = (const char*)(w2 + nt * 128);
    xrbs = 96 * 1024;
    X = (const char*)h + (size_t)rb0 * 96 * 1024 + lane * 16;
  }
  f32x16 acc[2][4];
  wgemm<2, 4>(W, DM * 2, X, xrbs, kit, (unsigned)(uintptr_t)(lds_ptr)smem, acc);
  f32x4* P = (f32x4*)part + (size_t)c * (SEQ * DM / 4);
#pragma unroll
  for (int b = 0; b < 2; ++b)
#pragma unroll
    for (int nb = 0; nb < 4; ++nb)
#pragma unroll
      for (int q = 0; q < 4; ++q)
        P[(((size_t)(rb0 + b) * 20 + nt * 4 + nb) * 4 + q) * 64 + lane] =
            f32x4{acc[b][nb][4 * q], acc[b][nb][4 * q + 1], acc[b][nb][4 * q + 2], acc[b][nb][4 * q + 3]};
}

__global__ __launch_bounds__(256) void k_final(const u16* __restrict__ x, const float* __restrict__ part,
                                               const u16* __restrict__ b2, u16* __restrict__ out) {
  const int idx = blockIdx.x * 256 + threadIdx.x;
  constexpr size_t PS = (size_t)SEQ * DM / 4;
  const f32x4* P = (const f32x4*)part + idx;
  const f32x4 p0 = P[0], p1 = P[PS], p2 = P[2 * PS], p3 = P[3 * PS], p4 = P[4 * PS], pm = P[5 * PS];
  const int lane = idx & 63, q = (idx >> 6) & 3, blk = idx >> 8, cb = blk % 20, rb = blk / 20;
  const int m = rb * 32 + (lane & 31), n = cb * 32 + 8 * q + 4 * (lane >> 5);
  const u32x2 xw = *(const u32x2*)(x + (size_t)m * DM + n);
  const u32x2 bw = *(const u32x2*)(b2 + n);
  float xo[4] = {__uint_as_float(xw[0] << 16), __uint_as_float(xw[0] & 0xffff0000u), __uint_as_float(xw[1] << 16),
                 __uint_as_float(xw[1] & 0xffff0000u)};
  float bo[4] = {__uint_as_float(bw[0] << 16), __uint_as_float(bw[0] & 0xffff0000u), __uint_as_float(bw[1] << 16),
                 __uint_as_float(bw[1] & 0xffff0000u)};
  float r[4];
#pragma unroll
  for (int i = 0; i < 4; ++i) {
    const float s = rbf(((p0[i] + p1[i]) + (p2[i] + p3[i])) + p4[i]);
    const float mm = rbf(rbf(pm[i]) + bo[i]);
    const float g = rbf(mm / (1.f + __expf(-mm)));
    r[i] = xo[i] + rbf(s * g);
  }
  u32x2 o = {cvtpk(r[0], r[1]), cvtpk(r[2], r[3])};
  *(u32x2*)(out + (size_t)m * DM + n) = o;
}

constexpr int TB = 24576;
constexpr float QSCALE = 0.07216878364870322f * 1.4426950408889634f;
constexpr float THR = 20.0f;

DI void glds_s(const void* sbase, unsigned voff, unsigned ldsaddr) {
  asm volatile("s_mov_b32 m0, %2\n\ts_nop 0\n\tglobal_load_lds_dwordx4 %0, %1"
               :: "v"(voff), "s"(sbase), "s"(ldsaddr) : "memory");
}
DI float max3f(float a, float b, float c) { float r; asm("v_max3_f32 %0, %1, %2, %3" : "=v"(r) : "v"(a), "v"(b), "v"(c)); return r; }
DI float max2f(float a, float b) { float r; asm("v_max_f32_e32 %0, %1, %2" : "=v"(r) : "v"(a), "v"(b)); return r; }
DI void bcast16(f32x16& d, float aone, float negm) {
  asm volatile("s_nop 1\n\tv_mfma_f32_32x32x2_f32 %0, %1, %2, 0" : "+v"(d) : "v"(aone), "v"(negm));
}
DI bf16x8 rdK(unsigned a) { return ldsread((lds_ptr)(uintptr_t)a); }
DI bf16x8 rdV(unsigned a) { return cat(trread((lds_ptr)(uintptr_t)a), trread((lds_ptr)(uintptr_t)(a + 3072))); }
DI unsigned nxt(unsigned s) { return s == 2 * TB ? 0 : s + TB; }
#define ACLOB "a0","a1","a2","a3","a4","a5","a6","a7","a8","a9","a10","a11","a12","a13","a14","a15","a16","a17","a18","a19","a20","a21","a22","a23","a24","a25","a26","a27","a28","a29","a30","a31","a32","a33","a34","a35","a36","a37","a38","a39","a40","a41","a42","a43","a44","a45","a46","a47","a48","a49","a50","a51","a52","a53","a54","a55","a56","a57","a58","a59","a60","a61","a62","a63","a64","a65","a66","a67","a68","a69","a70","a71","a72","a73","a74","a75","a76","a77","a78","a79","a80","a81","a82","a83","a84","a85","a86","a87","a88","a89","a90","a91","a92","a93","a94","a95","a96","a97","a98","a99","a100","a101","a102","a103","a104","a105","a106","a107","a108","a109","a110","a111","a112","a113","a114","a115","a116","a117","a118","a119","a120","a121","a122","a123","a124","a125","a126","a127","a128","a129","a130","a131","a132","a133","a134","a135","a136","a137","a138","a139","a140","a141","a142","a143","a144","a145","a146","a147","a148","a149","a150","a151","a152","a153","a154","a155","a156","a157","a158","a159","a160","a161","a162","a163","a164","a165","a166","a167","a168","a169","a170","a171","a172","a173","a174","a175","a176","a177","a178","a179","a180","a181","a182","a183","a184","a185","a186","a187","a188","a189","a190","a191","a192","a193","a194","a195","a196","a197","a198","a199","a200","a201","a202","a203","a204","a205","a206","a207","a208","a209","a210","a211","a212","a213","a214","a215","a216","a217","a218","a219","a220","a221","a222","a223","a224","a225","a226","a227","a228","a229","a230","a231","a232","a233","a234","a235","a236","a237","a238","a239","a240","a241","a242","a243","a244","a245","a246","a247","a248","a249","a250","a251","a252","a253","a254","a255"
constexpr int AO(int b, int db) { return 16 * (6 * b + db); }
constexpr int AQ(int b, int i) { return 192 + 32 * b + 4 * i; }
template <int R> DI void mfma_o(const bf16x8& a, const bf16x8& b) {
  asm volatile("v_mfma_f32_32x32x16_bf16 a[%c2:%c3], %0, %1, a[%c2:%c3]" :: "v"(a), "v"(b), "i"(R), "i"(R + 15) : ACLOB);
}
template <int R> DI void mfma_o_mx(const bf16x8& a, const bf16x8& b, float& m, float x, float y, float z) {
  asm volatile("v_mfma_f32_32x32x16_bf16 a[%c6:%c7], %1, %2, a[%c6:%c7]\n\tv_max3_f32 %0, %3, %4, %5"
               : "=v"(m) : "v"(a), "v"(b), "v"(x), "v"(y), "v"(z), "i"(R), "i"(R + 15) : ACLOB);
}
template <int R> DI void mfma_first(f32x16& d, const bf16x8& a, const f32x16& c) {
  asm volatile("v_mfma_f32_32x32x16_bf16 %0, %1, a[%c3:%c4], %2" : "=&v"(d) : "v"(a), "v"(c), "i"(R), "i"(R + 3) : ACLOB);
}
template <int R> DI void mfma_qa(f32x16& d, const bf16x8& a) {
  asm volatile("v_mfma_f32_32x32x16_bf16 %0, %1, a[%c2:%c3], %0" : "+v"(d) : "v"(a), "i"(R), "i"(R + 3) : ACLOB);
}
DI void mfma_qv(f32x16& d, const bf16x8& a, const bf16x8& b) {
  asm volatile("v_mfma_f32_32x32x16_bf16 %0, %1, %2, %0" : "+v"(d) : "v"(a), "v"(b));
}
template <int R> DI float acc_rd() { float r; asm volatile("v_accvgpr_read_b32 %0, a[%c1]" : "=v"(r) : "i"(R)); return r; }
template <int R> DI void acc_wr(unsigned v) { asm volatile("v_accvgpr_write_b32 a[%c1], %0" :: "v"(v), "i"(R) : ACLOB); }
template <int R> DI void acc_zero() { asm volatile("v_accvgpr_write_b32 a[%c0], 0" :: "i"(R) : ACLOB); }
template <int R> DI void acc_zero16(const bf16x8& z) {
  asm volatile("v_mfma_f32_32x32x16_bf16 a[%c1:%c2], %0, %0, 0" :: "v"(z), "i"(R), "i"(R + 15) : ACLOB);
}
template <int R> DI void acc_rd8(float (&r)[8]) {
  asm volatile("v_accvgpr_read_b32 %0, a[%c8]\n\tv_accvgpr_read_b32 %1, a[%c9]\n\tv_accvgpr_read_b32 %2, a[%c10]\n\tv_accvgpr_read_b32 %3, a[%c11]\n\t"
               "v_accvgpr_read_b32 %4, a[%c12]\n\tv_accvgpr_read_b32 %5, a[%c13]\n\tv_accvgpr_read_b32 %6, a[%c14]\n\tv_accvgpr_read_b32 %7, a[%c15]"
               : "=v"(r[0]), "=v"(r[1]), "=v"(r[2]), "=v"(r[3]), "=v"(r[4]), "=v"(r[5]), "=v"(r[6]), "=v"(r[7])
               : "i"(R), "i"(R + 1), "i"(R + 2), "i"(R + 3), "i"(R + 4), "i"(R + 5), "i"(R + 6), "i"(R + 7));
}
template <int R> DI void acc_wr4(unsigned a, unsigned b, unsigned c, unsigned d) {
  asm volatile("v_accvgpr_write_b32 a[%c4], %0\n\tv_accvgpr_write_b32 a[%c5], %1\n\tv_accvgpr_write_b32 a[%c6], %2\n\tv_accvgpr_write_b32 a[%c7], %3"
               :: "v"(a), "v"(b), "v"(c), "v"(d), "i"(R), "i"(R + 1), "i"(R + 2), "i"(R + 3) : ACLOB);
}
template <int R> DI void acc_scale(float f) {
  float t;
  asm volatile("v_accvgpr_read_b32 %0, a[%c2]\n\tv_mul_f32 %0, %1, %0\n\tv_accvgpr_write_b32 a[%c2], %0" : "=&v"(t) : "v"(f), "i"(R) : ACLOB);
}

__global__ __launch_bounds__(256, 1) void k_attn(const u16* __restrict__ xnp, const u16* __restrict__ wq,
                                                 const u16* __restrict__ Kc, const u16* __restrict__ Vc,
                                                 u16* __restrict__ O) {
  extern __shared__ __attribute__((aligned(16))) char smem[];
  const unsigned lds0 = (unsigned)(uintptr_t)(lds_ptr)smem;
  const int tid = threadIdx.x, lane = tid & 63, wid = __builtin_amdgcn_readfirstlane(tid >> 6);
  const int r32 = lane & 31, hi = lane >> 5;
  const int bid = blockIdx.x, xcd = bid & 7, idx = bid >> 3;
  const int head = xcd * 4 + (idx >> 3), qb = idx & 7;
  const int q0 = qb * 256 + wid * 64;

  const int krl = lane >> 3;
  const unsigned kvo0 = (unsigned)(krl * DH * 2 + (((lane & 7) ^ (krl >> 1)) << 4));
  const unsigned kvo1 = (unsigned)(krl * DH * 2 + (((lane & 7) ^ (4 + (krl >> 1))) << 4));
  const unsigned vvo = (unsigned)(((lane >> 2) & 7) * DH * 2 + (lane >> 5) * 64 + (lane & 3) * 16);
  const unsigned wvo = (unsigned)(((lane >> 2) & 7) * QD * 2 + (lane >> 5) * 64 + (lane & 3) * 16);
  const char* kbase = (const char*)(Kc + (size_t)head * SEQ * DH) + (size_t)(16 * wid) * DH * 2;
  const char* vbase = (const char*)(Vc + (size_t)head * SEQ * DH) + (size_t)(16 * wid) * DH * 2;
  const char* wbase = (const char*)(wq + head * DH) + (size_t)(16 * wid) * QD * 2;
  const unsigned kdst = lds0 + wid * 2048, vdst = lds0 + 3 * TB + wid * 6144;
  auto pieceK = [&](int key0, unsigned slot, int pc) {
    glds_s(kbase + (size_t)key0 * (DH * 2) + (pc & 1) * (8 * DH * 2) + (pc >> 1) * 128, (pc & 1) ? kvo1 : kvo0,
           kdst + slot + (pc & 1) * 1024 + (pc >> 1) * 8192);
  };
  auto pieceV = [&](int key0, unsigned slot, int pc) {
    glds_s(vbase + (size_t)key0 * (DH * 2) + (pc & 1) * (8 * DH * 2) + (pc >> 1) * 128, vvo,
           vdst + slot + (pc & 1) * 3072 + (pc >> 1) * 1024);
  };
  auto pieceW = [&](int t, unsigned slot, int pc) {
    glds_s(wbase + (size_t)t * (64 * QD * 2) + (size_t)(pc & 1) * (8 * QD * 2) + (pc >> 1) * 128, wvo,
           vdst + slot + (pc & 1) * 3072 + (pc >> 1) * 1024);
  };
  unsigned kpo[6], vpo[6];
#pragma unroll
  for (int pc = 0; pc < 6; ++pc) {
    kpo[pc] = ((pc & 1) ? kvo1 : kvo0) + (pc & 1) * (8 * DH * 2) + (pc >> 1) * 128;
    vpo[pc] = vvo + (pc & 1) * (8 * DH * 2) + (pc >> 1) * 128;
  }
  unsigned koff[4];
#pragma unroll
  for (int j = 0; j < 4; ++j) koff[j] = lds0 + r32 * 128 + (((2 * j + hi) ^ ((r32 >> 1) & 7)) << 4);
  const unsigned voff = lds0 + 3 * TB + (hi * 4 + ((lane >> 2) & 3)) * 64 + ((lane >> 4) & 1) * 32 + (lane & 3) * 8;

  bf16x8 zfr = bf16x8{};
  PIN(zfr);
  asm volatile("s_nop 3");
  sfor<12>([&](auto I) { acc_zero16<16 * decltype(I)::v>(zfr); });
  bf16x8 fr[4];

  const u16* xsrc0 = xnp + ((size_t)(q0 >> 5) * (40 * 64) + lane) * 8;
  const u16* xsrc1 = xsrc0 + 40 * 64 * 8;
  bf16x8 xa[2][4], xb[2][4];
  auto xload = [&](bf16x8 (&x)[2][4], int t) {
    const u16* p0 = xsrc0 + t * 2048;
    const u16* p1 = xsrc1 + t * 2048;
    asm volatile("global_load_dwordx4 %0, %8, off\n\tglobal_load_dwordx4 %1, %8, off offset:1024\n\t"
                 "global_load_dwordx4 %2, %8, off offset:2048\n\tglobal_load_dwordx4 %3, %8, off offset:3072\n\t"
                 "global_load_dwordx4 %4, %9, off\n\tglobal_load_dwordx4 %5, %9, off offset:1024\n\t"
                 "global_load_dwordx4 %6, %9, off offset:2048\n\tglobal_load_dwordx4 %7, %9, off offset:3072"
                 : "=&v"(x[0][0]), "=&v"(x[0][1]), "=&v"(x[0][2]), "=&v"(x[0][3]), "=&v"(x[1][0]), "=&v"(x[1][1]),
                   "=&v"(x[1][2]), "=&v"(x[1][3])
                 : "v"(p0), "v"(p1) : "memory");
  };
#pragma unroll
  for (int pc = 0; pc < 6; ++pc) pieceW(0, 2 * TB, pc);
#pragma unroll
  for (int pc = 0; pc < 6; ++pc) pieceW(1, 0, pc);
  xload(xa, 0);
  if (wid >= 2) {
#pragma unroll
    for (int pc = 0; pc < 6; ++pc) pieceK(-32, 2 * TB, pc);
  }
#pragma unroll
  for (int pc = 0; pc < 6; ++pc) pieceK(32, 0, pc);
#pragma unroll
  for (int pc = 0; pc < 6; ++pc) pieceK(96, TB, pc);
  {
    unsigned srd = 2 * TB, swr = TB;
    auto qstep = [&](bf16x8 (&xc)[2][4], bf16x8 (&xn)[2][4], const int t) {
      asm volatile("s_waitcnt vmcnt(6)\n\ts_barrier" : "+v"(xc[0][0]), "+v"(xc[0][1]), "+v"(xc[0][2]), "+v"(xc[0][3]), "+v"(xc[1][0]), "+v"(xc[1][1]), "+v"(xc[1][2]), "+v"(xc[1][3]) :: "memory");
      const unsigned vb = voff + srd;
#pragma unroll
      for (int i = 0; i < 4; ++i) fr[i] = rdV(vb + i * 512);
      const int tn = t + 1 < 10 ? t + 1 : 9;
      const u16* p0 = xsrc0 + tn * 2048;
      const u16* p1 = xsrc1 + tn * 2048;
      SB();
      const bool isw = t + 2 < 10;
      const char* ssrc = isw ? wbase + (size_t)(t + 2) * (64 * QD * 2) : vbase + (size_t)(t - 8) * (64 * DH * 2);
      const unsigned sgs = isw ? 8 * QD * 2 : 8 * DH * 2;
      const unsigned svo = isw ? wvo : vvo;
      sfor<24>([&](auto I) {
        constexpr int i = decltype(I)::v;
        mfma_o<AO(0, i % 6)>(fr[i & 3], xc[0][i / 6]);
        mfma_o<AO(1, i % 6)>(fr[i & 3], xc[1][i / 6]);
        if constexpr (i + 4 < 24) fr[i & 3] = rdV(vb + ((2 * ((i + 4) / 6)) * 6 + ((i + 4) % 6)) * 512);
        if constexpr (i < 8)
          asm volatile("global_load_dwordx4 %0, %1, off offset:%2" : "=&v"(xn[i >> 2][i & 3]) : "v"(i < 4 ? p0 : p1), "i"((i & 3) * 1024) : "memory");
        if constexpr (i >= 8 && i < 14) {
          constexpr int pc = i - 8;
          glds_s(ssrc + (size_t)(pc & 1) * sgs + (pc >> 1) * 128, svo, vdst + swr + (pc & 1) * 3072 + (pc >> 1) * 1024);
        }
        SB();
      });
      srd = nxt(srd);
      swr = nxt(swr);
    };
    asm volatile("s_waitcnt vmcnt(0)" : "+v"(xa[0][0]), "+v"(xa[0][1]), "+v"(xa[0][2]), "+v"(xa[0][3]), "+v"(xa[1][0]), "+v"(xa[1][1]), "+v"(xa[1][2]), "+v"(xa[1][3]) :: "memory");
#pragma unroll 1
    for (int t = 0; t < 10; t += 2) { qstep(xa, xb, t); qstep(xb, xa, t + 1); }
    asm volatile("s_waitcnt vmcnt(6)" : "+v"(xa[0][0]), "+v"(xa[0][1]), "+v"(xa[0][2]), "+v"(xa[0][3]), "+v"(xa[1][0]), "+v"(xa[1][1]), "+v"(xa[1][2]), "+v"(xa[1][3]) :: "memory");
  }
  asm volatile("s_nop 15\n\ts_nop 7" ::: "memory");
  SB();
  bf16x8 qf[2][4];
  sfor<24>([&](auto I) {
    constexpr int n = decltype(I)::v, b = n / 12, f = n % 12, R = AO(b, f >> 1) + 8 * (f & 1);
    float t8[8];
    acc_rd8<R>(t8);
    unsigned w0 = cvtpk(t8[0] * QSCALE, t8[1] * QSCALE);
    unsigned w1 = cvtpk(t8[2] * QSCALE, t8[3] * QSCALE);
    unsigned w2 = cvtpk(t8[4] * QSCALE, t8[5] * QSCALE);
    unsigned w3 = cvtpk(t8[6] * QSCALE, t8[7] * QSCALE);
    auto r0 = __builtin_amdgcn_permlane32_swap(w0, w2, false, false);
    auto r1 = __builtin_amdgcn_permlane32_swap(w1, w3, false, false);
    if constexpr (f < 8) {
      acc_wr4<AQ(b, f)>(r0[0], r1[0], r0[1], r1[1]);
    } else {
      u32x4 w = {r0[0], r1[0], r0[1], r1[1]};
      qf[b][f - 8] = __builtin_bit_cast(bf16x8, w);
    }
  });
  zfr = bf16x8{};
  PIN(zfr);
  SB();
  asm volatile("s_nop 7");
  sfor<12>([&](auto I) { acc_zero16<16 * decltype(I)::v>(zfr); });

  float m_run[2], l_run[2] = {0.f, 0.f}, alpha[2];
  bool pend = false;
  f32x16 S[2][2], nm[2];
  u32x4 pw[2][2];
  unsigned kb[4], vb;
  nm[0] = f32x16{}; nm[1] = f32x16{};
  PIN(nm[0]); PIN(nm[1]);
#pragma unroll
  for (int i = 0; i < 4; ++i) { PIN(qf[0][i]); PIN(qf[1][i]); }
#pragma unroll
  for (int i = 0; i < 4; ++i) fr[i] = rdK(koff[i] + 2 * TB + 4096);
  SB();
  asm volatile("s_nop 3" ::: "memory");
  SB();

  sfor<12>([&](auto I) {
    constexpr int i = decltype(I)::v;
    if constexpr (i == 0) { mfma_first<AQ(0, 0)>(S[0][0], fr[0], nm[0]); mfma_first<AQ(1, 0)>(S[0][1], fr[0], nm[1]); }
    else if constexpr (i < 8) { mfma_qa<AQ(0, i)>(S[0][0], fr[i & 3]); mfma_qa<AQ(1, i)>(S[0][1], fr[i & 3]); }
    else { mfma_qv(S[0][0], fr[i & 3], qf[0][i - 8]); mfma_qv(S[0][1], fr[i & 3], qf[1][i - 8]); }
    if constexpr (i + 4 < 12) fr[i & 3] = rdK(koff[i & 3] + 2 * TB + ((i + 4) >> 2) * 8192 + 4096);
    SB();
  });
  asm volatile("s_nop 15\n\ts_nop 7" :: "v"(nm[0]), "v"(nm[1]) : "memory");
  SB();
#pragma unroll
  for (int b = 0; b < 2; ++b) {
    float mx = S[0][b][0];
#pragma unroll
    for (int r = 1; r < 16; ++r) mx = fmaxf(mx, S[0][b][r]);
    auto rr = __builtin_amdgcn_permlane32_swap(__float_as_uint(mx), __float_as_uint(mx), false, false);
    mx = fmaxf(__uint_as_float(rr[0]), __uint_as_float(rr[1]));
    m_run[b] = mx;
#pragma unroll
    for (int r = 0; r < 16; ++r) { nm[b][r] = -mx; S[0][b][r] -= mx; }
  }
#pragma unroll
  for (int j = 0; j < 4; ++j) kb[j] = koff[j];
  vb = voff;
#pragma unroll
  for (int i = 0; i < 4; ++i) fr[i] = rdK(kb[i]);

  auto phaseA = [&](auto XX, auto HH, const char* ssrc, const unsigned sdst) {
    constexpr int X = decltype(XX)::v, HB = decltype(HH)::v;
    sfor<12>([&](auto I) {
      constexpr int i = decltype(I)::v;
      auto ex = [&](auto N) {
        constexpr int n = decltype(N)::v;
        if constexpr (n < 32) {
          constexpr int b = (n & 15) >> 3, r = (n >> 4) * 8 + (n & 7);
          S[X][b][r] = __builtin_amdgcn_exp2f(S[X][b][r]);
        }
      };
      auto pk = [&](auto N) {
        constexpr int n = decltype(N)::v;
        if constexpr (n >= 0 && n < 16) {
          constexpr int b = (n >> 2) & 1, ks = n >> 3, w = n & 3;
          pw[b][ks][w] = cvtpk(S[X][b][8 * ks + 2 * w], S[X][b][8 * ks + 2 * w + 1]);
        }
      };
      if constexpr (i == 0) mfma_first<AQ(0, 0)>(S[X ^ 1][0], fr[0], nm[0]);
      else if constexpr (i < 8) mfma_qa<AQ(0, i)>(S[X ^ 1][0], fr[i & 3]);
      else mfma_qv(S[X ^ 1][0], fr[i & 3], qf[0][i - 8]);
      SB();
      ex(IC<3 * i>{}); ex(IC<3 * i + 1>{});
      pk(IC<2 * i - 8>{});
      PIN(S[X][0]); PIN(S[X][1]);
      SB();
      if constexpr (i == 0) mfma_first<AQ(1, 0)>(S[X ^ 1][1], fr[0], nm[1]);
      else if constexpr (i < 8) mfma_qa<AQ(1, i)>(S[X ^ 1][1], fr[i & 3]);
      else mfma_qv(S[X ^ 1][1], fr[i & 3], qf[1][i - 8]);
      SB();
      if constexpr (i + 4 < 12) fr[i & 3] = rdK(kb[i & 3] + ((i + 4) >> 2) * 8192 + HB * 4096);
      else fr[i & 3] = rdV(vb + ((4 * HB) * 6 + (i - 8)) * 512);
      ex(IC<3 * i + 2>{});
      pk(IC<2 * i - 7>{});
      if constexpr (i == 2) asm volatile("" :: "v"(nm[0]), "v"(nm[1]));
      if constexpr (i == 1 || i == 5 || i == 9) {
        constexpr int pc = (i - 1) / 4;
        if constexpr (HB == 0) glds_i<(pc & 1) * 1024 + (pc >> 1) * 8192>(ssrc, kpo[pc], sdst);
        else glds_i<(pc & 1) * 3072 + (pc >> 1) * 1024>(ssrc, vpo[pc], sdst);
      }
      PIN(S[X][0]); PIN(S[X][1]); PIN(pw[0][0]); PIN(pw[0][1]); PIN(pw[1][0]); PIN(pw[1][1]);
      SB();
    });
  };
  auto phaseB = [&](auto XX, auto HH, auto HK, const char* ssrc, const unsigned sdst) {
    constexpr int X = decltype(XX)::v, HB = decltype(HH)::v, HBKN = decltype(HK)::v;
    float ls[2][2], mx[2];
    auto fill = [&](auto G) {
      constexpr int g = decltype(G)::v;
      if constexpr (g < 16) {
        constexpr int b = g >> 3, r = (g & 7) * 2;
        if constexpr ((g & 7) == 0) { ls[b][0] = S[X][b][r]; ls[b][1] = S[X][b][r + 1]; }
        else { ls[b][0] += S[X][b][r]; ls[b][1] += S[X][b][r + 1]; }
        PIN(ls[b][0]); PIN(ls[b][1]);
      }
      if constexpr (g == 16) { l_run[0] += ls[0][0] + ls[0][1]; PIN(l_run[0]); }
      if constexpr (g == 17) { l_run[1] += ls[1][0] + ls[1][1]; PIN(l_run[1]); }
      if constexpr (g == 20) {
#pragma unroll
        for (int b = 0; b < 2; ++b) {
          auto rr = __builtin_amdgcn_permlane32_swap(__float_as_uint(mx[b]), __float_as_uint(mx[b]), false, false);
          mx[b] = max2f(__uint_as_float(rr[0]), __uint_as_float(rr[1]));
          PIN(mx[b]);
        }
      }
      if constexpr (g == 21) {
        if (__builtin_expect(__any(max2f(mx[0], mx[1]) > THR), 0)) {
          const float aone = hi == 0 ? 1.f : 0.f;
#pragma unroll
          for (int b = 0; b < 2; ++b) {
            const float dl = fmaxf(mx[b], 0.f);
            m_run[b] += dl;
            alpha[b] = __builtin_amdgcn_exp2f(-dl);
            bcast16(nm[b], aone, -m_run[b]);
#pragma unroll
            for (int r = 0; r < 16; ++r) S[X ^ 1][b][r] -= dl;
          }
          pend = true;
        }
      }
      if constexpr (g == 16 || g == 18 || g == 22) {
        constexpr int pc = g == 16 ? 3 : (g == 18 ? 4 : 5);
        if constexpr (HB == 0) glds_i<(pc & 1) * 1024 + (pc >> 1) * 8192>(ssrc, kpo[pc], sdst);
        else glds_i<(pc & 1) * 3072 + (pc >> 1) * 1024>(ssrc, vpo[pc], sdst);
      }
    };
    auto pvm = [&](auto G) {
      constexpr int g = decltype(G)::v, i = g >> 1, b = g & 1, k = (g - 4) >> 1;
      const bf16x8 pb = __builtin_bit_cast(bf16x8, pw[b][i / 6]);
      if constexpr (g >= 4 && g < 20) {
        if constexpr (k == 0) mfma_o_mx<AO(b, i % 6)>(fr[i & 3], pb, mx[b], S[X ^ 1][b][0], S[X ^ 1][b][1], S[X ^ 1][b][2]);
        else if constexpr (k < 7) mfma_o_mx<AO(b, i % 6)>(fr[i & 3], pb, mx[b], mx[b], S[X ^ 1][b][2 * k + 1], S[X ^ 1][b][2 * k + 2]);
        else mfma_o_mx<AO(b, i % 6)>(fr[i & 3], pb, mx[b], mx[b], S[X ^ 1][b][15], S[X ^ 1][b][15]);
      } else mfma_o<AO(b, i % 6)>(fr[i & 3], pb);
    };
    sfor<12>([&](auto I) {
      constexpr int i = decltype(I)::v;
      pvm(IC<2 * i>{});
      SB();
      fill(IC<2 * i>{});
      SB();
      pvm(IC<2 * i + 1>{});
      SB();
      if constexpr (i + 4 < 12) fr[i & 3] = rdV(vb + ((4 * HB + 2 * ((i + 4) / 6)) * 6 + ((i + 4) % 6)) * 512);
      else fr[i & 3] = rdK(kb[i - 8] + HBKN * 4096);
      fill(IC<2 * i + 1>{});
      SB();
    });
    if (__builtin_expect(pend, 0)) {
      asm volatile("s_nop 15\n\ts_nop 7" ::: "memory");
      l_run[0] *= alpha[0]; l_run[1] *= alpha[1];
      sfor<192>([&](auto I) { constexpr int n = decltype(I)::v; acc_scale<n>(alpha[n / 96]); });
      asm volatile("s_nop 3" ::: "memory");
      pend = false;
    }
  };

  asm volatile("s_waitcnt vmcnt(0)" ::: "memory");
  unsigned scur = 0;
#pragma unroll 1
  for (int t = 0; t < 32; ++t) {
    const unsigned snxt = nxt(scur), swr = nxt(snxt);
    asm volatile("s_waitcnt vmcnt(6)\n\ts_barrier" ::: "memory");
    int tk = t + 2 < 32 ? t + 2 : 31;
    const int tv = tk;
    if (tk == 31 && wid >= 2) tk = 30;
    const char* ks_ = kbase + (size_t)(tk * 64 + 32) * (DH * 2);
    const char* vs_ = vbase + (size_t)(tv * 64) * (DH * 2);
    SB();
    phaseA(IC<0>{}, IC<0>{}, ks_, kdst + swr);
    phaseB(IC<0>{}, IC<0>{}, IC<1>{}, ks_, kdst + swr);
    phaseA(IC<1>{}, IC<1>{}, vs_, vdst + swr);
#pragma unroll
    for (int j = 0; j < 4; ++j) kb[j] = koff[j] + snxt;
    phaseB(IC<1>{}, IC<1>{}, IC<0>{}, vs_, vdst + swr);
    vb = voff + snxt;
    scur = snxt;
  }
  asm volatile("s_waitcnt vmcnt(0)\n\ts_nop 15\n\ts_nop 7" ::: "memory");
  int tz = threadIdx.x;
  asm volatile("" : "+v"(tz));
  float rls[2];
#pragma unroll
  for (int b = 0; b < 2; ++b) {
    auto rr = __builtin_amdgcn_permlane32_swap(__float_as_uint(l_run[b]), __float_as_uint(l_run[b]), false, false);
    rls[b] = 1.0f / (__uint_as_float(rr[0]) + __uint_as_float(rr[1]));
  }
  u32x4* ofr = (u32x4*)O + ((size_t)(qb * 8 + (tz >> 6) * 2) * 384 + head * 12) * 64 + (tz & 63);
  sfor<24>([&](auto I) {
    constexpr int n = decltype(I)::v, b = n / 12, f = n % 12, R = AO(b, f >> 1) + 8 * (f & 1);
    const float rl = rls[b];
    float t8[8];
    acc_rd8<R>(t8);
    ofr[(b * 384 + f) * 64] = u32x4{cvtpk(t8[0] * rl, t8[1] * rl), cvtpk(t8[2] * rl, t8[3] * rl),
                                    cvtpk(t8[4] * rl, t8[5] * rl), cvtpk(t8[6] * rl, t8[7] * rl)};
  });
}

extern "C" void kernel_launch(void* const* d_in, const int* in_sizes, int n_in, void* d_out, int out_size,
                              void* d_ws, size_t ws_size, hipStream_t stream) {
  const u16* x = (const u16*)d_in[0];
  const u16* kc = (const u16*)d_in[1];
  const u16* vc = (const u16*)d_in[2];
  const u16* wq = (const u16*)d_in[3];
  const u16* wo = (const u16*)d_in[4];
  const u16* w1 = (const u16*)d_in[6];
  const u16* b1 = (const u16*)d_in[7];
  const u16* w2 = (const u16*)d_in[8];
  const u16* b2 = (const u16*)d_in[9];
  char* ws = (char*)d_ws;
  u16* xnp = (u16*)(ws);
  u16* y = (u16*)(ws + (4u << 20));
  u16* h = (u16*)(ws + (8u << 20));
  u16* O = (u16*)(ws + (16u << 20));
  float* part = (float*)(ws + (48u << 20));

  hipFuncSetAttribute((const void*)k_attn, hipFuncAttributeMaxDynamicSharedMemorySize, 6 * TB);
  hipFuncSetAttribute((const void*)k_gemm1, hipFuncAttributeMaxDynamicSharedMemorySize, 3 * 12288);
  hipFuncSetAttribute((const void*)k_gemm2, hipFuncAttributeMaxDynamicSharedMemorySize, 3 * 16384);

  k_ln<<<SEQ / 4, 256, 0, stream>>>(x, xnp, y);
  k_gemm1<<<256, 256, 3 * 12288, stream>>>(y, w1, b1, h);
  k_attn<<<256, 256, 6 * TB, stream>>>(xnp, wq, kc, vc, O);
  k_gemm2<<<240, 256, 3 * 16384, stream>>>(O, wo, h, w2, part);
  k_final<<<SEQ * DM / 1024, 256, 0, stream>>>(x, part, b2, (u16*)d_out);
}
```

```cpp
#include <hip/hip_runtime.h>
#include <hip/hip_bf16.h>
#include <stdint.h>

typedef __attribute__((ext_vector_type(8))) short bf16x8;
typedef __attribute__((ext_vector_type(8))) __bf16 bf16x8b;
typedef __attribute__((ext_vector_type(4))) short s16x4;
typedef __attribute__((ext_vector_type(16))) float f32x16;
typedef __attribute__((ext_vector_type(4))) float f32x4;
typedef __attribute__((ext_vector_type(2))) float f32x2;
typedef __attribute__((ext_vector_type(2))) __bf16 bf16x2b;
typedef __attribute__((ext_vector_type(4))) unsigned u32x4;
typedef __attribute__((ext_vector_type(2))) unsigned u32x2;
typedef __attribute__((address_space(3))) char* lds_ptr;
typedef unsigned short u16;

#define DI __device__ __forceinline__

constexpr int SEQ = 2048, DM = 640, NH = 32, DH = 192, FF = 1536, QD = NH * DH;

DI f32x16 MFMA(bf16x8 a, bf16x8 b, f32x16 c) {
  return __builtin_amdgcn_mfma_f32_32x32x16_bf16(__builtin_bit_cast(bf16x8b, a), __builtin_bit_cast(bf16x8b, b), c, 0, 0, 0);
}
DI unsigned cvtpk(float lo, float hi) {
  f32x2 v = {lo, hi};
  bf16x2b b = __builtin_convertvector(v, bf16x2b);
  return __builtin_bit_cast(unsigned, b);
}
DI float bf2f(u16 v) { return __uint_as_float(((unsigned)v) << 16); }
DI float rbf(float v) { return __uint_as_float(cvtpk(v, 0.f) << 16); }
DI s16x4 trread(lds_ptr p) {
  return __builtin_amdgcn_ds_read_tr16_b64_v4i16((__attribute__((address_space(3))) s16x4*)p);
}
DI bf16x8 ldsread(lds_ptr p) { return *(__attribute__((address_space(3))) bf16x8*)p; }
DI bf16x8 cat(s16x4 a, s16x4 b) { return __builtin_shufflevector(a, b, 0, 1, 2, 3, 4, 5, 6, 7); }
DI void glds16(const void* g, lds_ptr l) {
  __builtin_amdgcn_global_load_lds((const __attribute__((address_space(1))) unsigned*)g,
                                   (__attribute__((address_space(3))) unsigned*)l, 16, 0, 0);
}

DI float wave_sum(float v) {
#pragma unroll
  for (int o = 32; o > 0; o >>= 1) v += __shfl_xor(v, o, 64);
  return v;
}
DI void ln_row(float (&v)[10]) {
  float s = 0.f;
#pragma unroll
  for (int i = 0; i < 10; ++i) s += v[i];
  s = wave_sum(s);
  const float mean = s * (1.0f / DM);
  float q = 0.f;
#pragma unroll
  for (int i = 0; i < 10; ++i) { float d = v[i] - mean; q += d * d; }
  q = wave_sum(q);
  const float mb = rbf(mean), vb = rbf(q * (1.0f / DM));
  const float den = rbf(sqrtf(rbf(vb + 1e-5f)));
#pragma unroll
  for (int i = 0; i < 10; ++i) v[i] = rbf(rbf(v[i] - mb) / den);
}
__global__ __launch_bounds__(256) void k_ln(const u16* __restrict__ x, u16* __restrict__ xnp, u16* __restrict__ y) {
  const int row = blockIdx.x * 4 + (threadIdx.x >> 6), lane = threadIdx.x & 63;
  const unsigned* xr = (const unsigned*)(x + (size_t)row * DM);
  float v[10];
#pragma unroll
  for (int i = 0; i < 5; ++i) {
    unsigned w = xr[lane + 64 * i];
    v[2 * i] = __uint_as_float(w << 16);
    v[2 * i + 1] = __uint_as_float(w & 0xffff0000u);
  }
  ln_row(v);
  unsigned* xo = (unsigned*)xnp + (size_t)(row >> 5) * (40 * 64 * 4) + (row & 31) * 4;
#pragma unroll
  for (int i = 0; i < 5; ++i) {
    int e = lane + 64 * i;
    int ep = (e & ~6) | ((e & 2) << 1) | ((e & 4) >> 1);
    xo[(ep >> 3) * 256 + ((ep >> 2) & 1) * 128 + (ep & 3)] =
        (__float_as_uint(v[2 * i]) >> 16) | (__float_as_uint(v[2 * i + 1]) & 0xffff0000u);
  }
  ln_row(v);
  unsigned* yo = (unsigned*)y + (size_t)(row >> 5) * (40 * 64 * 4) + (row & 31) * 4;
#pragma unroll
  for (int i = 0; i < 5; ++i) {
    int e = lane + 64 * i;
    int ep = (e & ~6) | ((e & 2) << 1) | ((e & 4) >> 1);
    yo[(ep >> 3) * 256 + ((ep >> 2) & 1) * 128 + (ep & 3)] =
        (__float_as_uint(v[2 * i]) >> 16) | (__float_as_uint(v[2 * i + 1]) & 0xffff0000u);
  }
}

#define SB() __builtin_amdgcn_sched_barrier(0)
#define PIN(x) asm volatile("" : "+v"(x))
template <int I> struct IC { static constexpr int v = I; };
template <int N, class F> DI void sfor(F&& f) {
  if constexpr (N > 0) { sfor<N - 1>(f); f(IC<N - 1>{}); }
}
template <int IMM> DI void glds_i(const void* sbase, unsigned voff, unsigned ldsbase) {
  asm volatile("s_add_i32 m0, %2, %3\n\ts_nop 0\n\tglobal_load_lds_dwordx4 %0, %1"
               :: "v"(voff), "s"(sbase), "s"(ldsbase), "i"(IMM) : "memory", "scc");
}
template <int OFF> DI void gload16(bf16x8& d, const char* p) {
  asm volatile("global_load_dwordx4 %0, %1, off offset:%2" : "=&v"(d) : "v"(p), "i"(OFF) : "memory");
}
template <int N> DI void xwait2(bf16x8 (&x)[2][4]) {
  asm volatile("s_waitcnt vmcnt(%8)\n\ts_barrier" : "+v"(x[0][0]), "+v"(x[0][1]), "+v"(x[0][2]), "+v"(x[0][3]), "+v"(x[1][0]), "+v"(x[1][1]), "+v"(x[1][2]), "+v"(x[1][3]) : "i"(N) : "memory");
}
DI void xpin2(bf16x8 (&x)[2][4]) {
  asm volatile("" : "+v"(x[0][0]), "+v"(x[0][1]), "+v"(x[0][2]), "+v"(x[0][3]), "+v"(x[1][0]), "+v"(x[1][1]), "+v"(x[1][2]), "+v"(x[1][3]));
}

DI void wgemm24(const char* wbase, unsigned ldwb, const char* xp, unsigned xrbs, int ktiles, unsigned lds0,
                f32x16 (&acc)[2][4]) {
  constexpr int NB = 4;
  constexpr unsigned TBY = 4096 * NB;
  const int tid = threadIdx.x, lane = tid & 63, wid = __builtin_amdgcn_readfirstlane(tid >> 6);
  const int hi = lane >> 5;
  unsigned wpo[NB];
#pragma unroll
  for (int i = 0; i < NB; ++i) {
    const int sub = 2 * (wid * NB + i) + hi, kg = sub / NB, nb = sub % NB;
    wpo[i] = (unsigned)(8 * kg + ((lane >> 2) & 7)) * ldwb + nb * 64 + (lane & 3) * 16;
  }
  const unsigned wdst = lds0 + wid * NB * 1024;
  const unsigned voff = lds0 + (hi * 4 + ((lane >> 2) & 3)) * 64 + ((lane >> 4) & 1) * 32 + (lane & 3) * 8;
  auto rdW = [&](unsigned a) { return cat(trread((lds_ptr)(uintptr_t)a), trread((lds_ptr)(uintptr_t)(a + NB * 512))); };
#pragma unroll
  for (int r = 0; r < 2; ++r)
#pragma unroll
    for (int n = 0; n < NB; ++n) acc[r][n] = f32x16{};
  bf16x8 x0[2][4], x1[2][4], x2[2][4], x3[2][4], fr[4];
  const size_t wts = (size_t)64 * ldwb;
  auto issue = [&](bf16x8 (&x)[2][4], int t, auto SLOT) {
    const char* xq = xp + (size_t)t * 4096;
    sfor<8>([&](auto I) { constexpr int i = decltype(I)::v; gload16<(i & 3) * 1024>(x[i >> 2][i & 3], xq + (i >> 2) * xrbs); });
    sfor<NB>([&](auto I) { glds_i<decltype(SLOT)::v * TBY + decltype(I)::v * 1024>(wbase + (size_t)t * wts, wpo[decltype(I)::v], wdst); });
  };
  issue(x0, 0, IC<0>{});
  issue(x1, 1, IC<1>{});
  issue(x2, 2, IC<2>{});
  auto step = [&](bf16x8 (&xc)[2][4], bf16x8 (&xn)[2][4], const int t, auto SLOT) {
    constexpr int sl = decltype(SLOT)::v, sw = (sl + 3) & 3;
    xwait2<24>(xc);
    const unsigned vb = voff + sl * TBY;
#pragma unroll
    for (int i = 0; i < 4; ++i) fr[i] = rdW(vb + i * 512);
    const int tn = t + 3 < ktiles ? t + 3 : ktiles - 1;
    const char* xq = xp + (size_t)tn * 4096;
    const char* wq_ = wbase + (size_t)tn * wts;
    SB();
    sfor<16>([&](auto I) {
      constexpr int i = decltype(I)::v;
      acc[0][i & 3] = MFMA(fr[i & 3], xc[0][i >> 2], acc[0][i & 3]);
      acc[1][i & 3] = MFMA(fr[i & 3], xc[1][i >> 2], acc[1][i & 3]);
      SB();
      if constexpr (i + 4 < 16) fr[i & 3] = rdW(vb + ((2 * ((i + 4) >> 2)) * NB + (i & 3)) * 512);
      if constexpr (i < 8) gload16<(i & 3) * 1024>(xn[i >> 2][i & 3], xq + (i >> 2) * xrbs);
      if constexpr (i >= 8 && i < 12) glds_i<sw * TBY + (i - 8) * 1024>(wq_, wpo[i - 8], wdst);
      SB();
    });
  };
#pragma unroll 1
  for (int t = 0; t < ktiles; t += 4) {
    step(x0, x3, t, IC<0>{});
    step(x1, x0, t + 1, IC<1>{});
    step(x2, x1, t + 2, IC<2>{});
    step(x3, x2, t + 3, IC<3>{});
  }
  asm volatile("s_waitcnt vmcnt(0)" ::: "memory");
  xpin2(x0); xpin2(x1); xpin2(x2);
}
DI int crow(int r, int hi) { return (r & 3) + 8 * (r >> 2) + 4 * hi; }

DI float gelu_tanh(float v) {
  const float u = 0.7978845608028654f * (v + 0.044715f * v * v * v);
  const float e = __builtin_amdgcn_exp2f(2.8853900817779268f * u);
  return v - v * __builtin_amdgcn_rcpf(e + 1.f);
}

__global__ __launch_bounds__(256, 1) void k_gemm1(const u16* __restrict__ y, const u16* __restrict__ w1,
                                                  const u16* __restrict__ b1, u16* __restrict__ h) {
  extern __shared__ __attribute__((aligned(16))) char smem[];
  const int bid = blockIdx.x, lid = (bid & 7) * 32 + (bid >> 3);
  const int nt = lid & 15, mt = lid >> 4, n0 = nt * 96;
  const int lane = threadIdx.x & 63, wid = threadIdx.x >> 6, hi = lane >> 5;
  const int rb = mt * 4 + wid;
  u32x2 bwv[3][4];
#pragma unroll
  for (int nb = 0; nb < 3; ++nb)
#pragma unroll
    for (int q = 0; q < 4; ++q) { bwv[nb][q] = *(const u32x2*)(b1 + n0 + nb * 32 + 8 * q + 4 * hi); PIN(bwv[nb][q]); }
  f32x16 acc[1][3];
  {
    const unsigned lds0 = (unsigned)(uintptr_t)(lds_ptr)smem;
    const int wu = __builtin_amdgcn_readfirstlane(wid);
    const char* wb = (const char*)(w1 + n0);
    const char* xp = (const char*)y + ((size_t)rb * 40 * 64 + lane) * 16;
    unsigned wpo[3];
#pragma unroll
    for (int i = 0; i < 3; ++i) {
      const int sub = 2 * (wu * 3 + i) + hi, kg = sub / 3, nb = sub % 3;
      wpo[i] = (unsigned)(8 * kg + ((lane >> 2) & 7)) * (FF * 2) + nb * 64 + (lane & 3) * 16;
    }
    const unsigned wdst = lds0 + wu * 3072;
    bf16x8 xf[40];
    sfor<10>([&](auto T) {
      constexpr int t = decltype(T)::v;
      sfor<3>([&](auto I) { glds_i<t * 12288 + decltype(I)::v * 1024>(wb + (size_t)t * 64 * FF * 2, wpo[decltype(I)::v], wdst); });
      sfor<4>([&](auto I) { gload16<decltype(I)::v * 1024>(xf[4 * t + decltype(I)::v], xp + t * 4096); });
    });
    const unsigned voff = lds0 + (hi * 4 + ((lane >> 2) & 3)) * 64 + ((lane >> 4) & 1) * 32 + (lane & 3) * 8;
#pragma unroll
    for (int n = 0; n < 3; ++n) acc[0][n] = f32x16{};
    sfor<2>([&](auto HF) {
      constexpr int hf = decltype(HF)::v;
      if constexpr (hf == 0)
        asm volatile("s_waitcnt vmcnt(35)\n\ts_barrier" : "+v"(xf[0]), "+v"(xf[1]), "+v"(xf[2]), "+v"(xf[3]), "+v"(xf[4]), "+v"(xf[5]), "+v"(xf[6]), "+v"(xf[7]), "+v"(xf[8]), "+v"(xf[9]), "+v"(xf[10]), "+v"(xf[11]), "+v"(xf[12]), "+v"(xf[13]), "+v"(xf[14]), "+v"(xf[15]), "+v"(xf[16]), "+v"(xf[17]), "+v"(xf[18]), "+v"(xf[19]) :: "memory");
      else
        asm volatile("s_waitcnt vmcnt(0)\n\ts_barrier" : "+v"(xf[20]), "+v"(xf[21]), "+v"(xf[22]), "+v"(xf[23]), "+v"(xf[24]), "+v"(xf[25]), "+v"(xf[26]), "+v"(xf[27]), "+v"(xf[28]), "+v"(xf[29]), "+v"(xf[30]), "+v"(xf[31]), "+v"(xf[32]), "+v"(xf[33]), "+v"(xf[34]), "+v"(xf[35]), "+v"(xf[36]), "+v"(xf[37]), "+v"(xf[38]), "+v"(xf[39]) :: "memory");
#pragma unroll
      for (int ks = 20 * hf; ks < 20 * hf + 20; ++ks)
#pragma unroll
        for (int nb = 0; nb < 3; ++nb) {
          const unsigned a = voff + (ks >> 2) * 12288 + ((2 * (ks & 3)) * 3 + nb) * 512;
          acc[0][nb] = MFMA(cat(trread((lds_ptr)(uintptr_t)a), trread((lds_ptr)(uintptr_t)(a + 1536))), xf[ks], acc[0][nb]);
        }
    });
  }
  u32x4* ho = (u32x4*)h + ((size_t)rb * 96 + nt * 6) * 64 + lane;
#pragma unroll
  for (int nb = 0; nb < 3; ++nb) {
    float g[16];
#pragma unroll
    for (int q = 0; q < 4; ++q) {
      const u32x2 bw = bwv[nb][q];
      const float bs[4] = {__uint_as_float(bw[0] << 16), __uint_as_float(bw[0] & 0xffff0000u), __uint_as_float(bw[1] << 16),
                           __uint_as_float(bw[1] & 0xffff0000u)};
#pragma unroll
      for (int e = 0; e < 4; ++e) g[4 * q + e] = gelu_tanh(rbf(rbf(acc[0][nb][4 * q + e]) + bs[e]));
    }
#pragma unroll
    for (int sidx = 0; sidx < 2; ++sidx)
      ho[(nb * 2 + sidx) * 64] = u32x4{cvtpk(g[8 * sidx], g[8 * sidx + 1]), cvtpk(g[8 * sidx + 2], g[8 * sidx + 3]),
                                       cvtpk(g[8 * sidx + 4], g[8 * sidx + 5]), cvtpk(g[8 * sidx + 6], g[8 * sidx + 7])};
  }
}

__global__ __launch_bounds__(256, 1) void k_gemm2(const u16* __restrict__ O, const u16* __restrict__ wo,
                                                  const u16* __restrict__ h, const u16* __restrict__ w2,
                                                  float* __restrict__ part) {
  extern __shared__ __attribute__((aligned(16))) char smem[];
  const int bid = blockIdx.x, mt = bid & 7, r = bid >> 3;
  const int nt = r % 5, c = r / 5;
  const int lane = threadIdx.x & 63, wid = threadIdx.x >> 6;
  const int rb0 = mt * 8 + wid * 2;
  const char* W; const char* X; unsigned xrbs;
  if (c < 4) {
    W = (const char*)(wo + (size_t)c * 24 * 64 * DM + nt * 128);
    xrbs = 384 * 1024;
    X = (const char*)O + ((size_t)rb0 * 384 + c * 96) * 1024 + lane * 16;
  } else {
    W = (const char*)(w2 + nt * 128);
    xrbs = 96 * 1024;
    X = (const char*)h + (size_t)rb0 * 96 * 1024 + lane * 16;
  }
  f32x16 acc[2][4];
  wgemm24(W, DM * 2, X, xrbs, 24, (unsigned)(uintptr_t)(lds_ptr)smem, acc);
  f32x4* P = (f32x4*)part + (size_t)c * (SEQ * DM / 4);
#pragma unroll
  for (int b = 0; b < 2; ++b)
#pragma unroll
    for (int nb = 0; nb < 4; ++nb)
#pragma unroll
      for (int q = 0; q < 4; ++q)
        P[(((size_t)(rb0 + b) * 20 + nt * 4 + nb) * 4 + q) * 64 + lane] =
            f32x4{acc[b][nb][4 * q], acc[b][nb][4 * q + 1], acc[b][nb][4 * q + 2], acc[b][nb][4 * q + 3]};
}

__global__ __launch_bounds__(256) void k_final(const u16* __restrict__ x, const float* __restrict__ part,
                                               const u16* __restrict__ b2, u16* __restrict__ out) {
  const int idx = blockIdx.x * 256 + threadIdx.x;
  constexpr size_t PS = (size_t)SEQ * DM / 4;
  const f32x4* P = (const f32x4*)part + idx;
  const f32x4 p0 = P[0], p1 = P[PS], p2 = P[2 * PS], p3 = P[3 * PS], pm = P[4 * PS];
  const int lane = idx & 63, q = (idx >> 6) & 3, blk = idx >> 8, cb = blk % 20, rb = blk / 20;
  const int m = rb * 32 + (lane & 31), n = cb * 32 + 8 * q + 4 * (lane >> 5);
  const u32x2 xw = *(const u32x2*)(x + (size_t)m * DM + n);
  const u32x2 bw = *(const u32x2*)(b2 + n);
  float xo[4] = {__uint_as_float(xw[0] << 16), __uint_as_float(xw[0] & 0xffff0000u), __uint_as_float(xw[1] << 16),
                 __uint_as_float(xw[1] & 0xffff0000u)};
  float bo[4] = {__uint_as_float(bw[0] << 16), __uint_as_float(bw[0] & 0xffff0000u), __uint_as_float(bw[1] << 16),
                 __uint_as_float(bw[1] & 0xffff0000u)};
  float r[4];
#pragma unroll
  for (int i = 0; i < 4; ++i) {
    const float s = rbf((p0[i] + p1[i]) + (p2[i] + p3[i]));
    const float mm = rbf(rbf(pm[i]) + bo[i]);
    const float g = rbf(mm / (1.f + __expf(-mm)));
    r[i] = xo[i] + rbf(s * g);
  }
  u32x2 o = {cvtpk(r[0], r[1]), cvtpk(r[2], r[3])};
  *(u32x2*)(out + (size_t)m * DM + n) = o;
}

constexpr int TB = 24576;
constexpr float QSCALE = 0.07216878364870322f * 1.4426950408889634f;
constexpr float THR = 20.0f;

DI void glds_s(const void* sbase, unsigned voff, unsigned ldsaddr) {
  asm volatile("s_mov_b32 m0, %2\n\ts_nop 0\n\tglobal_load_lds_dwordx4 %0, %1"
               :: "v"(voff), "s"(sbase), "s"(ldsaddr) : "memory");
}
DI float max3f(float a, float b, float c) { float r; asm("v_max3_f32 %0, %1, %2, %3" : "=v"(r) : "v"(a), "v"(b), "v"(c)); return r; }
DI float max2f(float a, float b) { float r; asm("v_max_f32_e32 %0, %1, %2" : "=v"(r) : "v"(a), "v"(b)); return r; }
DI void bcast16(f32x16& d, float aone, float negm) {
  asm volatile("s_nop 1\n\tv_mfma_f32_32x32x2_f32 %0, %1, %2, 0" : "+v"(d) : "v"(aone), "v"(negm));
}
DI bf16x8 rdK(unsigned a) { return ldsread((lds_ptr)(uintptr_t)a); }
DI bf16x8 rdV(unsigned a) { return cat(trread((lds_ptr)(uintptr_t)a), trread((lds_ptr)(uintptr_t)(a + 3072))); }
DI unsigned nxt(unsigned s) { return s == 2 * TB ? 0 : s + TB; }
#define ACLOB "a0","a1","a2","a3","a4","a5","a6","a7","a8","a9","a10","a11","a12","a13","a14","a15","a16","a17","a18","a19","a20","a21","a22","a23","a24","a25","a26","a27","a28","a29","a30","a31","a32","a33","a34","a35","a36","a37","a38","a39","a40","a41","a42","a43","a44","a45","a46","a47","a48","a49","a50","a51","a52","a53","a54","a55","a56","a57","a58","a59","a60","a61","a62","a63","a64","a65","a66","a67","a68","a69","a70","a71","a72","a73","a74","a75","a76","a77","a78","a79","a80","a81","a82","a83","a84","a85","a86","a87","a88","a89","a90","a91","a92","a93","a94","a95","a96","a97","a98","a99","a100","a101","a102","a103","a104","a105","a106","a107","a108","a109","a110","a111","a112","a113","a114","a115","a116","a117","a118","a119","a120","a121","a122","a123","a124","a125","a126","a127","a128","a129","a130","a131","a132","a133","a134","a135","a136","a137","a138","a139","a140","a141","a142","a143","a144","a145","a146","a147","a148","a149","a150","a151","a152","a153","a154","a155","a156","a157","a158","a159","a160","a161","a162","a163","a164","a165","a166","a167","a168","a169","a170","a171","a172","a173","a174","a175","a176","a177","a178","a179","a180","a181","a182","a183","a184","a185","a186","a187","a188","a189","a190","a191","a192","a193","a194","a195","a196","a197","a198","a199","a200","a201","a202","a203","a204","a205","a206","a207","a208","a209","a210","a211","a212","a213","a214","a215","a216","a217","a218","a219","a220","a221","a222","a223","a224","a225","a226","a227","a228","a229","a230","a231","a232","a233","a234","a235","a236","a237","a238","a239","a240","a241","a242","a243","a244","a245","a246","a247","a248","a249","a250","a251","a252","a253","a254","a255"
constexpr int AO(int b, int db) { return 16 * (6 * b + db); }
constexpr int AQ(int b, int i) { return 192 + 32 * b + 4 * i; }
template <int R> DI void mfma_o(const bf16x8& a, const bf16x8& b) {
  asm volatile("v_mfma_f32_32x32x16_bf16 a[%c2:%c3], %0, %1, a[%c2:%c3]" :: "v"(a), "v"(b), "i"(R), "i"(R + 15) : ACLOB);
}
template <int R> DI void mfma_o_mx(const bf16x8& a, const bf16x8& b, float& m, float x, float y, float z) {
  asm volatile("v_mfma_f32_32x32x16_bf16 a[%c6:%c7], %1, %2, a[%c6:%c7]\n\tv_max3_f32 %0, %3, %4, %5"
               : "=v"(m) : "v"(a), "v"(b), "v"(x), "v"(y), "v"(z), "i"(R), "i"(R + 15) : ACLOB);
}
template <int R> DI void mfma_first(f32x16& d, const bf16x8& a, const f32x16& c) {
  asm volatile("v_mfma_f32_32x32x16_bf16 %0, %1, a[%c3:%c4], %2" : "=&v"(d) : "v"(a), "v"(c), "i"(R), "i"(R + 3) : ACLOB);
}
template <int R> DI void mfma_qa(f32x16& d, const bf16x8& a) {
  asm volatile("v_mfma_f32_32x32x16_bf16 %0, %1, a[%c2:%c3], %0" : "+v"(d) : "v"(a), "i"(R), "i"(R + 3) : ACLOB);
}
DI void mfma_qv(f32x16& d, const bf16x8& a, const bf16x8& b) {
  asm volatile("v_mfma_f32_32x32x16_bf16 %0, %1, %2, %0" : "+v"(d) : "v"(a), "v"(b));
}
template <int R> DI float acc_rd() { float r; asm volatile("v_accvgpr_read_b32 %0, a[%c1]" : "=v"(r) : "i"(R)); return r; }
template <int R> DI void acc_wr(unsigned v) { asm volatile("v_accvgpr_write_b32 a[%c1], %0" :: "v"(v), "i"(R) : ACLOB); }
template <int R> DI void acc_zero() { asm volatile("v_accvgpr_write_b32 a[%c0], 0" :: "i"(R) : ACLOB); }
template <int R> DI void acc_zero16(const bf16x8& z) {
  asm volatile("v_mfma_f32_32x32x16_bf16 a[%c1:%c2], %0, %0, 0" :: "v"(z), "i"(R), "i"(R + 15) : ACLOB);
}
template <int R> DI void acc_rd8(float (&r)[8]) {
  asm volatile("v_accvgpr_read_b32 %0, a[%c8]\n\tv_accvgpr_read_b32 %1, a[%c9]\n\tv_accvgpr_read_b32 %2, a[%c10]\n\tv_accvgpr_read_b32 %3, a[%c11]\n\t"
               "v_accvgpr_read_b32 %4, a[%c12]\n\tv_accvgpr_read_b32 %5, a[%c13]\n\tv_accvgpr_read_b32 %6, a[%c14]\n\tv_accvgpr_read_b32 %7, a[%c15]"
               : "=v"(r[0]), "=v"(r[1]), "=v"(r[2]), "=v"(r[3]), "=v"(r[4]), "=v"(r[5]), "=v"(r[6]), "=v"(r[7])
               : "i"(R), "i"(R + 1), "i"(R + 2), "i"(R + 3), "i"(R + 4), "i"(R + 5), "i"(R + 6), "i"(R + 7));
}
template <int R> DI void acc_wr4(unsigned a, unsigned b, unsigned c, unsigned d) {
  asm volatile("v_accvgpr_write_b32 a[%c4], %0\n\tv_accvgpr_write_b32 a[%c5], %1\n\tv_accvgpr_write_b32 a[%c6], %2\n\tv_accvgpr_write_b32 a[%c7], %3"
               :: "v"(a), "v"(b), "v"(c), "v"(d), "i"(R), "i"(R + 1), "i"(R + 2), "i"(R + 3) : ACLOB);
}
template <int R> DI void acc_scale(float f) {
  float t;
  asm volatile("v_accvgpr_read_b32 %0, a[%c2]\n\tv_mul_f32 %0, %1, %0\n\tv_accvgpr_write_b32 a[%c2], %0" : "=&v"(t) : "v"(f), "i"(R) : ACLOB);
}

__global__ __launch_bounds__(256, 1) void k_attn(const u16* __restrict__ xnp, const u16* __restrict__ wq,
                                                 const u16* __restrict__ Kc, const u16* __restrict__ Vc,
                                                 u16* __restrict__ O) {
  extern __shared__ __attribute__((aligned(16))) char smem[];
  const unsigned lds0 = (unsigned)(uintptr_t)(lds_ptr)smem;
  const int tid = threadIdx.x, lane = tid & 63, wid = __builtin_amdgcn_readfirstlane(tid >> 6);
  const int r32 = lane & 31, hi = lane >> 5;
  const int bid = blockIdx.x, xcd = bid & 7, idx = bid >> 3;
  const int head = xcd * 4 + (idx >> 3), qb = idx & 7;
  const int q0 = qb * 256 + wid * 64;

  const int krl = lane >> 3;
  const unsigned kvo0 = (unsigned)(krl * DH * 2 + (((lane & 7) ^ (krl >> 1)) << 4));
  const unsigned kvo1 = (unsigned)(krl * DH * 2 + (((lane & 7) ^ (4 + (krl >> 1))) << 4));
  const unsigned vvo = (unsigned)(((lane >> 2) & 7) * DH * 2 + (lane >> 5) * 64 + (lane & 3) * 16);
  const unsigned wvo = (unsigned)(((lane >> 2) & 7) * QD * 2 + (lane >> 5) * 64 + (lane & 3) * 16);
  const char* kbase = (const char*)(Kc + (size_t)head * SEQ * DH) + (size_t)(16 * wid) * DH * 2;
  const char* vbase = (const char*)(Vc + (size_t)head * SEQ * DH) + (size_t)(16 * wid) * DH * 2;
  const char* wbase = (const char*)(wq + head * DH) + (size_t)(16 * wid) * QD * 2;
  const unsigned kdst = lds0 + wid * 2048, vdst = lds0 + 3 * TB + wid * 6144;
  auto pieceK = [&](int key0, unsigned slot, int pc) {
    glds_s(kbase + (size_t)key0 * (DH * 2) + (pc & 1) * (8 * DH * 2) + (pc >> 1) * 128, (pc & 1) ? kvo1 : kvo0,
           kdst + slot + (pc & 1) * 1024 + (pc >> 1) * 8192);
  };
  auto pieceV = [&](int key0, unsigned slot, int pc) {
    glds_s(vbase + (size_t)key0 * (DH * 2) + (pc & 1) * (8 * DH * 2) + (pc >> 1) * 128, vvo,
           vdst + slot + (pc & 1) * 3072 + (pc >> 1) * 1024);
  };
  auto pieceW = [&](int t, unsigned slot, int pc) {
    glds_s(wbase + (size_t)t * (64 * QD * 2) + (size_t)(pc & 1) * (8 * QD * 2) + (pc >> 1) * 128, wvo,
           vdst + slot + (pc & 1) * 3072 + (pc >> 1) * 1024);
  };
  unsigned kpo[6], vpo[6];
#pragma unroll
  for (int pc = 0; pc < 6; ++pc) {
    kpo[pc] = ((pc & 1) ? kvo1 : kvo0) + (pc & 1) * (8 * DH * 2) + (pc >> 1) * 128;
    vpo[pc] = vvo + (pc & 1) * (8 * DH * 2) + (pc >> 1) * 128;
  }
  unsigned koff[4];
#pragma unroll
  for (int j = 0; j < 4; ++j) koff[j] = lds0 + r32 * 128 + (((2 * j + hi) ^ ((r32 >> 1) & 7)) << 4);
  const unsigned voff = lds0 + 3 * TB + (hi * 4 + ((lane >> 2) & 3)) * 64 + ((lane >> 4) & 1) * 32 + (lane & 3) * 8;

  bf16x8 zfr = bf16x8{};
  PIN(zfr);
  asm volatile("s_nop 3");
  sfor<12>([&](auto I) { acc_zero16<16 * decltype(I)::v>(zfr); });
  bf16x8 fr[4];

  const u16* xsrc0 = xnp + ((size_t)(q0 >> 5) * (40 * 64) + lane) * 8;
  const u16* xsrc1 = xsrc0 + 40 * 64 * 8;
  bf16x8 xa[2][4], xb[2][4];
  auto xload = [&](bf16x8 (&x)[2][4], int t) {
    const u16* p0 = xsrc0 + t * 2048;
    const u16* p1 = xsrc1 + t * 2048;
    asm volatile("global_load_dwordx4 %0, %8, off\n\tglobal_load_dwordx4 %1, %8, off offset:1024\n\t"
                 "global_load_dwordx4 %2, %8, off offset:2048\n\tglobal_load_dwordx4 %3, %8, off offset:3072\n\t"
                 "global_load_dwordx4 %4, %9, off\n\tglobal_load_dwordx4 %5, %9, off offset:1024\n\t"
                 "global_load_dwordx4 %6, %9, off offset:2048\n\tglobal_load_dwordx4 %7, %9, off offset:3072"
                 : "=&v"(x[0][0]), "=&v"(x[0][1]), "=&v"(x[0][2]), "=&v"(x[0][3]), "=&v"(x[1][0]), "=&v"(x[1][1]),
                   "=&v"(x[1][2]), "=&v"(x[1][3])
                 : "v"(p0), "v"(p1) : "memory");
  };
#pragma unroll
  for (int pc = 0; pc < 6; ++pc) pieceW(0, 2 * TB, pc);
#pragma unroll
  for (int pc = 0; pc < 6; ++pc) pieceW(1, 0, pc);
  xload(xa, 0);
  if (wid >= 2) {
#pragma unroll
    for (int pc = 0; pc < 6; ++pc) pieceK(-32, 2 * TB, pc);
  }
#pragma unroll
  for (int pc = 0; pc < 6; ++pc) pieceK(32, 0, pc);
#pragma unroll
  for (int pc = 0; pc < 6; ++pc) pieceK(96, TB, pc);
  {
    unsigned srd = 2 * TB, swr = TB;
    auto qstep = [&](bf16x8 (&xc)[2][4], bf16x8 (&xn)[2][4], const int t) {
      asm volatile("s_waitcnt vmcnt(6)\n\ts_barrier" : "+v"(xc[0][0]), "+v"(xc[0][1]), "+v"(xc[0][2]), "+v"(xc[0][3]), "+v"(xc[1][0]), "+v"(xc[1][1]), "+v"(xc[1][2]), "+v"(xc[1][3]) :: "memory");
      const unsigned vb = voff + srd;
#pragma unroll
      for (int i = 0; i < 4; ++i) fr[i] = rdV(vb + i * 512);
      const int tn = t + 1 < 10 ? t + 1 : 9;
      const u16* p0 = xsrc0 + tn * 2048;
      const u16* p1 = xsrc1 + tn * 2048;
      SB();
      const bool isw = t + 2 < 10;
      const char* ssrc = isw ? wbase + (size_t)(t + 2) * (64 * QD * 2) : vbase + (size_t)(t - 8) * (64 * DH * 2);
      const unsigned sgs = isw ? 8 * QD * 2 : 8 * DH * 2;
      const unsigned svo = isw ? wvo : vvo;
      sfor<24>([&](auto I) {
        constexpr int i = decltype(I)::v;
        mfma_o<AO(0, i % 6)>(fr[i & 3], xc[0][i / 6]);
        mfma_o<AO(1, i % 6)>(fr[i & 3], xc[1][i / 6]);
        if constexpr (i + 4 < 24) fr[i & 3] = rdV(vb + ((2 * ((i + 4) / 6)) * 6 + ((i + 4) % 6)) * 512);
        if constexpr (i < 8)
          asm volatile("global_load_dwordx4 %0, %1, off offset:%2" : "=&v"(xn[i >> 2][i & 3]) : "v"(i < 4 ? p0 : p1), "i"((i & 3) * 1024) : "memory");
        if constexpr (i >= 8 && i < 14) {
          constexpr int pc = i - 8;
          glds_s(ssrc + (size_t)(pc & 1) * sgs + (pc >> 1) * 128, svo, vdst + swr + (pc & 1) * 3072 + (pc >> 1) * 1024);
        }
        SB();
      });
      srd = nxt(srd);
      swr = nxt(swr);
    };
    asm volatile("s_waitcnt vmcnt(0)" : "+v"(xa[0][0]), "+v"(xa[0][1]), "+v"(xa[0][2]), "+v"(xa[0][3]), "+v"(xa[1][0]), "+v"(xa[1][1]), "+v"(xa[1][2]), "+v"(xa[1][3]) :: "memory");
#pragma unroll 1
    for (int t = 0; t < 10; t += 2) { qstep(xa, xb, t); qstep(xb, xa, t + 1); }
    asm volatile("s_waitcnt vmcnt(6)" : "+v"(xa[0][0]), "+v"(xa[0][1]), "+v"(xa[0][2]), "+v"(xa[0][3]), "+v"(xa[1][0]), "+v"(xa[1][1]), "+v"(xa[1][2]), "+v"(xa[1][3]) :: "memory");
  }
  asm volatile("s_nop 15\n\ts_nop 7" ::: "memory");
  SB();
  bf16x8 qf[2][4];
  sfor<24>([&](auto I) {
    constexpr int n = decltype(I)::v, b = n / 12, f = n % 12, R = AO(b, f >> 1) + 8 * (f & 1);
    float t8[8];
    acc_rd8<R>(t8);
    unsigned w0 = cvtpk(t8[0] * QSCALE, t8[1] * QSCALE);
    unsigned w1 = cvtpk(t8[2] * QSCALE, t8[3] * QSCALE);
    unsigned w2 = cvtpk(t8[4] * QSCALE, t8[5] * QSCALE);
    unsigned w3 = cvtpk(t8[6] * QSCALE, t8[7] * QSCALE);
    auto r0 = __builtin_amdgcn_permlane32_swap(w0, w2, false, false);
    auto r1 = __builtin_amdgcn_permlane32_swap(w1, w3, false, false);
    if constexpr (f < 8) {
      acc_wr4<AQ(b, f)>(r0[0], r1[0], r0[1], r1[1]);
    } else {
      u32x4 w = {r0[0], r1[0], r0[1], r1[1]};
      qf[b][f - 8] = __builtin_bit_cast(bf16x8, w);
    }
  });
  zfr = bf16x8{};
  PIN(zfr);
  SB();
  asm volatile("s_nop 7");
  sfor<12>([&](auto I) { acc_zero16<16 * decltype(I)::v>(zfr); });

  float m_run[2], l_run[2] = {0.f, 0.f}, alpha[2];
  bool pend = false;
  f32x16 S[2][2], nm[2];
  u32x4 pw[2][2];
  unsigned kb[4], vb;
  nm[0] = f32x16{}; nm[1] = f32x16{};
  PIN(nm[0]); PIN(nm[1]);
#pragma unroll
  for (int i = 0; i < 4; ++i) { PIN(qf[0][i]); PIN(qf[1][i]); }
#pragma unroll
  for (int i = 0; i < 4; ++i) fr[i] = rdK(koff[i] + 2 * TB + 4096);
  SB();
  asm volatile("s_nop 3" ::: "memory");
  SB();

  sfor<12>([&](auto I) {
    constexpr int i = decltype(I)::v;
    if constexpr (i == 0) { mfma_first<AQ(0, 0)>(S[0][0], fr[0], nm[0]); mfma_first<AQ(1, 0)>(S[0][1], fr[0], nm[1]); }
    else if constexpr (i < 8) { mfma_qa<AQ(0, i)>(S[0][0], fr[i & 3]); mfma_qa<AQ(1, i)>(S[0][1], fr[i & 3]); }
    else { mfma_qv(S[0][0], fr[i & 3], qf[0][i - 8]); mfma_qv(S[0][1], fr[i & 3], qf[1][i - 8]); }
    if constexpr (i + 4 < 12) fr[i & 3] = rdK(koff[i & 3] + 2 * TB + ((i + 4) >> 2) * 8192 + 4096);
    SB();
  });
  asm volatile("s_nop 15\n\ts_nop 7" :: "v"(nm[0]), "v"(nm[1]) : "memory");
  SB();
#pragma unroll
  for (int b = 0; b < 2; ++b) {
    float mx = S[0][b][0];
#pragma unroll
    for (int r = 1; r < 16; ++r) mx = fmaxf(mx, S[0][b][r]);
    auto rr = __builtin_amdgcn_permlane32_swap(__float_as_uint(mx), __float_as_uint(mx), false, false);
    mx = fmaxf(__uint_as_float(rr[0]), __uint_as_float(rr[1]));
    m_run[b] = mx;
#pragma unroll
    for (int r = 0; r < 16; ++r) { nm[b][r] = -mx; S[0][b][r] -= mx; }
  }
#pragma unroll
  for (int j = 0; j < 4; ++j) kb[j] = koff[j];
  vb = voff;
#pragma unroll
  for (int i = 0; i < 4; ++i) fr[i] = rdK(kb[i]);

  auto phaseA = [&](auto XX, auto HH, const char* ssrc, const unsigned sdst) {
    constexpr int X = decltype(XX)::v, HB = decltype(HH)::v;
    sfor<12>([&](auto I) {
      constexpr int i = decltype(I)::v;
      auto ex = [&](auto N) {
        constexpr int n = decltype(N)::v;
        if constexpr (n < 32) {
          constexpr int b = (n & 15) >> 3, r = (n >> 4) * 8 + (n & 7);
          S[X][b][r] = __builtin_amdgcn_exp2f(S[X][b][r]);
        }
      };
      auto pk = [&](auto N) {
        constexpr int n = decltype(N)::v;
        if constexpr (n >= 0 && n < 16) {
          constexpr int b = (n >> 2) & 1, ks = n >> 3, w = n & 3;
          pw[b][ks][w] = cvtpk(S[X][b][8 * ks + 2 * w], S[X][b][8 * ks + 2 * w + 1]);
        }
      };
      if constexpr (i == 0) mfma_first<AQ(0, 0)>(S[X ^ 1][0], fr[0], nm[0]);
      else if constexpr (i < 8) mfma_qa<AQ(0, i)>(S[X ^ 1][0], fr[i & 3]);
      else mfma_qv(S[X ^ 1][0], fr[i & 3], qf[0][i - 8]);
      SB();
      ex(IC<3 * i>{}); ex(IC<3 * i + 1>{});
      pk(IC<2 * i - 8>{});
      PIN(S[X][0]); PIN(S[X][1]);
      SB();
      if constexpr (i == 0) mfma_first<AQ(1, 0)>(S[X ^ 1][1], fr[0], nm[1]);
      else if constexpr (i < 8) mfma_qa<AQ(1, i)>(S[X ^ 1][1], fr[i & 3]);
      else mfma_qv(S[X ^ 1][1], fr[i & 3], qf[1][i - 8]);
      SB();
      if constexpr (i + 4 < 12) fr[i & 3] = rdK(kb[i & 3] + ((i + 4) >> 2) * 8192 + HB * 4096);
      else fr[i & 3] = rdV(vb + ((4 * HB) * 6 + (i - 8)) * 512);
      ex(IC<3 * i + 2>{});
      pk(IC<2 * i - 7>{});
      if constexpr (i == 2) asm volatile("" :: "v"(nm[0]), "v"(nm[1]));
      if constexpr (i == 1 || i == 5 || i == 9) {
        constexpr int pc = (i - 1) / 4;
        if constexpr (HB == 0) glds_i<(pc & 1) * 1024 + (pc >> 1) * 8192>(ssrc, kpo[pc], sdst);
        else glds_i<(pc & 1) * 3072 + (pc >> 1) * 1024>(ssrc, vpo[pc], sdst);
      }
      PIN(S[X][0]); PIN(S[X][1]); PIN(pw[0][0]); PIN(pw[0][1]); PIN(pw[1][0]); PIN(pw[1][1]);
      SB();
    });
  };
  auto phaseB = [&](auto XX, auto HH, auto HK, const char* ssrc, const unsigned sdst) {
    constexpr int X = decltype(XX)::v, HB = decltype(HH)::v, HBKN = decltype(HK)::v;
    float ls[2][2], mx[2];
    auto fill = [&](auto G) {
      constexpr int g = decltype(G)::v;
      if constexpr (g < 16) {
        constexpr int b = g >> 3, r = (g & 7) * 2;
        if constexpr ((g & 7) == 0) { ls[b][0] = S[X][b][r]; ls[b][1] = S[X][b][r + 1]; }
        else { ls[b][0] += S[X][b][r]; ls[b][1] += S[X][b][r + 1]; }
        PIN(ls[b][0]); PIN(ls[b][1]);
      }
      if constexpr (g == 16) { l_run[0] += ls[0][0] + ls[0][1]; PIN(l_run[0]); }
      if constexpr (g == 17) { l_run[1] += ls[1][0] + ls[1][1]; PIN(l_run[1]); }
      if constexpr (g == 20) {
#pragma unroll
        for (int b = 0; b < 2; ++b) {
          auto rr = __builtin_amdgcn_permlane32_swap(__float_as_uint(mx[b]), __float_as_uint(mx[b]), false, false);
          mx[b] = max2f(__uint_as_float(rr[0]), __uint_as_float(rr[1]));
          PIN(mx[b]);
        }
      }
      if constexpr (g == 21) {
        if (__builtin_expect(__any(max2f(mx[0], mx[1]) > THR), 0)) {
          const float aone = hi == 0 ? 1.f : 0.f;
#pragma unroll
          for (int b = 0; b < 2; ++b) {
            const float dl = fmaxf(mx[b], 0.f);
            m_run[b] += dl;
            alpha[b] = __builtin_amdgcn_exp2f(-dl);
            bcast16(nm[b], aone, -m_run[b]);
#pragma unroll
            for (int r = 0; r < 16; ++r) S[X ^ 1][b][r] -= dl;
          }
          pend = true;
        }
      }
      if constexpr (g == 16 || g == 18 || g == 22) {
        constexpr int pc = g == 16 ? 3 : (g == 18 ? 4 : 5);
        if constexpr (HB == 0) glds_i<(pc & 1) * 1024 + (pc >> 1) * 8192>(ssrc, kpo[pc], sdst);
        else glds_i<(pc & 1) * 3072 + (pc >> 1) * 1024>(ssrc, vpo[pc], sdst);
      }
    };
    auto pvm = [&](auto G) {
      constexpr int g = decltype(G)::v, i = g >> 1, b = g & 1, k = (g - 4) >> 1;
      const bf16x8 pb = __builtin_bit_cast(bf16x8, pw[b][i / 6]);
      if constexpr (g >= 4 && g < 20) {
        if constexpr (k == 0) mfma_o_mx<AO(b, i % 6)>(fr[i & 3], pb, mx[b], S[X ^ 1][b][0], S[X ^ 1][b][1], S[X ^ 1][b][2]);
        else if constexpr (k < 7) mfma_o_mx<AO(b, i % 6)>(fr[i & 3], pb, mx[b], mx[b], S[X ^ 1][b][2 * k + 1], S[X ^ 1][b][2 * k + 2]);
        else mfma_o_mx<AO(b, i % 6)>(fr[i & 3], pb, mx[b], mx[b], S[X ^ 1][b][15], S[X ^ 1][b][15]);
      } else mfma_o<AO(b, i % 6)>(fr[i & 3], pb);
    };
    sfor<12>([&](auto I) {
      constexpr int i = decltype(I)::v;
      pvm(IC<2 * i>{});
      SB();
      fill(IC<2 * i>{});
      SB();
      pvm(IC<2 * i + 1>{});
      SB();
      if constexpr (i + 4 < 12) fr[i & 3] = rdV(vb + ((4 * HB + 2 * ((i + 4) / 6)) * 6 + ((i + 4) % 6)) * 512);
      else fr[i & 3] = rdK(kb[i - 8] + HBKN * 4096);
      fill(IC<2 * i + 1>{});
      SB();
    });
    if (__builtin_expect(pend, 0)) {
      asm volatile("s_nop 15\n\ts_nop 7" ::: "memory");
      l_run[0] *= alpha[0]; l_run[1] *= alpha[1];
      sfor<192>([&](auto I) { constexpr int n = decltype(I)::v; acc_scale<n>(alpha[n / 96]); });
      asm volatile("s_nop 3" ::: "memory");
      pend = false;
    }
  };

  asm volatile("s_waitcnt vmcnt(0)" ::: "memory");
  unsigned scur = 0;
#pragma unroll 1
  for (int t = 0; t < 32; ++t) {
    const unsigned snxt = nxt(scur), swr = nxt(snxt);
    asm volatile("s_waitcnt vmcnt(6)\n\ts_barrier" ::: "memory");
    int tk = t + 2 < 32 ? t + 2 : 31;
    const int tv = tk;
    if (tk == 31 && wid >= 2) tk = 30;
    const char* ks_ = kbase + (size_t)(tk * 64 + 32) * (DH * 2);
    const char* vs_ = vbase + (size_t)(tv * 64) * (DH * 2);
    SB();
    phaseA(IC<0>{}, IC<0>{}, ks_, kdst + swr);
    phaseB(IC<0>{}, IC<0>{}, IC<1>{}, ks_, kdst + swr);
    phaseA(IC<1>{}, IC<1>{}, vs_, vdst + swr);
#pragma unroll
    for (int j = 0; j < 4; ++j) kb[j] = koff[j] + snxt;
    phaseB(IC<1>{}, IC<1>{}, IC<0>{}, vs_, vdst + swr);
    vb = voff + snxt;
    scur = snxt;
  }
  asm volatile("s_waitcnt vmcnt(0)\n\ts_nop 15\n\ts_nop 7" ::: "memory");
  int tz = threadIdx.x;
  asm volatile("" : "+v"(tz));
  float rls[2];
#pragma unroll
  for (int b = 0; b < 2; ++b) {
    auto rr = __builtin_amdgcn_permlane32_swap(__float_as_uint(l_run[b]), __float_as_uint(l_run[b]), false, false);
    rls[b] = 1.0f / (__uint_as_float(rr[0]) + __uint_as_float(rr[1]));
  }
  u32x4* ofr = (u32x4*)O + ((size_t)(qb * 8 + (tz >> 6) * 2) * 384 + head * 12) * 64 + (tz & 63);
  sfor<24>([&](auto I) {
    constexpr int n = decltype(I)::v, b = n / 12, f = n % 12, R = AO(b, f >> 1) + 8 * (f & 1);
    const float rl = rls[b];
    float t8[8];
    acc_rd8<R>(t8);
    ofr[(b * 384 + f) * 64] = u32x4{cvtpk(t8[0] * rl, t8[1] * rl), cvtpk(t8[2] * rl, t8[3] * rl),
                                    cvtpk(t8[4] * rl, t8[5] * rl), cvtpk(t8[6] * rl, t8[7] * rl)};
  });
}

extern "C" void kernel_launch(void* const* d_in, const int* in_sizes, int n_in, void* d_out, int out_size,
                              void* d_ws, size_t ws_size, hipStream_t stream) {
  const u16* x = (const u16*)d_in[0];
  const u16* kc = (const u16*)d_in[1];
  const u16* vc = (const u16*)d_in[2];
  const u16* wq = (const u16*)d_in[3];
  const u16* wo = (const u16*)d_in[4];
  const u16* w1 = (const u16*)d_in[6];
  const u16* b1 = (const u16*)d_in[7];
  const u16* w2 = (const u16*)d_in[8];
  const u16* b2 = (const u16*)d_in[9];
  char* ws = (char*)d_ws;
  u16* xnp = (u16*)(ws);
  u16* y = (u16*)(ws + (4u << 20));
  u16* h = (u16*)(ws + (8u << 20));
  u16* O = (u16*)(ws + (16u << 20));
  float* part = (float*)(ws + (48u << 20));

  hipFuncSetAttribute((const void*)k_attn, hipFuncAttributeMaxDynamicSharedMemorySize, 6 * TB);
  hipFuncSetAttribute((const void*)k_gemm1, hipFuncAttributeMaxDynamicSharedMemorySize, 10 * 12288);
  hipFuncSetAttribute((const void*)k_gemm2, hipFuncAttributeMaxDynamicSharedMemorySize, 4 * 16384);

  k_ln<<<SEQ / 4, 256, 0, stream>>>(x, xnp, y);
  k_gemm1<<<256, 256, 10 * 12288, stream>>>(y, w1, b1, h);
  k_attn<<<256, 256, 6 * TB, stream>>>(xnp, wq, kc, vc, O);
  k_gemm2<<<200, 256, 4 * 16384, stream>>>(O, wo, h, w2, part);
  k_final<<<SEQ * DM / 1024, 256, 0, stream>>>(x, part, b2, (u16*)d_out);
}
```

```cpp
#include <hip/hip_runtime.h>
#include <hip/hip_bf16.h>
#include <stdint.h>

typedef __attribute__((ext_vector_type(8))) short bf16x8;
typedef __attribute__((ext_vector_type(8))) __bf16 bf16x8b;
typedef __attribute__((ext_vector_type(4))) short s16x4;
typedef __attribute__((ext_vector_type(16))) float f32x16;
typedef __attribute__((ext_vector_type(4))) float f32x4;
typedef __attribute__((ext_vector_type(2))) float f32x2;
typedef __attribute__((ext_vector_type(2))) __bf16 bf16x2b;
typedef __attribute__((ext_vector_type(4))) unsigned u32x4;
typedef __attribute__((ext_vector_type(2))) unsigned u32x2;
typedef __attribute__((address_space(3))) char* lds_ptr;
typedef unsigned short u16;

#define DI __device__ __forceinline__

constexpr int SEQ = 2048, DM = 640, NH = 32, DH = 192, FF = 1536, QD = NH * DH;

DI f32x16 MFMA(bf16x8 a, bf16x8 b, f32x16 c) {
  return __builtin_amdgcn_mfma_f32_32x32x16_bf16(__builtin_bit_cast(bf16x8b, a), __builtin_bit_cast(bf16x8b, b), c, 0, 0, 0);
}
DI unsigned cvtpk(float lo, float hi) {
  f32x2 v = {lo, hi};
  bf16x2b b = __builtin_convertvector(v, bf16x2b);
  return __builtin_bit_cast(unsigned, b);
}
DI float bf2f(u16 v) { return __uint_as_float(((unsigned)v) << 16); }
DI float rbf(float v) { return __uint_as_float(cvtpk(v, 0.f) << 16); }
DI s16x4 trread(lds_ptr p) {
  return __builtin_amdgcn_ds_read_tr16_b64_v4i16((__attribute__((address_space(3))) s16x4*)p);
}
DI bf16x8 ldsread(lds_ptr p) { return *(__attribute__((address_space(3))) bf16x8*)p; }
DI bf16x8 cat(s16x4 a, s16x4 b) { return __builtin_shufflevector(a, b, 0, 1, 2, 3, 4, 5, 6, 7); }
DI void glds16(const void* g, lds_ptr l) {
  __builtin_amdgcn_global_load_lds((const __attribute__((address_space(1))) unsigned*)g,
                                   (__attribute__((address_space(3))) unsigned*)l, 16, 0, 0);
}

DI float wave_sum(float v) {
#pragma unroll
  for (int o = 32; o > 0; o >>= 1) v += __shfl_xor(v, o, 64);
  return v;
}
DI void ln_row(float (&v)[10]) {
  float s = 0.f;
#pragma unroll
  for (int i = 0; i < 10; ++i) s += v[i];
  s = wave_sum(s);
  const float mean = s * (1.0f / DM);
  float q = 0.f;
#pragma unroll
  for (int i = 0; i < 10; ++i) { float d = v[i] - mean; q += d * d; }
  q = wave_sum(q);
  const float mb = rbf(mean), vb = rbf(q * (1.0f / DM));
  const float den = rbf(sqrtf(rbf(vb + 1e-5f)));
#pragma unroll
  for (int i = 0; i < 10; ++i) v[i] = rbf(rbf(v[i] - mb) / den);
}
__global__ __launch_bounds__(256) void k_ln(const u16* __restrict__ x, u16* __restrict__ xnp, u16* __restrict__ y) {
  const int row = blockIdx.x * 4 + (threadIdx.x >> 6), lane = threadIdx.x & 63;
  const unsigned* xr = (const unsigned*)(x + (size_t)row * DM);
  float v[10];
#pragma unroll
  for (int i = 0; i < 5; ++i) {
    unsigned w = xr[lane + 64 * i];
    v[2 * i] = __uint_as_float(w << 16);
    v[2 * i + 1] = __uint_as_float(w & 0xffff0000u);
  }
  ln_row(v);
  unsigned* xo = (unsigned*)xnp + (size_t)(row >> 5) * (40 * 64 * 4) + (row & 31) * 4;
#pragma unroll
  for (int i = 0; i < 5; ++i) {
    int e = lane + 64 * i;
    int ep = (e & ~6) | ((e & 2) << 1) | ((e & 4) >> 1);
    xo[(ep >> 3) * 256 + ((ep >> 2) & 1) * 128 + (ep & 3)] =
        (__float_as_uint(v[2 * i]) >> 16) | (__float_as_uint(v[2 * i + 1]) & 0xffff0000u);
  }
  ln_row(v);
  unsigned* yo = (unsigned*)y + (size_t)(row >> 5) * (40 * 64 * 4) + (row & 31) * 4;
#pragma unroll
  for (int i = 0; i < 5; ++i) {
    int e = lane + 64 * i;
    int ep = (e & ~6) | ((e & 2) << 1) | ((e & 4) >> 1);
    yo[(ep >> 3) * 256 + ((ep >> 2) & 1) * 128 + (ep & 3)] =
        (__float_as_uint(v[2 * i]) >> 16) | (__float_as_uint(v[2 * i + 1]) & 0xffff0000u);
  }
}

#define SB() __builtin_amdgcn_sched_barrier(0)
#define PIN(x) asm volatile("" : "+v"(x))
template <int I> struct IC { static constexpr int v = I; };
template <int N, class F> DI void sfor(F&& f) {
  if constexpr (N > 0) { sfor<N - 1>(f); f(IC<N - 1>{}); }
}
template <int IMM> DI void glds_i(const void* sbase, unsigned voff, unsigned ldsbase) {
  asm volatile("s_add_i32 m0, %2, %3\n\ts_nop 0\n\tglobal_load_lds_dwordx4 %0, %1"
               :: "v"(voff), "s"(sbase), "s"(ldsbase), "i"(IMM) : "memory", "scc");
}
template <int OFF> DI void gload16(bf16x8& d, const char* p) {
  asm volatile("global_load_dwordx4 %0, %1, off offset:%2" : "=&v"(d) : "v"(p), "i"(OFF) : "memory");
}
template <int N> DI void xwait2(bf16x8 (&x)[2][4]) {
  asm volatile("s_waitcnt vmcnt(%8)\n\ts_barrier" : "+v"(x[0][0]), "+v"(x[0][1]), "+v"(x[0][2]), "+v"(x[0][3]), "+v"(x[1][0]), "+v"(x[1][1]), "+v"(x[1][2]), "+v"(x[1][3]) : "i"(N) : "memory");
}
DI void xpin2(bf16x8 (&x)[2][4]) {
  asm volatile("" : "+v"(x[0][0]), "+v"(x[0][1]), "+v"(x[0][2]), "+v"(x[0][3]), "+v"(x[1][0]), "+v"(x[1][1]), "+v"(x[1][2]), "+v"(x[1][3]));
}

DI void wgemm24(const char* wbase, unsigned ldwb, const char* xp, unsigned xrbs, int ktiles, unsigned lds0,
                f32x16 (&acc)[2][4]) {
  constexpr int NB = 4;
  constexpr unsigned TBY = 4096 * NB;
  const int tid = threadIdx.x, lane = tid & 63, wid = __builtin_amdgcn_readfirstlane(tid >> 6);
  const int hi = lane >> 5;
  unsigned wpo[NB];
#pragma unroll
  for (int i = 0; i < NB; ++i) {
    const int sub = 2 * (wid * NB + i) + hi, kg = sub / NB, nb = sub % NB;
    wpo[i] = (unsigned)(8 * kg + ((lane >> 2) & 7)) * ldwb + nb * 64 + (lane & 3) * 16;
  }
  const unsigned wdst = lds0 + wid * NB * 1024;
  const unsigned voff = lds0 + (hi * 4 + ((lane >> 2) & 3)) * 64 + ((lane >> 4) & 1) * 32 + (lane & 3) * 8;
  auto rdW = [&](unsigned a) { return cat(trread((lds_ptr)(uintptr_t)a), trread((lds_ptr)(uintptr_t)(a + NB * 512))); };
#pragma unroll
  for (int r = 0; r < 2; ++r)
#pragma unroll
    for (int n = 0; n < NB; ++n) acc[r][n] = f32x16{};
  bf16x8 x0[2][4], x1[2][4], x2[2][4], x3[2][4], fr[4];
  const size_t wts = (size_t)64 * ldwb;
  auto issue = [&](bf16x8 (&x)[2][4], int t, auto SLOT) {
    const char* xq = xp + (size_t)t * 4096;
    sfor<8>([&](auto I) { constexpr int i = decltype(I)::v; gload16<(i & 3) * 1024>(x[i >> 2][i & 3], xq + (i >> 2) * xrbs); });
    sfor<NB>([&](auto I) { glds_i<decltype(SLOT)::v * TBY + decltype(I)::v * 1024>(wbase + (size_t)t * wts, wpo[decltype(I)::v], wdst); });
  };
  issue(x0, 0, IC<0>{});
  issue(x1, 1, IC<1>{});
  issue(x2, 2, IC<2>{});
  auto step = [&](bf16x8 (&xc)[2][4], bf16x8 (&xn)[2][4], const int t, auto SLOT) {
    constexpr int sl = decltype(SLOT)::v, sw = (sl + 3) & 3;
    xwait2<24>(xc);
    const unsigned vb = voff + sl * TBY;
#pragma unroll
    for (int i = 0; i < 4; ++i) fr[i] = rdW(vb + i * 512);
    const int tn = t + 3 < ktiles ? t + 3 : ktiles - 1;
    const char* xq = xp + (size_t)tn * 4096;
    const char* wq_ = wbase + (size_t)tn * wts;
    SB();
    sfor<16>([&](auto I) {
      constexpr int i = decltype(I)::v;
      acc[0][i & 3] = MFMA(fr[i & 3], xc[0][i >> 2], acc[0][i & 3]);
      acc[1][i & 3] = MFMA(fr[i & 3], xc[1][i >> 2], acc[1][i & 3]);
      SB();
      if constexpr (i + 4 < 16) fr[i & 3] = rdW(vb + ((2 * ((i + 4) >> 2)) * NB + (i & 3)) * 512);
      if constexpr (i < 8) gload16<(i & 3) * 1024>(xn[i >> 2][i & 3], xq + (i >> 2) * xrbs);
      if constexpr (i >= 8 && i < 12) glds_i<sw * TBY + (i - 8) * 1024>(wq_, wpo[i - 8], wdst);
      SB();
    });
  };
#pragma unroll 1
  for (int t = 0; t < ktiles; t += 4) {
    step(x0, x3, t, IC<0>{});
    step(x1, x0, t + 1, IC<1>{});
    step(x2, x1, t + 2, IC<2>{});
    step(x3, x2, t + 3, IC<3>{});
  }
  asm volatile("s_waitcnt vmcnt(0)" ::: "memory");
  xpin2(x0); xpin2(x1); xpin2(x2);
}
DI int crow(int r, int hi) { return (r & 3) + 8 * (r >> 2) + 4 * hi; }

DI float gelu_tanh(float v) {
  const float u = 0.7978845608028654f * (v + 0.044715f * v * v * v);
  const float e = __builtin_amdgcn_exp2f(2.8853900817779268f * u);
  return v - v * __builtin_amdgcn_rcpf(e + 1.f);
}

__global__ __launch_bounds__(256, 1) void k_gemm2(const u16* __restrict__ O, const u16* __restrict__ wo,
                                                  const u16* __restrict__ h, const u16* __restrict__ w2,
                                                  float* __restrict__ part) {
  extern __shared__ __attribute__((aligned(16))) char smem[];
  const int bid = blockIdx.x, mt = bid & 7, r = bid >> 3;
  const int nt = r % 5, c = r / 5;
  const int lane = threadIdx.x & 63, wid = threadIdx.x >> 6;
  const int rb0 = mt * 8 + wid * 2;
  const char* W; const char* X; unsigned xrbs;
  if (c < 4) {
    W = (const char*)(wo + (size_t)c * 24 * 64 * DM + nt * 128);
    xrbs = 384 * 1024;
    X = (const char*)O + ((size_t)rb0 * 384 + c * 96) * 1024 + lane * 16;
  } else {
    W = (const char*)(w2 + nt * 128);
    xrbs = 96 * 1024;
    X = (const char*)h + (size_t)rb0 * 96 * 1024 + lane * 16;
  }
  f32x16 acc[2][4];
  wgemm24(W, DM * 2, X, xrbs, 24, (unsigned)(uintptr_t)(lds_ptr)smem, acc);
  f32x4* P = (f32x4*)part + (size_t)c * (SEQ * DM / 4);
#pragma unroll
  for (int b = 0; b < 2; ++b)
#pragma unroll
    for (int nb = 0; nb < 4; ++nb)
#pragma unroll
      for (int q = 0; q < 4; ++q)
        P[(((size_t)(rb0 + b) * 20 + nt * 4 + nb) * 4 + q) * 64 + lane] =
            f32x4{acc[b][nb][4 * q], acc[b][nb][4 * q + 1], acc[b][nb][4 * q + 2], acc[b][nb][4 * q + 3]};
}

__global__ __launch_bounds__(256) void k_final(const u16* __restrict__ x, const float* __restrict__ part,
                                               const u16* __restrict__ b2, u16* __restrict__ out) {
  const int idx = blockIdx.x * 256 + threadIdx.x;
  constexpr size_t PS = (size_t)SEQ * DM / 4;
  const f32x4* P = (const f32x4*)part + idx;
  const f32x4 p0 = P[0], p1 = P[PS], p2 = P[2 * PS], p3 = P[3 * PS], pm = P[4 * PS];
  const int lane = idx & 63, q = (idx >> 6) & 3, blk = idx >> 8, cb = blk % 20, rb = blk / 20;
  const int m = rb * 32 + (lane & 31), n = cb * 32 + 8 * q + 4 * (lane >> 5);
  const u32x2 xw = *(const u32x2*)(x + (size_t)m * DM + n);
  const u32x2 bw = *(const u32x2*)(b2 + n);
  float xo[4] = {__uint_as_float(xw[0] << 16), __uint_as_float(xw[0] & 0xffff0000u), __uint_as_float(xw[1] << 16),
                 __uint_as_float(xw[1] & 0xffff0000u)};
  float bo[4] = {__uint_as_float(bw[0] << 16), __uint_as_float(bw[0] & 0xffff0000u), __uint_as_float(bw[1] << 16),
                 __uint_as_float(bw[1] & 0xffff0000u)};
  float r[4];
#pragma unroll
  for (int i = 0; i < 4; ++i) {
    const float s = rbf((p0[i] + p1[i]) + (p2[i] + p3[i]));
    const float mm = rbf(rbf(pm[i]) + bo[i]);
    const float g = rbf(mm / (1.f + __expf(-mm)));
    r[i] = xo[i] + rbf(s * g);
  }
  u32x2 o = {cvtpk(r[0], r[1]), cvtpk(r[2], r[3])};
  *(u32x2*)(out + (size_t)m * DM + n) = o;
}

constexpr int TB = 24576;
constexpr float QSCALE = 0.07216878364870322f * 1.4426950408889634f;
constexpr float THR = 20.0f;

DI void glds_s(const void* sbase, unsigned voff, unsigned ldsaddr) {
  asm volatile("s_mov_b32 m0, %2\n\ts_nop 0\n\tglobal_load_lds_dwordx4 %0, %1"
               :: "v"(voff), "s"(sbase), "s"(ldsaddr) : "memory");
}
DI float max3f(float a, float b, float c) { float r; asm("v_max3_f32 %0, %1, %2, %3" : "=v"(r) : "v"(a), "v"(b), "v"(c)); return r; }
DI float max2f(float a, float b) { float r; asm("v_max_f32_e32 %0, %1, %2" : "=v"(r) : "v"(a), "v"(b)); return r; }
DI void bcast16(f32x16& d, float aone, float negm) {
  asm volatile("s_nop 1\n\tv_mfma_f32_32x32x2_f32 %0, %1, %2, 0" : "+v"(d) : "v"(aone), "v"(negm));
}
DI bf16x8 rdK(unsigned a) { return ldsread((lds_ptr)(uintptr_t)a); }
DI bf16x8 rdV(unsigned a) { return cat(trread((lds_ptr)(uintptr_t)a), trread((lds_ptr)(uintptr_t)(a + 3072))); }
DI unsigned nxt(unsigned s) { return s == 2 * TB ? 0 : s + TB; }
#define ACLOB "a0","a1","a2","a3","a4","a5","a6","a7","a8","a9","a10","a11","a12","a13","a14","a15","a16","a17","a18","a19","a20","a21","a22","a23","a24","a25","a26","a27","a28","a29","a30","a31","a32","a33","a34","a35","a36","a37","a38","a39","a40","a41","a42","a43","a44","a45","a46","a47","a48","a49","a50","a51","a52","a53","a54","a55","a56","a57","a58","a59","a60","a61","a62","a63","a64","a65","a66","a67","a68","a69","a70","a71","a72","a73","a74","a75","a76","a77","a78","a79","a80","a81","a82","a83","a84","a85","a86","a87","a88","a89","a90","a91","a92","a93","a94","a95","a96","a97","a98","a99","a100","a101","a102","a103","a104","a105","a106","a107","a108","a109","a110","a111","a112","a113","a114","a115","a116","a117","a118","a119","a120","a121","a122","a123","a124","a125","a126","a127","a128","a129","a130","a131","a132","a133","a134","a135","a136","a137","a138","a139","a140","a141","a142","a143","a144","a145","a146","a147","a148","a149","a150","a151","a152","a153","a154","a155","a156","a157","a158","a159","a160","a161","a162","a163","a164","a165","a166","a167","a168","a169","a170","a171","a172","a173","a174","a175","a176","a177","a178","a179","a180","a181","a182","a183","a184","a185","a186","a187","a188","a189","a190","a191","a192","a193","a194","a195","a196","a197","a198","a199","a200","a201","a202","a203","a204","a205","a206","a207","a208","a209","a210","a211","a212","a213","a214","a215","a216","a217","a218","a219","a220","a221","a222","a223","a224","a225","a226","a227","a228","a229","a230","a231","a232","a233","a234","a235","a236","a237","a238","a239","a240","a241","a242","a243","a244","a245","a246","a247","a248","a249","a250","a251","a252","a253","a254","a255"
constexpr int AO(int b, int db) { return 16 * (6 * b + db); }
constexpr int AQ(int b, int i) { return 192 + 32 * b + 4 * i; }
template <int R> DI void mfma_o(const bf16x8& a, const bf16x8& b) {
  asm volatile("v_mfma_f32_32x32x16_bf16 a[%c2:%c3], %0, %1, a[%c2:%c3]" :: "v"(a), "v"(b), "i"(R), "i"(R + 15) : ACLOB);
}
template <int R> DI void mfma_o_mx(const bf16x8& a, const bf16x8& b, float& m, float x, float y, float z) {
  asm volatile("v_mfma_f32_32x32x16_bf16 a[%c6:%c7], %1, %2, a[%c6:%c7]\n\tv_max3_f32 %0, %3, %4, %5"
               : "=v"(m) : "v"(a), "v"(b), "v"(x), "v"(y), "v"(z), "i"(R), "i"(R + 15) : ACLOB);
}
template <int R> DI void mfma_first(f32x16& d, const bf16x8& a, const f32x16& c) {
  asm volatile("v_mfma_f32_32x32x16_bf16 %0, %1, a[%c3:%c4], %2" : "=&v"(d) : "v"(a), "v"(c), "i"(R), "i"(R + 3) : ACLOB);
}
template <int R> DI void mfma_qa(f32x16& d, const bf16x8& a) {
  asm volatile("v_mfma_f32_32x32x16_bf16 %0, %1, a[%c2:%c3], %0" : "+v"(d) : "v"(a), "i"(R), "i"(R + 3) : ACLOB);
}
DI void mfma_qv(f32x16& d, const bf16x8& a, const bf16x8& b) {
  asm volatile("v_mfma_f32_32x32x16_bf16 %0, %1, %2, %0" : "+v"(d) : "v"(a), "v"(b));
}
template <int R> DI float acc_rd() { float r; asm volatile("v_accvgpr_read_b32 %0, a[%c1]" : "=v"(r) : "i"(R)); return r; }
template <int R> DI void acc_wr(unsigned v) { asm volatile("v_accvgpr_write_b32 a[%c1], %0" :: "v"(v), "i"(R) : ACLOB); }
template <int R> DI void acc_zero() { asm volatile("v_accvgpr_write_b32 a[%c0], 0" :: "i"(R) : ACLOB); }
template <int R> DI void acc_zero16(const bf16x8& z) {
  asm volatile("v_mfma_f32_32x32x16_bf16 a[%c1:%c2], %0, %0, 0" :: "v"(z), "i"(R), "i"(R + 15) : ACLOB);
}
template <int R> DI void acc_rd8(float (&r)[8]) {
  asm volatile("v_accvgpr_read_b32 %0, a[%c8]\n\tv_accvgpr_read_b32 %1, a[%c9]\n\tv_accvgpr_read_b32 %2, a[%c10]\n\tv_accvgpr_read_b32 %3, a[%c11]\n\t"
               "v_accvgpr_read_b32 %4, a[%c12]\n\tv_accvgpr_read_b32 %5, a[%c13]\n\tv_accvgpr_read_b32 %6, a[%c14]\n\tv_accvgpr_read_b32 %7, a[%c15]"
               : "=v"(r[0]), "=v"(r[1]), "=v"(r[2]), "=v"(r[3]), "=v"(r[4]), "=v"(r[5]), "=v"(r[6]), "=v"(r[7])
               : "i"(R), "i"(R + 1), "i"(R + 2), "i"(R + 3), "i"(R + 4), "i"(R + 5), "i"(R + 6), "i"(R + 7));
}
template <int R> DI void acc_wr4(unsigned a, unsigned b, unsigned c, unsigned d) {
  asm volatile("v_accvgpr_write_b32 a[%c4], %0\n\tv_accvgpr_write_b32 a[%c5], %1\n\tv_accvgpr_write_b32 a[%c6], %2\n\tv_accvgpr_write_b32 a[%c7], %3"
               :: "v"(a), "v"(b), "v"(c), "v"(d), "i"(R), "i"(R + 1), "i"(R + 2), "i"(R + 3) : ACLOB);
}
template <int R> DI void acc_scale(float f) {
  float t;
  asm volatile("v_accvgpr_read_b32 %0, a[%c2]\n\tv_mul_f32 %0, %1, %0\n\tv_accvgpr_write_b32 a[%c2], %0" : "=&v"(t) : "v"(f), "i"(R) : ACLOB);
}

__global__ __launch_bounds__(256, 1) void k_attn(const u16* __restrict__ xnp, const u16* __restrict__ wq,
                                                 const u16* __restrict__ Kc, const u16* __restrict__ Vc,
                                                 u16* __restrict__ O, const u16* __restrict__ yfr,
                                                 const u16* __restrict__ w1, const u16* __restrict__ b1,
                                                 u16* __restrict__ hfr) {
  extern __shared__ __attribute__((aligned(16))) char smem[];
  const unsigned lds0 = (unsigned)(uintptr_t)(lds_ptr)smem;
  const int tid = threadIdx.x, lane = tid & 63, wid = __builtin_amdgcn_readfirstlane(tid >> 6);
  const int r32 = lane & 31, hi = lane >> 5;
  const int bid = blockIdx.x, xcd = bid & 7, idx = bid >> 3;
  const int head = xcd * 4 + (idx >> 3), qb = idx & 7;
  const int q0 = qb * 256 + wid * 64;

  const int krl = lane >> 3;
  const unsigned kvo0 = (unsigned)(krl * DH * 2 + (((lane & 7) ^ (krl >> 1)) << 4));
  const unsigned kvo1 = (unsigned)(krl * DH * 2 + (((lane & 7) ^ (4 + (krl >> 1))) << 4));
  const unsigned vvo = (unsigned)(((lane >> 2) & 7) * DH * 2 + (lane >> 5) * 64 + (lane & 3) * 16);
  const unsigned wvo = (unsigned)(((lane >> 2) & 7) * QD * 2 + (lane >> 5) * 64 + (lane & 3) * 16);
  const char* kbase = (const char*)(Kc + (size_t)head * SEQ * DH) + (size_t)(16 * wid) * DH * 2;
  const char* vbase = (const char*)(Vc + (size_t)head * SEQ * DH) + (size_t)(16 * wid) * DH * 2;
  const char* wbase = (const char*)(wq + head * DH) + (size_t)(16 * wid) * QD * 2;
  const unsigned kdst = lds0 + wid * 2048, vdst = lds0 + 3 * TB + wid * 6144;
  auto pieceK = [&](int key0, unsigned slot, int pc) {
    glds_s(kbase + (size_t)key0 * (DH * 2) + (pc & 1) * (8 * DH * 2) + (pc >> 1) * 128, (pc & 1) ? kvo1 : kvo0,
           kdst + slot + (pc & 1) * 1024 + (pc >> 1) * 8192);
  };
  auto pieceV = [&](int key0, unsigned slot, int pc) {
    glds_s(vbase + (size_t)key0 * (DH * 2) + (pc & 1) * (8 * DH * 2) + (pc >> 1) * 128, vvo,
           vdst + slot + (pc & 1) * 3072 + (pc >> 1) * 1024);
  };
  auto pieceW = [&](int t, unsigned slot, int pc) {
    glds_s(wbase + (size_t)t * (64 * QD * 2) + (size_t)(pc & 1) * (8 * QD * 2) + (pc >> 1) * 128, wvo,
           vdst + slot + (pc & 1) * 3072 + (pc >> 1) * 1024);
  };
  unsigned kpo[6], vpo[6];
#pragma unroll
  for (int pc = 0; pc < 6; ++pc) {
    kpo[pc] = ((pc & 1) ? kvo1 : kvo0) + (pc & 1) * (8 * DH * 2) + (pc >> 1) * 128;
    vpo[pc] = vvo + (pc & 1) * (8 * DH * 2) + (pc >> 1) * 128;
  }
  unsigned koff[4];
#pragma unroll
  for (int j = 0; j < 4; ++j) koff[j] = lds0 + r32 * 128 + (((2 * j + hi) ^ ((r32 >> 1) & 7)) << 4);
  const unsigned voff = lds0 + 3 * TB + (hi * 4 + ((lane >> 2) & 3)) * 64 + ((lane >> 4) & 1) * 32 + (lane & 3) * 8;

  bf16x8 zfr = bf16x8{};
  PIN(zfr);
  asm volatile("s_nop 3");
  sfor<12>([&](auto I) { acc_zero16<16 * decltype(I)::v>(zfr); });
  bf16x8 fr[4];

  const int odd = head & 1, hj = head >> 1;
  const char* xsrc0 = (const char*)xnp + ((size_t)(q0 >> 5) * (40 * 64) + lane) * 16;
  const char* ysrcA = (const char*)yfr + ((size_t)((q0 >> 5) + odd) * (40 * 64) + lane) * 16;
  const char* ysrcB = (const char*)yfr + ((size_t)((q0 >> 5) + 1 - odd) * (40 * 64) + lane) * 16;
  const char* w1base = (const char*)(w1 + (3 * hj + odd) * 32) + (size_t)(16 * wid) * (FF * 2);
  const unsigned w1vo = (unsigned)(((lane >> 2) & 7) * FF * 2 + (lane >> 5) * 64 + (lane & 3) * 16);
  const unsigned w1dst = lds0 + TB + wid * 2048;
  const unsigned w1rd = lds0 + TB + (hi * 4 + ((lane >> 2) & 3)) * 64 + ((lane >> 4) & 1) * 32 + (lane & 3) * 8;
  const unsigned w1P = w1rd + (odd ? 512 : 0), w1Q = w1rd + (odd ? 0 : 512);
  auto rdH = [&](unsigned a) { return cat(trread((lds_ptr)(uintptr_t)a), trread((lds_ptr)(uintptr_t)(a + 1024))); };
  bf16x8 xa[4][4], xb[4][4];
  f32x16 hacc[3];
  hacc[0] = f32x16{}; hacc[1] = f32x16{}; hacc[2] = f32x16{};
  PIN(hacc[0]); PIN(hacc[1]); PIN(hacc[2]);
#pragma unroll
  for (int pc = 0; pc < 6; ++pc) pieceW(0, 2 * TB, pc);
#pragma unroll
  for (int pc = 0; pc < 6; ++pc) pieceW(1, 0, pc);
#pragma unroll
  for (int t = 0; t < 2; ++t)
#pragma unroll
    for (int g = 0; g < 2; ++g)
      glds_s(w1base + (size_t)t * (64 * FF * 2) + (size_t)g * (8 * FF * 2), w1vo, w1dst + (t == 0 ? 2 : 0) * 8192 + g * 1024);
  sfor<16>([&](auto I) {
    constexpr int i = decltype(I)::v;
    gload16<(i & 3) * 1024>(xa[i >> 2][i & 3], (i >> 2) == 0 ? xsrc0 : (i >> 2) == 1 ? xsrc0 + 40960 : (i >> 2) == 2 ? ysrcA : ysrcB);
  });
  if (wid >= 2) {
#pragma unroll
    for (int pc = 0; pc < 6; ++pc) pieceK(-32, 2 * TB, pc);
  }
#pragma unroll
  for (int pc = 0; pc < 6; ++pc) pieceK(32, 0, pc);
#define XOPS(x) "+v"(x[0][0]), "+v"(x[0][1]), "+v"(x[0][2]), "+v"(x[0][3]), "+v"(x[1][0]), "+v"(x[1][1]), "+v"(x[1][2]), "+v"(x[1][3]), \
                "+v"(x[2][0]), "+v"(x[2][1]), "+v"(x[2][2]), "+v"(x[2][3]), "+v"(x[3][0]), "+v"(x[3][1]), "+v"(x[3][2]), "+v"(x[3][3])
  {
    unsigned srd = 2 * TB, swr = TB;
    auto qstep = [&](bf16x8 (&xc)[4][4], bf16x8 (&xn)[4][4], const int t) {
      asm volatile("s_waitcnt vmcnt(8)\n\ts_barrier" : XOPS(xc) :: "memory");
      const unsigned vb = voff + srd;
      const unsigned hP = w1P + srd / 3, hQ = w1Q + srd / 3;
#pragma unroll
      for (int i = 0; i < 4; ++i) fr[i] = rdV(vb + i * 512);
      const int tn = t + 1 < 10 ? t + 1 : 9, tw = t + 2 < 10 ? t + 2 : 9;
      const char* p0 = xsrc0 + tn * 4096;
      const char* p2 = ysrcA + tn * 4096;
      const char* p3 = ysrcB + tn * 4096;
      SB();
      const bool isw = t + 2 < 10;
      const char* ssrc = isw ? wbase + (size_t)(t + 2) * (64 * QD * 2) : vbase + (size_t)(t - 8) * (64 * DH * 2);
      const unsigned sgs = isw ? 8 * QD * 2 : 8 * DH * 2;
      const unsigned svo = isw ? wvo : vvo;
      const char* w1s = w1base + (size_t)tw * (64 * FF * 2);
      auto rdN = [&](auto N) {
        constexpr int n = decltype(N)::v, ks = n >> 3, g = n & 7;
        if constexpr (g < 6) return rdV(vb + ((2 * ks) * 6 + g) * 512);
        else if constexpr (g == 6) return rdH(hP + (4 * ks) * 512);
        else return rdH(hQ + (4 * ks) * 512);
      };
      sfor<32>([&](auto I) {
        constexpr int i = decltype(I)::v, ks = i >> 3, g = i & 7;
        if constexpr (g < 6) { mfma_o<AO(0, g)>(fr[i & 3], xc[0][ks]); mfma_o<AO(1, g)>(fr[i & 3], xc[1][ks]); }
        else if constexpr (g == 6) { mfma_qv(hacc[0], fr[i & 3], xc[2][ks]); mfma_qv(hacc[1], fr[i & 3], xc[3][ks]); }
        else mfma_qv(hacc[2], fr[i & 3], xc[2][ks]);
        if constexpr (i + 4 < 32) fr[i & 3] = rdN(IC<(i + 4 < 32 ? i + 4 : 0)>{});
        if constexpr (i < 16)
          gload16<(i & 3) * 1024>(xn[i >> 2][i & 3], (i >> 2) == 0 ? p0 : (i >> 2) == 1 ? p0 + 40960 : (i >> 2) == 2 ? p2 : p3);
        if constexpr (i >= 16 && i < 22) {
          constexpr int pc = i - 16;
          glds_s(ssrc + (size_t)(pc & 1) * sgs + (pc >> 1) * 128, svo, vdst + swr + (pc & 1) * 3072 + (pc >> 1) * 1024);
        }
        if constexpr (i >= 22 && i < 24) glds_s(w1s + (size_t)(i - 22) * (8 * FF * 2), w1vo, w1dst + swr / 3 + (i - 22) * 1024);
        SB();
      });
      srd = nxt(srd);
      swr = nxt(swr);
    };
    asm volatile("s_waitcnt vmcnt(0)" : XOPS(xa) :: "memory");
#pragma unroll 1
    for (int t = 0; t < 10; t += 2) { qstep(xa, xb, t); qstep(xb, xa, t + 1); }
    asm volatile("s_waitcnt vmcnt(0)\n\ts_barrier" : XOPS(xa) :: "memory");
  }
#pragma unroll
  for (int pc = 0; pc < 6; ++pc) pieceK(96, TB, pc);
  asm volatile("s_nop 15\n\ts_nop 7" ::: "memory");
  SB();
  {
    const int rbA = (q0 >> 5) + odd, rbB = (q0 >> 5) + 1 - odd, cP = 3 * hj + 2 * odd, cQ = 3 * hj + 1;
#pragma unroll
    for (int k = 0; k < 3; ++k) {
      const int rbk = k == 1 ? rbB : rbA, ck = k == 2 ? cQ : cP;
      float g[16];
#pragma unroll
      for (int q = 0; q < 4; ++q) {
        const u32x2 bw = *(const u32x2*)(b1 + ck * 32 + 8 * q + 4 * hi);
        const float bs[4] = {__uint_as_float(bw[0] << 16), __uint_as_float(bw[0] & 0xffff0000u), __uint_as_float(bw[1] << 16),
                             __uint_as_float(bw[1] & 0xffff0000u)};
#pragma unroll
        for (int e = 0; e < 4; ++e) g[4 * q + e] = gelu_tanh(rbf(rbf(hacc[k][4 * q + e]) + bs[e]));
      }
      u32x4* ho = (u32x4*)hfr + ((size_t)rbk * 96 + 2 * ck) * 64 + lane;
#pragma unroll
      for (int sidx = 0; sidx < 2; ++sidx)
        ho[sidx * 64] = u32x4{cvtpk(g[8 * sidx], g[8 * sidx + 1]), cvtpk(g[8 * sidx + 2], g[8 * sidx + 3]),
                              cvtpk(g[8 * sidx + 4], g[8 * sidx + 5]), cvtpk(g[8 * sidx + 6], g[8 * sidx + 7])};
    }
  }
  SB();
  bf16x8 qf[2][4];
  sfor<24>([&](auto I) {
    constexpr int n = decltype(I)::v, b = n / 12, f = n % 12, R = AO(b, f >> 1) + 8 * (f & 1);
    float t8[8];
    acc_rd8<R>(t8);
    unsigned w0 = cvtpk(t8[0] * QSCALE, t8[1] * QSCALE);
    unsigned w1 = cvtpk(t8[2] * QSCALE, t8[3] * QSCALE);
    unsigned w2 = cvtpk(t8[4] * QSCALE, t8[5] * QSCALE);
    unsigned w3 = cvtpk(t8[6] * QSCALE, t8[7] * QSCALE);
    auto r0 = __builtin_amdgcn_permlane32_swap(w0, w2, false, false);
    auto r1 = __builtin_amdgcn_permlane32_swap(w1, w3, false, false);
    if constexpr (f < 8) {
      acc_wr4<AQ(b, f)>(r0[0], r1[0], r0[1], r1[1]);
    } else {
      u32x4 w = {r0[0], r1[0], r0[1], r1[1]};
      qf[b][f - 8] = __builtin_bit_cast(bf16x8, w);
    }
  });
  zfr = bf16x8{};
  PIN(zfr);
  SB();
  asm volatile("s_nop 7");
  sfor<12>([&](auto I) { acc_zero16<16 * decltype(I)::v>(zfr); });

  float m_run[2], l_run[2] = {0.f, 0.f}, alpha[2];
  bool pend = false;
  f32x16 S[2][2], nm[2];
  u32x4 pw[2][2];
  unsigned kb[4], vb;
  nm[0] = f32x16{}; nm[1] = f32x16{};
  PIN(nm[0]); PIN(nm[1]);
#pragma unroll
  for (int i = 0; i < 4; ++i) { PIN(qf[0][i]); PIN(qf[1][i]); }
#pragma unroll
  for (int i = 0; i < 4; ++i) fr[i] = rdK(koff[i] + 2 * TB + 4096);
  SB();
  asm volatile("s_nop 3" ::: "memory");
  SB();

  sfor<12>([&](auto I) {
    constexpr int i = decltype(I)::v;
    if constexpr (i == 0) { mfma_first<AQ(0, 0)>(S[0][0], fr[0], nm[0]); mfma_first<AQ(1, 0)>(S[0][1], fr[0], nm[1]); }
    else if constexpr (i < 8) { mfma_qa<AQ(0, i)>(S[0][0], fr[i & 3]); mfma_qa<AQ(1, i)>(S[0][1], fr[i & 3]); }
    else { mfma_qv(S[0][0], fr[i & 3], qf[0][i - 8]); mfma_qv(S[0][1], fr[i & 3], qf[1][i - 8]); }
    if constexpr (i + 4 < 12) fr[i & 3] = rdK(koff[i & 3] + 2 * TB + ((i + 4) >> 2) * 8192 + 4096);
    SB();
  });
  asm volatile("s_nop 15\n\ts_nop 7" :: "v"(nm[0]), "v"(nm[1]) : "memory");
  SB();
#pragma unroll
  for (int b = 0; b < 2; ++b) {
    float mx = S[0][b][0];
#pragma unroll
    for (int r = 1; r < 16; ++r) mx = fmaxf(mx, S[0][b][r]);
    auto rr = __builtin_amdgcn_permlane32_swap(__float_as_uint(mx), __float_as_uint(mx), false, false);
    mx = fmaxf(__uint_as_float(rr[0]), __uint_as_float(rr[1]));
    m_run[b] = mx;
#pragma unroll
    for (int r = 0; r < 16; ++r) { nm[b][r] = -mx; S[0][b][r] -= mx; }
  }
#pragma unroll
  for (int j = 0; j < 4; ++j) kb[j] = koff[j];
  vb = voff;
#pragma unroll
  for (int i = 0; i < 4; ++i) fr[i] = rdK(kb[i]);

  auto phaseA = [&](auto XX, auto HH, const char* ssrc, const unsigned sdst) {
    constexpr int X = decltype(XX)::v, HB = decltype(HH)::v;
    sfor<12>([&](auto I) {
      constexpr int i = decltype(I)::v;
      auto ex = [&](auto N) {
        constexpr int n = decltype(N)::v;
        if constexpr (n < 32) {
          constexpr int b = (n & 15) >> 3, r = (n >> 4) * 8 + (n & 7);
          S[X][b][r] = __builtin_amdgcn_exp2f(S[X][b][r]);
        }
      };
      auto pk = [&](auto N) {
        constexpr int n = decltype(N)::v;
        if constexpr (n >= 0 && n < 16) {
          constexpr int b = (n >> 2) & 1, ks = n >> 3, w = n & 3;
          pw[b][ks][w] = cvtpk(S[X][b][8 * ks + 2 * w], S[X][b][8 * ks + 2 * w + 1]);
        }
      };
      if constexpr (i == 0) mfma_first<AQ(0, 0)>(S[X ^ 1][0], fr[0], nm[0]);
      else if constexpr (i < 8) mfma_qa<AQ(0, i)>(S[X ^ 1][0], fr[i & 3]);
      else mfma_qv(S[X ^ 1][0], fr[i & 3], qf[0][i - 8]);
      SB();
      ex(IC<3 * i>{}); ex(IC<3 * i + 1>{});
      pk(IC<2 * i - 8>{});
      PIN(S[X][0]); PIN(S[X][1]);
      SB();
      if constexpr (i == 0) mfma_first<AQ(1, 0)>(S[X ^ 1][1], fr[0], nm[1]);
      else if constexpr (i < 8) mfma_qa<AQ(1, i)>(S[X ^ 1][1], fr[i & 3]);
      else mfma_qv(S[X ^ 1][1], fr[i & 3], qf[1][i - 8]);
      SB();
      if constexpr (i + 4 < 12) fr[i & 3] = rdK(kb[i & 3] + ((i + 4) >> 2) * 8192 + HB * 4096);
      else fr[i & 3] = rdV(vb + ((4 * HB) * 6 + (i - 8)) * 512);
      ex(IC<3 * i + 2>{});
      pk(IC<2 * i - 7>{});
      if constexpr (i == 2) asm volatile("" :: "v"(nm[0]), "v"(nm[1]));
      if constexpr (i == 1 || i == 5 || i == 9) {
        constexpr int pc = (i - 1) / 4;
        if constexpr (HB == 0) glds_i<(pc & 1) * 1024 + (pc >> 1) * 8192>(ssrc, kpo[pc], sdst);
        else glds_i<(pc & 1) * 3072 + (pc >> 1) * 1024>(ssrc, vpo[pc], sdst);
      }
      PIN(S[X][0]); PIN(S[X][1]); PIN(pw[0][0]); PIN(pw[0][1]); PIN(pw[1][0]); PIN(pw[1][1]);
      SB();
    });
  };
  auto phaseB = [&](auto XX, auto HH, auto HK, const char* ssrc, const unsigned sdst) {
    constexpr int X = decltype(XX)::v, HB = decltype(HH)::v, HBKN = decltype(HK)::v;
    float ls[2][2], mx[2];
    auto fill = [&](auto G) {
      constexpr int g = decltype(G)::v;
      if constexpr (g < 16) {
        constexpr int b = g >> 3, r = (g & 7) * 2;
        if constexpr ((g & 7) == 0) { ls[b][0] = S[X][b][r]; ls[b][1] = S[X][b][r + 1]; }
        else { ls[b][0] += S[X][b][r]; ls[b][1] += S[X][b][r + 1]; }
        PIN(ls[b][0]); PIN(ls[b][1]);
      }
      if constexpr (g == 16) { l_run[0] += ls[0][0] + ls[0][1]; PIN(l_run[0]); }
      if constexpr (g == 17) { l_run[1] += ls[1][0] + ls[1][1]; PIN(l_run[1]); }
      if constexpr (g == 20) {
#pragma unroll
        for (int b = 0; b < 2; ++b) {
          auto rr = __builtin_amdgcn_permlane32_swap(__float_as_uint(mx[b]), __float_as_uint(mx[b]), false, false);
          mx[b] = max2f(__uint_as_float(rr[0]), __uint_as_float(rr[1]));
          PIN(mx[b]);
        }
      }
      if constexpr (g == 21) {
        if (__builtin_expect(__any(max2f(mx[0], mx[1]) > THR), 0)) {
          const float aone = hi == 0 ? 1.f : 0.f;
#pragma unroll
          for (int b = 0; b < 2; ++b) {
            const float dl = fmaxf(mx[b], 0.f);
            m_run[b] += dl;
            alpha[b] = __builtin_amdgcn_exp2f(-dl);
            bcast16(nm[b], aone, -m_run[b]);
#pragma unroll
            for (int r = 0; r < 16; ++r) S[X ^ 1][b][r] -= dl;
          }
          pend = true;
        }
      }
      if constexpr (g == 16 || g == 18 || g == 22) {
        constexpr int pc = g == 16 ? 3 : (g == 18 ? 4 : 5);
        if constexpr (HB == 0) glds_i<(pc & 1) * 1024 + (pc >> 1) * 8192>(ssrc, kpo[pc], sdst);
        else glds_i<(pc & 1) * 3072 + (pc >> 1) * 1024>(ssrc, vpo[pc], sdst);
      }
    };
    auto pvm = [&](auto G) {
      constexpr int g = decltype(G)::v, i = g >> 1, b = g & 1, k = (g - 4) >> 1;
      const bf16x8 pb = __builtin_bit_cast(bf16x8, pw[b][i / 6]);
      if constexpr (g >= 4 && g < 20) {
        if constexpr (k == 0) mfma_o_mx<AO(b, i % 6)>(fr[i & 3], pb, mx[b], S[X ^ 1][b][0], S[X ^ 1][b][1], S[X ^ 1][b][2]);
        else if constexpr (k < 7) mfma_o_mx<AO(b, i % 6)>(fr[i & 3], pb, mx[b], mx[b], S[X ^ 1][b][2 * k + 1], S[X ^ 1][b][2 * k + 2]);
        else mfma_o_mx<AO(b, i % 6)>(fr[i & 3], pb, mx[b], mx[b], S[X ^ 1][b][15], S[X ^ 1][b][15]);
      } else mfma_o<AO(b, i % 6)>(fr[i & 3], pb);
    };
    sfor<12>([&](auto I) {
      constexpr int i = decltype(I)::v;
      pvm(IC<2 * i>{});
      SB();
      fill(IC<2 * i>{});
      SB();
      pvm(IC<2 * i + 1>{});
      SB();
      if constexpr (i + 4 < 12) fr[i & 3] = rdV(vb + ((4 * HB + 2 * ((i + 4) / 6)) * 6 + ((i + 4) % 6)) * 512);
      else fr[i & 3] = rdK(kb[i - 8] + HBKN * 4096);
      fill(IC<2 * i + 1>{});
      SB();
    });
    if (__builtin_expect(pend, 0)) {
      asm volatile("s_nop 15\n\ts_nop 7" ::: "memory");
      l_run[0] *= alpha[0]; l_run[1] *= alpha[1];
      sfor<192>([&](auto I) { constexpr int n = decltype(I)::v; acc_scale<n>(alpha[n / 96]); });
      asm volatile("s_nop 3" ::: "memory");
      pend = false;
    }
  };

  asm volatile("s_waitcnt vmcnt(0)" ::: "memory");
  unsigned scur = 0;
#pragma unroll 1
  for (int t = 0; t < 32; ++t) {
    const unsigned snxt = nxt(scur), swr = nxt(snxt);
    asm volatile("s_waitcnt vmcnt(6)\n\ts_barrier" ::: "memory");
    int tk = t + 2 < 32 ? t + 2 : 31;
    const int tv = tk;
    if (tk == 31 && wid >= 2) tk = 30;
    const char* ks_ = kbase + (size_t)(tk * 64 + 32) * (DH * 2);
    const char* vs_ = vbase + (size_t)(tv * 64) * (DH * 2);
    SB();
    phaseA(IC<0>{}, IC<0>{}, ks_, kdst + swr);
    phaseB(IC<0>{}, IC<0>{}, IC<1>{}, ks_, kdst + swr);
    phaseA(IC<1>{}, IC<1>{}, vs_, vdst + swr);
#pragma unroll
    for (int j = 0; j < 4; ++j) kb[j] = koff[j] + snxt;
    phaseB(IC<1>{}, IC<1>{}, IC<0>{}, vs_, vdst + swr);
    vb = voff + snxt;
    scur = snxt;
  }
  asm volatile("s_waitcnt vmcnt(0)\n\ts_nop 15\n\ts_nop 7" ::: "memory");
  int tz = threadIdx.x;
  asm volatile("" : "+v"(tz));
  float rls[2];
#pragma unroll
  for (int b = 0; b < 2; ++b) {
    auto rr = __builtin_amdgcn_permlane32_swap(__float_as_uint(l_run[b]), __float_as_uint(l_run[b]), false, false);
    rls[b] = 1.0f / (__uint_as_float(rr[0]) + __uint_as_float(rr[1]));
  }
  u32x4* ofr = (u32x4*)O + ((size_t)(qb * 8 + (tz >> 6) * 2) * 384 + head * 12) * 64 + (tz & 63);
  sfor<24>([&](auto I) {
    constexpr int n = decltype(I)::v, b = n / 12, f = n % 12, R = AO(b, f >> 1) + 8 * (f & 1);
    const float rl = rls[b];
    float t8[8];
    acc_rd8<R>(t8);
    ofr[(b * 384 + f) * 64] = u32x4{cvtpk(t8[0] * rl, t8[1] * rl), cvtpk(t8[2] * rl, t8[3] * rl),
                                    cvtpk(t8[4] * rl, t8[5] * rl), cvtpk(t8[6] * rl, t8[7] * rl)};
  });
}

extern "C" void kernel_launch(void* const* d_in, const int* in_sizes, int n_in, void* d_out, int out_size,
                              void* d_ws, size_t ws_size, hipStream_t stream) {
  const u16* x = (const u16*)d_in[0];
  const u16* kc = (const u16*)d_in[1];
  const u16* vc = (const u16*)d_in[2];
  const u16* wq = (const u16*)d_in[3];
  const u16* wo = (const u16*)d_in[4];
  const u16* w1 = (const u16*)d_in[6];
  const u16* b1 = (const u16*)d_in[7];
  const u16* w2 = (const u16*)d_in[8];
  const u16* b2 = (const u16*)d_in[9];
  char* ws = (char*)d_ws;
  u16* xnp = (u16*)(ws);
  u16* y = (u16*)(ws + (4u << 20));
  u16* h = (u16*)(ws + (8u << 20));
  u16* O = (u16*)(ws + (16u << 20));
  float* part = (float*)(ws + (48u << 20));

  hipFuncSetAttribute((const void*)k_attn, hipFuncAttributeMaxDynamicSharedMemorySize, 6 * TB);
  hipFuncSetAttribute((const void*)k_gemm2, hipFuncAttributeMaxDynamicSharedMemorySize, 4 * 16384);

  k_ln<<<SEQ / 4, 256, 0, stream>>>(x, xnp, y);
  k_attn<<<256, 256, 6 * TB, stream>>>(xnp, wq, kc, vc, O, y, w1, b1, h);
  k_gemm2<<<200, 256, 4 * 16384, stream>>>(O, wo, h, w2, part);
  k_final<<<SEQ * DM / 1024, 256, 0, stream>>>(x, part, b2, (u16*)d_out);
}
```

```cpp
#include <hip/hip_runtime.h>
#include <hip/hip_bf16.h>
#include <stdint.h>

typedef __attribute__((ext_vector_type(8))) short bf16x8;
typedef __attribute__((ext_vector_type(8))) __bf16 bf16x8b;
typedef __attribute__((ext_vector_type(4))) short s16x4;
typedef __attribute__((ext_vector_type(16))) float f32x16;
typedef __attribute__((ext_vector_type(4))) float f32x4;
typedef __attribute__((ext_vector_type(2))) float f32x2;
typedef __attribute__((ext_vector_type(2))) __bf16 bf16x2b;
typedef __attribute__((ext_vector_type(4))) unsigned u32x4;
typedef __attribute__((ext_vector_type(2))) unsigned u32x2;
typedef __attribute__((address_space(3))) char* lds_ptr;
typedef unsigned short u16;

#define DI __device__ __forceinline__

constexpr int SEQ = 2048, DM = 640, NH = 32, DH = 192, FF = 1536, QD = NH * DH;

DI f32x16 MFMA(bf16x8 a, bf16x8 b, f32x16 c) {
  return __builtin_amdgcn_mfma_f32_32x32x16_bf16(__builtin_bit_cast(bf16x8b, a), __builtin_bit_cast(bf16x8b, b), c, 0, 0, 0);
}
DI unsigned cvtpk(float lo, float hi) {
  f32x2 v = {lo, hi};
  bf16x2b b = __builtin_convertvector(v, bf16x2b);
  return __builtin_bit_cast(unsigned, b);
}
DI float bf2f(u16 v) { return __uint_as_float(((unsigned)v) << 16); }
DI float rbf(float v) { return __uint_as_float(cvtpk(v, 0.f) << 16); }
DI s16x4 trread(lds_ptr p) {
  return __builtin_amdgcn_ds_read_tr16_b64_v4i16((__attribute__((address_space(3))) s16x4*)p);
}
DI bf16x8 ldsread(lds_ptr p) { return *(__attribute__((address_space(3))) bf16x8*)p; }
DI bf16x8 cat(s16x4 a, s16x4 b) { return __builtin_shufflevector(a, b, 0, 1, 2, 3, 4, 5, 6, 7); }
DI void glds16(const void* g, lds_ptr l) {
  __builtin_amdgcn_global_load_lds((const __attribute__((address_space(1))) unsigned*)g,
                                   (__attribute__((address_space(3))) unsigned*)l, 16, 0, 0);
}

DI float wave_sum(float v) {
#pragma unroll
  for (int o = 32; o > 0; o >>= 1) v += __shfl_xor(v, o, 64);
  return v;
}
DI void ln_row(float (&v)[10]) {
  float s = 0.f;
#pragma unroll
  for (int i = 0; i < 10; ++i) s += v[i];
  s = wave_sum(s);
  const float mean = s * (1.0f / DM);
  float q = 0.f;
#pragma unroll
  for (int i = 0; i < 10; ++i) { float d = v[i] - mean; q += d * d; }
  q = wave_sum(q);
  const float mb = rbf(mean), vb = rbf(q * (1.0f / DM));
  const float den = rbf(sqrtf(rbf(vb + 1e-5f)));
#pragma unroll
  for (int i = 0; i < 10; ++i) v[i] = rbf(rbf(v[i] - mb) / den);
}
__global__ __launch_bounds__(256) void k_ln(const u16* __restrict__ x, u16* __restrict__ xnp, u16* __restrict__ y) {
  const int row = blockIdx.x * 4 + (threadIdx.x >> 6), lane = threadIdx.x & 63;
  const unsigned* xr = (const unsigned*)(x + (size_t)row * DM);
  float v[10];
#pragma unroll
  for (int i = 0; i < 5; ++i) {
    unsigned w = xr[lane + 64 * i];
    v[2 * i] = __uint_as_float(w << 16);
    v[2 * i + 1] = __uint_as_float(w & 0xffff0000u);
  }
  ln_row(v);
  unsigned* xo = (unsigned*)xnp + (size_t)(row >> 5) * (40 * 64 * 4) + (row & 31) * 4;
#pragma unroll
  for (int i = 0; i < 5; ++i) {
    int e = lane + 64 * i;
    int ep = (e & ~6) | ((e & 2) << 1) | ((e & 4) >> 1);
    xo[(ep >> 3) * 256 + ((ep >> 2) & 1) * 128 + (ep & 3)] =
        (__float_as_uint(v[2 * i]) >> 16) | (__float_as_uint(v[2 * i + 1]) & 0xffff0000u);
  }
  ln_row(v);
  unsigned* yo = (unsigned*)y + (size_t)(row >> 5) * (40 * 64 * 4) + (row & 31) * 4;
#pragma unroll
  for (int i = 0; i < 5; ++i) {
    int e = lane + 64 * i;
    int ep = (e & ~6) | ((e & 2) << 1) | ((e & 4) >> 1);
    yo[(ep >> 3) * 256 + ((ep >> 2) & 1) * 128 + (ep & 3)] =
        (__float_as_uint(v[2 * i]) >> 16) | (__float_as_uint(v[2 * i + 1]) & 0xffff0000u);
  }
}

#define SB() __builtin_amdgcn_sched_barrier(0)
#define PIN(x) asm volatile("" : "+v"(x))
template <int I> struct IC { static constexpr int v = I; };
template <int N, class F> DI void sfor(F&& f) {
  if constexpr (N > 0) { sfor<N - 1>(f); f(IC<N - 1>{}); }
}
template <int IMM> DI void glds_i(const void* sbase, unsigned voff, unsigned ldsbase) {
  asm volatile("s_add_i32 m0, %2, %3\n\ts_nop 0\n\tglobal_load_lds_dwordx4 %0, %1"
               :: "v"(voff), "s"(sbase), "s"(ldsbase), "i"(IMM) : "memory", "scc");
}
template <int OFF> DI void gload16(bf16x8& d, const char* p) {
  asm volatile("global_load_dwordx4 %0, %1, off offset:%2" : "=&v"(d) : "v"(p), "i"(OFF) : "memory");
}
template <int OFF> DI void gload8(u32x2& d, const void* p) {
  asm volatile("global_load_dwordx2 %0, %1, off offset:%2" : "=&v"(d) : "v"(p), "i"(OFF) : "memory");
}
template <int N> DI void xwait2(bf16x8 (&x)[2][4]) {
  asm volatile("s_waitcnt vmcnt(%8)\n\ts_barrier" : "+v"(x[0][0]), "+v"(x[0][1]), "+v"(x[0][2]), "+v"(x[0][3]), "+v"(x[1][0]), "+v"(x[1][1]), "+v"(x[1][2]), "+v"(x[1][3]) : "i"(N) : "memory");
}
DI void xpin2(bf16x8 (&x)[2][4]) {
  asm volatile("" : "+v"(x[0][0]), "+v"(x[0][1]), "+v"(x[0][2]), "+v"(x[0][3]), "+v"(x[1][0]), "+v"(x[1][1]), "+v"(x[1][2]), "+v"(x[1][3]));
}

DI void wgemm24(const char* wbase, unsigned ldwb, const char* xp, unsigned xrbs, int ktiles, unsigned lds0,
                f32x16 (&acc)[2][4]) {
  constexpr int NB = 4;
  constexpr unsigned TBY = 4096 * NB;
  const int tid = threadIdx.x, lane = tid & 63, wid = __builtin_amdgcn_readfirstlane(tid >> 6);
  const int hi = lane >> 5;
  unsigned wpo[NB];
#pragma unroll
  for (int i = 0; i < NB; ++i) {
    const int sub = 2 * (wid * NB + i) + hi, kg = sub / NB, nb = sub % NB;
    wpo[i] = (unsigned)(8 * kg + ((lane >> 2) & 7)) * ldwb + nb * 64 + (lane & 3) * 16;
  }
  const unsigned wdst = lds0 + wid * NB * 1024;
  const unsigned voff = lds0 + (hi * 4 + ((lane >> 2) & 3)) * 64 + ((lane >> 4) & 1) * 32 + (lane & 3) * 8;
  auto rdW = [&](unsigned a) { return cat(trread((lds_ptr)(uintptr_t)a), trread((lds_ptr)(uintptr_t)(a + NB * 512))); };
#pragma unroll
  for (int r = 0; r < 2; ++r)
#pragma unroll
    for (int n = 0; n < NB; ++n) acc[r][n] = f32x16{};
  bf16x8 x0[2][4], x1[2][4], x2[2][4], x3[2][4], fr[4];
  const size_t wts = (size_t)64 * ldwb;
  auto issue = [&](bf16x8 (&x)[2][4], int t, auto SLOT) {
    const char* xq = xp + (size_t)t * 4096;
    sfor<8>([&](auto I) { constexpr int i = decltype(I)::v; gload16<(i & 3) * 1024>(x[i >> 2][i & 3], xq + (i >> 2) * xrbs); });
    sfor<NB>([&](auto I) { glds_i<decltype(SLOT)::v * TBY + decltype(I)::v * 1024>(wbase + (size_t)t * wts, wpo[decltype(I)::v], wdst); });
  };
  issue(x0, 0, IC<0>{});
  issue(x1, 1, IC<1>{});
  issue(x2, 2, IC<2>{});
  asm volatile("s_waitcnt vmcnt(24)\n\ts_barrier" ::: "memory");
#pragma unroll
  for (int i = 0; i < 4; ++i) fr[i] = rdW(voff + i * 512);
  auto step = [&](bf16x8 (&xc)[2][4], bf16x8 (&xn)[2][4], const int t, auto SLOT) {
    constexpr int sl = decltype(SLOT)::v, sw = (sl + 3) & 3;
    xwait2<12>(xc);
    const unsigned vb = voff + sl * TBY, vbn = voff + ((sl + 1) & 3) * TBY;
    const int tn = t + 3 < ktiles ? t + 3 : ktiles - 1;
    const char* xq = xp + (size_t)tn * 4096;
    const char* wq_ = wbase + (size_t)tn * wts;
    SB();
    sfor<16>([&](auto I) {
      constexpr int i = decltype(I)::v;
      acc[0][i & 3] = MFMA(fr[i & 3], xc[0][i >> 2], acc[0][i & 3]);
      acc[1][i & 3] = MFMA(fr[i & 3], xc[1][i >> 2], acc[1][i & 3]);
      SB();
      if constexpr (i + 4 < 16) fr[i & 3] = rdW(vb + ((2 * ((i + 4) >> 2)) * NB + (i & 3)) * 512);
      else fr[i & 3] = rdW(vbn + (i & 3) * 512);
      if constexpr (i < 8) gload16<(i & 3) * 1024>(xn[i >> 2][i & 3], xq + (i >> 2) * xrbs);
      if constexpr (i >= 8 && i < 12) glds_i<sw * TBY + (i - 8) * 1024>(wq_, wpo[i - 8], wdst);
      SB();
    });
  };
#pragma unroll 1
  for (int t = 0; t < ktiles; t += 4) {
    step(x0, x3, t, IC<0>{});
    step(x1, x0, t + 1, IC<1>{});
    step(x2, x1, t + 2, IC<2>{});
    step(x3, x2, t + 3, IC<3>{});
  }
  asm volatile("s_waitcnt vmcnt(0)" ::: "memory");
  xpin2(x0); xpin2(x1); xpin2(x2);
}
DI int crow(int r, int hi) { return (r & 3) + 8 * (r >> 2) + 4 * hi; }

DI float gelu_tanh(float v) {
  const float u = 0.7978845608028654f * (v + 0.044715f * v * v * v);
  const float e = __builtin_amdgcn_exp2f(2.8853900817779268f * u);
  return v - v * __builtin_amdgcn_rcpf(e + 1.f);
}

__global__ __launch_bounds__(256, 1) void k_gemm2(const u16* __restrict__ O, const u16* __restrict__ wo,
                                                  const u16* __restrict__ h, const u16* __restrict__ w2,
                                                  float* __restrict__ part) {
  extern __shared__ __attribute__((aligned(16))) char smem[];
  const int bid = blockIdx.x, x = bid & 7, r = bid >> 3;
  const int nt = r % 5, c = r < 20 ? (x >> 1) : 4, mt = r < 20 ? 4 * (x & 1) + r / 5 : x;
  const int lane = threadIdx.x & 63, wid = threadIdx.x >> 6;
  const int rb0 = mt * 8 + wid * 2;
  const char* W; const char* X; unsigned xrbs;
  if (c < 4) {
    W = (const char*)(wo + (size_t)c * 24 * 64 * DM + nt * 128);
    xrbs = 384 * 1024;
    X = (const char*)O + ((size_t)rb0 * 384 + c * 96) * 1024 + lane * 16;
  } else {
    W = (const char*)(w2 + nt * 128);
    xrbs = 96 * 1024;
    X = (const char*)h + (size_t)rb0 * 96 * 1024 + lane * 16;
  }
  f32x16 acc[2][4];
  wgemm24(W, DM * 2, X, xrbs, 24, (unsigned)(uintptr_t)(lds_ptr)smem, acc);
  f32x4* P = (f32x4*)part + (size_t)c * (SEQ * DM / 4);
#pragma unroll
  for (int b = 0; b < 2; ++b)
#pragma unroll
    for (int nb = 0; nb < 4; ++nb)
#pragma unroll
      for (int q = 0; q < 4; ++q)
        P[(((size_t)(rb0 + b) * 20 + nt * 4 + nb) * 4 + q) * 64 + lane] =
            f32x4{acc[b][nb][4 * q], acc[b][nb][4 * q + 1], acc[b][nb][4 * q + 2], acc[b][nb][4 * q + 3]};
}

__global__ __launch_bounds__(256) void k_final(const u16* __restrict__ x, const float* __restrict__ part,
                                               const u16* __restrict__ b2, u16* __restrict__ out) {
  const int idx = blockIdx.x * 256 + threadIdx.x;
  constexpr size_t PS = (size_t)SEQ * DM / 4;
  const f32x4* P = (const f32x4*)part + idx;
  const f32x4 p0 = P[0], p1 = P[PS], p2 = P[2 * PS], p3 = P[3 * PS], pm = P[4 * PS];
  const int lane = idx & 63, q = (idx >> 6) & 3, blk = idx >> 8, cb = blk % 20, rb = blk / 20;
  const int m = rb * 32 + (lane & 31), n = cb * 32 + 8 * q + 4 * (lane >> 5);
  const u32x2 xw = *(const u32x2*)(x + (size_t)m * DM + n);
  const u32x2 bw = *(const u32x2*)(b2 + n);
  float xo[4] = {__uint_as_float(xw[0] << 16), __uint_as_float(xw[0] & 0xffff0000u), __uint_as_float(xw[1] << 16),
                 __uint_as_float(xw[1] & 0xffff0000u)};
  float bo[4] = {__uint_as_float(bw[0] << 16), __uint_as_float(bw[0] & 0xffff0000u), __uint_as_float(bw[1] << 16),
                 __uint_as_float(bw[1] & 0xffff0000u)};
  float r[4];
#pragma unroll
  for (int i = 0; i < 4; ++i) {
    const float s = rbf((p0[i] + p1[i]) + (p2[i] + p3[i]));
    const float mm = rbf(rbf(pm[i]) + bo[i]);
    const float g = rbf(mm / (1.f + __expf(-mm)));
    r[i] = xo[i] + rbf(s * g);
  }
  u32x2 o = {cvtpk(r[0], r[1]), cvtpk(r[2], r[3])};
  *(u32x2*)(out + (size_t)m * DM + n) = o;
}

constexpr int TB = 24576;
constexpr float QSCALE = 0.07216878364870322f * 1.4426950408889634f;
constexpr float THR = 20.0f;

DI void glds_s(const void* sbase, unsigned voff, unsigned ldsaddr) {
  asm volatile("s_mov_b32 m0, %2\n\ts_nop 0\n\tglobal_load_lds_dwordx4 %0, %1"
               :: "v"(voff), "s"(sbase), "s"(ldsaddr) : "memory");
}
DI float max3f(float a, float b, float c) { float r; asm("v_max3_f32 %0, %1, %2, %3" : "=v"(r) : "v"(a), "v"(b), "v"(c)); return r; }
DI float max2f(float a, float b) { float r; asm("v_max_f32_e32 %0, %1, %2" : "=v"(r) : "v"(a), "v"(b)); return r; }
DI void bcast16(f32x16& d, float aone, float negm) {
  asm volatile("s_nop 1\n\tv_mfma_f32_32x32x2_f32 %0, %1, %2, 0" : "+v"(d) : "v"(aone), "v"(negm));
}
DI bf16x8 rdK(unsigned a) { return ldsread((lds_ptr)(uintptr_t)a); }
DI bf16x8 rdV(unsigned a) { return cat(trread((lds_ptr)(uintptr_t)a), trread((lds_ptr)(uintptr_t)(a + 3072))); }
DI unsigned nxt(unsigned s) { return s == 2 * TB ? 0 : s + TB; }
#define ACLOB "a0","a1","a2","a3","a4","a5","a6","a7","a8","a9","a10","a11","a12","a13","a14","a15","a16","a17","a18","a19","a20","a21","a22","a23","a24","a25","a26","a27","a28","a29","a30","a31","a32","a33","a34","a35","a36","a37","a38","a39","a40","a41","a42","a43","a44","a45","a46","a47","a48","a49","a50","a51","a52","a53","a54","a55","a56","a57","a58","a59","a60","a61","a62","a63","a64","a65","a66","a67","a68","a69","a70","a71","a72","a73","a74","a75","a76","a77","a78","a79","a80","a81","a82","a83","a84","a85","a86","a87","a88","a89","a90","a91","a92","a93","a94","a95","a96","a97","a98","a99","a100","a101","a102","a103","a104","a105","a106","a107","a108","a109","a110","a111","a112","a113","a114","a115","a116","a117","a118","a119","a120","a121","a122","a123","a124","a125","a126","a127","a128","a129","a130","a131","a132","a133","a134","a135","a136","a137","a138","a139","a140","a141","a142","a143","a144","a145","a146","a147","a148","a149","a150","a151","a152","a153","a154","a155","a156","a157","a158","a159","a160","a161","a162","a163","a164","a165","a166","a167","a168","a169","a170","a171","a172","a173","a174","a175","a176","a177","a178","a179","a180","a181","a182","a183","a184","a185","a186","a187","a188","a189","a190","a191","a192","a193","a194","a195","a196","a197","a198","a199","a200","a201","a202","a203","a204","a205","a206","a207","a208","a209","a210","a211","a212","a213","a214","a215","a216","a217","a218","a219","a220","a221","a222","a223","a224","a225","a226","a227","a228","a229","a230","a231","a232","a233","a234","a235","a236","a237","a238","a239","a240","a241","a242","a243","a244","a245","a246","a247","a248","a249","a250","a251","a252","a253","a254","a255"
constexpr int AO(int b, int db) { return 16 * (6 * b + db); }
constexpr int AQ(int b, int i) { return 192 + 32 * b + 4 * i; }
template <int R> DI void mfma_o(const bf16x8& a, const bf16x8& b) {
  asm volatile("v_mfma_f32_32x32x16_bf16 a[%c2:%c3], %0, %1, a[%c2:%c3]" :: "v"(a), "v"(b), "i"(R), "i"(R + 15) : ACLOB);
}
template <int R> DI void mfma_o_mx(const bf16x8& a, const bf16x8& b, float& m, float x, float y, float z) {
  asm volatile("v_mfma_f32_32x32x16_bf16 a[%c6:%c7], %1, %2, a[%c6:%c7]\n\tv_max3_f32 %0, %3, %4, %5"
               : "=v"(m) : "v"(a), "v"(b), "v"(x), "v"(y), "v"(z), "i"(R), "i"(R + 15) : ACLOB);
}
template <int R> DI void mfma_first(f32x16& d, const bf16x8& a, const f32x16& c) {
  asm volatile("v_mfma_f32_32x32x16_bf16 %0, %1, a[%c3:%c4], %2" : "=&v"(d) : "v"(a), "v"(c), "i"(R), "i"(R + 3) : ACLOB);
}
template <int R> DI void mfma_qa(f32x16& d, const bf16x8& a) {
  asm volatile("v_mfma_f32_32x32x16_bf16 %0, %1, a[%c2:%c3], %0" : "+v"(d) : "v"(a), "i"(R), "i"(R + 3) : ACLOB);
}
DI void mfma_qv(f32x16& d, const bf16x8& a, const bf16x8& b) {
  asm volatile("v_mfma_f32_32x32x16_bf16 %0, %1, %2, %0" : "+v"(d) : "v"(a), "v"(b));
}
template <int R> DI float acc_rd() { float r; asm volatile("v_accvgpr_read_b32 %0, a[%c1]" : "=v"(r) : "i"(R)); return r; }
template <int R> DI void acc_wr(unsigned v) { asm volatile("v_accvgpr_write_b32 a[%c1], %0" :: "v"(v), "i"(R) : ACLOB); }
template <int R> DI void acc_zero() { asm volatile("v_accvgpr_write_b32 a[%c0], 0" :: "i"(R) : ACLOB); }
template <int R> DI void acc_zero16(const bf16x8& z) {
  asm volatile("v_mfma_f32_32x32x16_bf16 a[%c1:%c2], %0, %0, 0" :: "v"(z), "i"(R), "i"(R + 15) : ACLOB);
}
template <int R> DI void acc_rd8(float (&r)[8]) {
  asm volatile("v_accvgpr_read_b32 %0, a[%c8]\n\tv_accvgpr_read_b32 %1, a[%c9]\n\tv_accvgpr_read_b32 %2, a[%c10]\n\tv_accvgpr_read_b32 %3, a[%c11]\n\t"
               "v_accvgpr_read_b32 %4, a[%c12]\n\tv_accvgpr_read_b32 %5, a[%c13]\n\tv_accvgpr_read_b32 %6, a[%c14]\n\tv_accvgpr_read_b32 %7, a[%c15]"
               : "=v"(r[0]), "=v"(r[1]), "=v"(r[2]), "=v"(r[3]), "=v"(r[4]), "=v"(r[5]), "=v"(r[6]), "=v"(r[7])
               : "i"(R), "i"(R + 1), "i"(R + 2), "i"(R + 3), "i"(R + 4), "i"(R + 5), "i"(R + 6), "i"(R + 7));
}
template <int R> DI void acc_wr4(unsigned a, unsigned b, unsigned c, unsigned d) {
  asm volatile("v_accvgpr_write_b32 a[%c4], %0\n\tv_accvgpr_write_b32 a[%c5], %1\n\tv_accvgpr_write_b32 a[%c6], %2\n\tv_accvgpr_write_b32 a[%c7], %3"
               :: "v"(a), "v"(b), "v"(c), "v"(d), "i"(R), "i"(R + 1), "i"(R + 2), "i"(R + 3) : ACLOB);
}
template <int R> DI void acc_scale(float f) {
  float t;
  asm volatile("v_accvgpr_read_b32 %0, a[%c2]\n\tv_mul_f32 %0, %1, %0\n\tv_accvgpr_write_b32 a[%c2], %0" : "=&v"(t) : "v"(f), "i"(R) : ACLOB);
}

__global__ __launch_bounds__(256, 1) void k_attn(const u16* __restrict__ xnp, const u16* __restrict__ wq,
                                                 const u16* __restrict__ Kc, const u16* __restrict__ Vc,
                                                 u16* __restrict__ O, const u16* __restrict__ yfr,
                                                 const u16* __restrict__ w1, const u16* __restrict__ b1,
                                                 u16* __restrict__ hfr) {
  extern __shared__ __attribute__((aligned(16))) char smem[];
  const unsigned lds0 = (unsigned)(uintptr_t)(lds_ptr)smem;
  const int tid = threadIdx.x, lane = tid & 63, wid = __builtin_amdgcn_readfirstlane(tid >> 6);
  const int r32 = lane & 31, hi = lane >> 5;
  const int bid = blockIdx.x, xcd = bid & 7, idx = bid >> 3;
  const int head = xcd * 4 + (idx >> 3), qb = idx & 7;
  const int q0 = qb * 256 + wid * 64;

  const int krl = lane >> 3;
  const unsigned kvo0 = (unsigned)(krl * DH * 2 + (((lane & 7) ^ (krl >> 1)) << 4));
  const unsigned kvo1 = (unsigned)(krl * DH * 2 + (((lane & 7) ^ (4 + (krl >> 1))) << 4));
  const unsigned vvo = (unsigned)(((lane >> 2) & 7) * DH * 2 + (lane >> 5) * 64 + (lane & 3) * 16);
  const unsigned wvo = (unsigned)(((lane >> 2) & 7) * QD * 2 + (lane >> 5) * 64 + (lane & 3) * 16);
  const char* kbase = (const char*)(Kc + (size_t)head * SEQ * DH) + (size_t)(16 * wid) * DH * 2;
  const char* vbase = (const char*)(Vc + (size_t)head * SEQ * DH) + (size_t)(16 * wid) * DH * 2;
  const char* wbase = (const char*)(wq + head * DH) + (size_t)(16 * wid) * QD * 2;
  const unsigned kdst = lds0 + wid * 2048, vdst = lds0 + 3 * TB + wid * 6144;
  auto pieceK = [&](int key0, unsigned slot, int pc) {
    glds_s(kbase + (size_t)key0 * (DH * 2) + (pc & 1) * (8 * DH * 2) + (pc >> 1) * 128, (pc & 1) ? kvo1 : kvo0,
           kdst + slot + (pc & 1) * 1024 + (pc >> 1) * 8192);
  };
  auto pieceV = [&](int key0, unsigned slot, int pc) {
    glds_s(vbase + (size_t)key0 * (DH * 2) + (pc & 1) * (8 * DH * 2) + (pc >> 1) * 128, vvo,
           vdst + slot + (pc & 1) * 3072 + (pc >> 1) * 1024);
  };
  auto pieceW = [&](int t, unsigned slot, int pc) {
    glds_s(wbase + (size_t)t * (64 * QD * 2) + (size_t)(pc & 1) * (8 * QD * 2) + (pc >> 1) * 128, wvo,
           vdst + slot + (pc & 1) * 3072 + (pc >> 1) * 1024);
  };
  unsigned kpo[6], vpo[6];
#pragma unroll
  for (int pc = 0; pc < 6; ++pc) {
    kpo[pc] = ((pc & 1) ? kvo1 : kvo0) + (pc & 1) * (8 * DH * 2) + (pc >> 1) * 128;
    vpo[pc] = vvo + (pc & 1) * (8 * DH * 2) + (pc >> 1) * 128;
  }
  unsigned koff[4];
#pragma unroll
  for (int j = 0; j < 4; ++j) koff[j] = lds0 + r32 * 128 + (((2 * j + hi) ^ ((r32 >> 1) & 7)) << 4);
  const unsigned voff = lds0 + 3 * TB + (hi * 4 + ((lane >> 2) & 3)) * 64 + ((lane >> 4) & 1) * 32 + (lane & 3) * 8;

  bf16x8 zfr = bf16x8{};
  PIN(zfr);
  asm volatile("s_nop 3");
  sfor<12>([&](auto I) { acc_zero16<16 * decltype(I)::v>(zfr); });
  bf16x8 fr[4];

  const int odd = head & 1, hj = head >> 1;
  const char* xsrc0 = (const char*)xnp + ((size_t)(q0 >> 5) * (40 * 64) + lane) * 16;
  const char* ysrcA = (const char*)yfr + ((size_t)((q0 >> 5) + odd) * (40 * 64) + lane) * 16;
  const char* ysrcB = (const char*)yfr + ((size_t)((q0 >> 5) + 1 - odd) * (40 * 64) + lane) * 16;
  const char* w1base = (const char*)(w1 + (3 * hj + odd) * 32) + (size_t)(16 * wid) * (FF * 2);
  const unsigned w1vo = (unsigned)(((lane >> 2) & 7) * FF * 2 + (lane >> 5) * 64 + (lane & 3) * 16);
  const unsigned w1dst = lds0 + TB + wid * 2048;
  const unsigned w1rd = lds0 + TB + (hi * 4 + ((lane >> 2) & 3)) * 64 + ((lane >> 4) & 1) * 32 + (lane & 3) * 8;
  const unsigned w1P = w1rd + (odd ? 512 : 0), w1Q = w1rd + (odd ? 0 : 512);
  auto rdH = [&](unsigned a) { return cat(trread((lds_ptr)(uintptr_t)a), trread((lds_ptr)(uintptr_t)(a + 1024))); };
  u32x2 bwv[2][4];
  {
    const u16* bp = b1 + (3 * hj + 2 * odd) * 32 + 4 * hi;
    const u16* bq = b1 + (3 * hj + 1) * 32 + 4 * hi;
    sfor<8>([&](auto I) {
      constexpr int i = decltype(I)::v;
      gload8<(i & 3) * 16>(bwv[i >> 2][i & 3], i < 4 ? bp : bq);
    });
  }
  bf16x8 xa[4][4], xb[4][4];
  f32x16 hacc[3];
  hacc[0] = f32x16{}; hacc[1] = f32x16{}; hacc[2] = f32x16{};
  PIN(hacc[0]); PIN(hacc[1]); PIN(hacc[2]);
#pragma unroll
  for (int pc = 0; pc < 6; ++pc) pieceW(0, 2 * TB, pc);
#pragma unroll
  for (int pc = 0; pc < 6; ++pc) pieceW(1, 0, pc);
#pragma unroll
  for (int t = 0; t < 2; ++t)
#pragma unroll
    for (int g = 0; g < 2; ++g)
      glds_s(w1base + (size_t)t * (64 * FF * 2) + (size_t)g * (8 * FF * 2), w1vo, w1dst + (t == 0 ? 2 : 0) * 8192 + g * 1024);
  sfor<16>([&](auto I) {
    constexpr int i = decltype(I)::v;
    gload16<(i & 3) * 1024>(xa[i >> 2][i & 3], (i >> 2) == 0 ? xsrc0 : (i >> 2) == 1 ? xsrc0 + 40960 : (i >> 2) == 2 ? ysrcA : ysrcB);
  });
  if (wid >= 2) {
#pragma unroll
    for (int pc = 0; pc < 6; ++pc) pieceK(-32, 2 * TB, pc);
  }
#pragma unroll
  for (int pc = 0; pc < 6; ++pc) pieceK(32, 0, pc);
#define XOPS(x) "+v"(x[0][0]), "+v"(x[0][1]), "+v"(x[0][2]), "+v"(x[0][3]), "+v"(x[1][0]), "+v"(x[1][1]), "+v"(x[1][2]), "+v"(x[1][3]), \
                "+v"(x[2][0]), "+v"(x[2][1]), "+v"(x[2][2]), "+v"(x[2][3]), "+v"(x[3][0]), "+v"(x[3][1]), "+v"(x[3][2]), "+v"(x[3][3])
  {
    unsigned srd = 2 * TB, swr = TB;
    auto qstep = [&](bf16x8 (&xc)[4][4], bf16x8 (&xn)[4][4], const int t) {
      asm volatile("s_waitcnt vmcnt(8)\n\ts_barrier" : XOPS(xc) :: "memory");
      const unsigned vb = voff + srd;
      const unsigned hP = w1P + srd / 3, hQ = w1Q + srd / 3;
#pragma unroll
      for (int i = 0; i < 4; ++i) fr[i] = rdV(vb + i * 512);
      const int tn = t + 1 < 10 ? t + 1 : 9, tw = t + 2 < 10 ? t + 2 : 9;
      const char* p0 = xsrc0 + tn * 4096;
      const char* p2 = ysrcA + tn * 4096;
      const char* p3 = ysrcB + tn * 4096;
      SB();
      const bool isw = t + 2 < 10;
      const char* ssrc = isw ? wbase + (size_t)(t + 2) * (64 * QD * 2) : vbase + (size_t)(t - 8) * (64 * DH * 2);
      const unsigned sgs = isw ? 8 * QD * 2 : 8 * DH * 2;
      const unsigned svo = isw ? wvo : vvo;
      const char* w1s = w1base + (size_t)tw * (64 * FF * 2);
      auto rdN = [&](auto N) {
        constexpr int n = decltype(N)::v, ks = n >> 3, g = n & 7;
        if constexpr (g < 6) return rdV(vb + ((2 * ks) * 6 + g) * 512);
        else if constexpr (g == 6) return rdH(hP + (4 * ks) * 512);
        else return rdH(hQ + (4 * ks) * 512);
      };
      sfor<32>([&](auto I) {
        constexpr int i = decltype(I)::v, ks = i >> 3, g = i & 7;
        if constexpr (g < 6) { mfma_o<AO(0, g)>(fr[i & 3], xc[0][ks]); mfma_o<AO(1, g)>(fr[i & 3], xc[1][ks]); }
        else if constexpr (g == 6) { mfma_qv(hacc[0], fr[i & 3], xc[2][ks]); mfma_qv(hacc[1], fr[i & 3], xc[3][ks]); }
        else mfma_qv(hacc[2], fr[i & 3], xc[2][ks]);
        if constexpr (i + 4 < 32) fr[i & 3] = rdN(IC<(i + 4 < 32 ? i + 4 : 0)>{});
        if constexpr (i < 16)
          gload16<(i & 3) * 1024>(xn[i >> 2][i & 3], (i >> 2) == 0 ? p0 : (i >> 2) == 1 ? p0 + 40960 : (i >> 2) == 2 ? p2 : p3);
        if constexpr (i >= 16 && i < 22) {
          constexpr int pc = i - 16;
          glds_s(ssrc + (size_t)(pc & 1) * sgs + (pc >> 1) * 128, svo, vdst + swr + (pc & 1) * 3072 + (pc >> 1) * 1024);
        }
        if constexpr (i >= 22 && i < 24) glds_s(w1s + (size_t)(i - 22) * (8 * FF * 2), w1vo, w1dst + swr / 3 + (i - 22) * 1024);
        SB();
      });
      srd = nxt(srd);
      swr = nxt(swr);
    };
    asm volatile("s_waitcnt vmcnt(0)" : XOPS(xa) :: "memory");
    asm volatile("" : "+v"(bwv[0][0]), "+v"(bwv[0][1]), "+v"(bwv[0][2]), "+v"(bwv[0][3]), "+v"(bwv[1][0]), "+v"(bwv[1][1]), "+v"(bwv[1][2]), "+v"(bwv[1][3]));
#pragma unroll 1
    for (int t = 0; t < 10; t += 2) { qstep(xa, xb, t); qstep(xb, xa, t + 1); }
    asm volatile("s_waitcnt vmcnt(0)\n\ts_barrier" : XOPS(xa) :: "memory");
  }
#pragma unroll
  for (int pc = 0; pc < 6; ++pc) pieceK(96, TB, pc);
  asm volatile("s_nop 15\n\ts_nop 7" ::: "memory");
  SB();
  {
    const int rbA = (q0 >> 5) + odd, rbB = (q0 >> 5) + 1 - odd, cP = 3 * hj + 2 * odd, cQ = 3 * hj + 1;
#pragma unroll
    for (int k = 0; k < 3; ++k) {
      const int rbk = k == 1 ? rbB : rbA, ck = k == 2 ? cQ : cP;
      float g[16];
#pragma unroll
      for (int q = 0; q < 4; ++q) {
        const u32x2 bw = bwv[k == 2 ? 1 : 0][q];
        const float bs[4] = {__uint_as_float(bw[0] << 16), __uint_as_float(bw[0] & 0xffff0000u), __uint_as_float(bw[1] << 16),
                             __uint_as_float(bw[1] & 0xffff0000u)};
#pragma unroll
        for (int e = 0; e < 4; ++e) g[4 * q + e] = gelu_tanh(rbf(rbf(hacc[k][4 * q + e]) + bs[e]));
      }
      u32x4* ho = (u32x4*)hfr + ((size_t)rbk * 96 + 2 * ck) * 64 + lane;
#pragma unroll
      for (int sidx = 0; sidx < 2; ++sidx)
        ho[sidx * 64] = u32x4{cvtpk(g[8 * sidx], g[8 * sidx + 1]), cvtpk(g[8 * sidx + 2], g[8 * sidx + 3]),
                              cvtpk(g[8 * sidx + 4], g[8 * sidx + 5]), cvtpk(g[8 * sidx + 6], g[8 * sidx + 7])};
    }
  }
  SB();
  bf16x8 qf[2][4];
  sfor<24>([&](auto I) {
    constexpr int n = decltype(I)::v, b = n / 12, f = n % 12, R = AO(b, f >> 1) + 8 * (f & 1);
    float t8[8];
    acc_rd8<R>(t8);
    unsigned w0 = cvtpk(t8[0] * QSCALE, t8[1] * QSCALE);
    unsigned w1 = cvtpk(t8[2] * QSCALE, t8[3] * QSCALE);
    unsigned w2 = cvtpk(t8[4] * QSCALE, t8[5] * QSCALE);
    unsigned w3 = cvtpk(t8[6] * QSCALE, t8[7] * QSCALE);
    auto r0 = __builtin_amdgcn_permlane32_swap(w0, w2, false, false);
    auto r1 = __builtin_amdgcn_permlane32_swap(w1, w3, false, false);
    if constexpr (f < 8) {
      acc_wr4<AQ(b, f)>(r0[0], r1[0], r0[1], r1[1]);
    } else {
      u32x4 w = {r0[0], r1[0], r0[1], r1[1]};
      qf[b][f - 8] = __builtin_bit_cast(bf16x8, w);
    }
  });
  zfr = bf16x8{};
  PIN(zfr);
  SB();
  asm volatile("s_nop 7");
  sfor<12>([&](auto I) { acc_zero16<16 * decltype(I)::v>(zfr); });

  float m_run[2], l_run[2] = {0.f, 0.f}, alpha[2];
  bool pend = false;
  f32x16 S[2][2], nm[2];
  u32x4 pw[2][2];
  unsigned kb[4], vb;
  nm[0] = f32x16{}; nm[1] = f32x16{};
  PIN(nm[0]); PIN(nm[1]);
#pragma unroll
  for (int i = 0; i < 4; ++i) { PIN(qf[0][i]); PIN(qf[1][i]); }
#pragma unroll
  for (int i = 0; i < 4; ++i) fr[i] = rdK(koff[i] + 2 * TB + 4096);
  SB();
  asm volatile("s_nop 3" ::: "memory");
  SB();

  sfor<12>([&](auto I) {
    constexpr int i = decltype(I)::v;
    if constexpr (i == 0) { mfma_first<AQ(0, 0)>(S[0][0], fr[0], nm[0]); mfma_first<AQ(1, 0)>(S[0][1], fr[0], nm[1]); }
    else if constexpr (i < 8) { mfma_qa<AQ(0, i)>(S[0][0], fr[i & 3]); mfma_qa<AQ(1, i)>(S[0][1], fr[i & 3]); }
    else { mfma_qv(S[0][0], fr[i & 3], qf[0][i - 8]); mfma_qv(S[0][1], fr[i & 3], qf[1][i - 8]); }
    if constexpr (i + 4 < 12) fr[i & 3] = rdK(koff[i & 3] + 2 * TB + ((i + 4) >> 2) * 8192 + 4096);
    SB();
  });
  asm volatile("s_nop 15\n\ts_nop 7" :: "v"(nm[0]), "v"(nm[1]) : "memory");
  SB();
#pragma unroll
  for (int b = 0; b < 2; ++b) {
    float mx = S[0][b][0];
#pragma unroll
    for (int r = 1; r < 16; ++r) mx = fmaxf(mx, S[0][b][r]);
    auto rr = __builtin_amdgcn_permlane32_swap(__float_as_uint(mx), __float_as_uint(mx), false, false);
    mx = fmaxf(__uint_as_float(rr[0]), __uint_as_float(rr[1]));
    m_run[b] = mx;
#pragma unroll
    for (int r = 0; r < 16; ++r) { nm[b][r] = -mx; S[0][b][r] -= mx; }
  }
#pragma unroll
  for (int j = 0; j < 4; ++j) kb[j] = koff[j];
  vb = voff;
#pragma unroll
  for (int i = 0; i < 4; ++i) fr[i] = rdK(kb[i]);

  auto phaseA = [&](auto XX, auto HH, const char* ssrc, const unsigned sdst) {
    constexpr int X = decltype(XX)::v, HB = decltype(HH)::v;
    sfor<12>([&](auto I) {
      constexpr int i = decltype(I)::v;
      auto ex = [&](auto N) {
        constexpr int n = decltype(N)::v;
        if constexpr (n < 32) {
          constexpr int b = (n & 15) >> 3, r = (n >> 4) * 8 + (n & 7);
          S[X][b][r] = __builtin_amdgcn_exp2f(S[X][b][r]);
        }
      };
      auto pk = [&](auto N) {
        constexpr int n = decltype(N)::v;
        if constexpr (n >= 0 && n < 16) {
          constexpr int b = (n >> 2) & 1, ks = n >> 3, w = n & 3;
          pw[b][ks][w] = cvtpk(S[X][b][8 * ks + 2 * w], S[X][b][8 * ks + 2 * w + 1]);
        }
      };
      if constexpr (i == 0) mfma_first<AQ(0, 0)>(S[X ^ 1][0], fr[0], nm[0]);
      else if constexpr (i < 8) mfma_qa<AQ(0, i)>(S[X ^ 1][0], fr[i & 3]);
      else mfma_qv(S[X ^ 1][0], fr[i & 3], qf[0][i - 8]);
      SB();
      ex(IC<3 * i>{}); ex(IC<3 * i + 1>{});
      pk(IC<2 * i - 8>{});
      PIN(S[X][0]); PIN(S[X][1]);
      SB();
      if constexpr (i == 0) mfma_first<AQ(1, 0)>(S[X ^ 1][1], fr[0], nm[1]);
      else if constexpr (i < 8) mfma_qa<AQ(1, i)>(S[X ^ 1][1], fr[i & 3]);
      else mfma_qv(S[X ^ 1][1], fr[i & 3], qf[1][i - 8]);
      SB();
      if constexpr (i + 4 < 12) fr[i & 3] = rdK(kb[i & 3] + ((i + 4) >> 2) * 8192 + HB * 4096);
      else fr[i & 3] = rdV(vb + ((4 * HB) * 6 + (i - 8)) * 512);
      ex(IC<3 * i + 2>{});
      pk(IC<2 * i - 7>{});
      if constexpr (i == 2) asm volatile("" :: "v"(nm[0]), "v"(nm[1]));
      if constexpr (i == 1 || i == 5 || i == 9) {
        constexpr int pc = (i - 1) / 4;
        if constexpr (HB == 0) glds_i<(pc & 1) * 1024 + (pc >> 1) * 8192>(ssrc, kpo[pc], sdst);
        else glds_i<(pc & 1) * 3072 + (pc >> 1) * 1024>(ssrc, vpo[pc], sdst);
      }
      PIN(S[X][0]); PIN(S[X][1]); PIN(pw[0][0]); PIN(pw[0][1]); PIN(pw[1][0]); PIN(pw[1][1]);
      SB();
    });
  };
  auto phaseB = [&](auto XX, auto HH, auto HK, const char* ssrc, const unsigned sdst) {
    constexpr int X = decltype(XX)::v, HB = decltype(HH)::v, HBKN = decltype(HK)::v;
    float ls[2][2], mx[2];
    auto fill = [&](auto G) {
      constexpr int g = decltype(G)::v;
      if constexpr (g < 16) {
        constexpr int b = g >> 3, r = (g & 7) * 2;
        if constexpr ((g & 7) == 0) { ls[b][0] = S[X][b][r]; ls[b][1] = S[X][b][r + 1]; }
        else { ls[b][0] += S[X][b][r]; ls[b][1] += S[X][b][r + 1]; }
        PIN(ls[b][0]); PIN(ls[b][1]);
      }
      if constexpr (g == 16) { l_run[0] += ls[0][0] + ls[0][1]; PIN(l_run[0]); }
      if constexpr (g == 17) { l_run[1] += ls[1][0] + ls[1][1]; PIN(l_run[1]); }
      if constexpr (g == 20) {
#pragma unroll
        for (int b = 0; b < 2; ++b) {
          auto rr = __builtin_amdgcn_permlane32_swap(__float_as_uint(mx[b]), __float_as_uint(mx[b]), false, false);
          mx[b] = max2f(__uint_as_float(rr[0]), __uint_as_float(rr[1]));
          PIN(mx[b]);
        }
      }
      if constexpr (g == 21) {
        if (__builtin_expect(__any(max2f(mx[0], mx[1]) > THR), 0)) {
          const float aone = hi == 0 ? 1.f : 0.f;
#pragma unroll
          for (int b = 0; b < 2; ++b) {
            const float dl = fmaxf(mx[b], 0.f);
            m_run[b] += dl;
            alpha[b] = __builtin_amdgcn_exp2f(-dl);
            bcast16(nm[b], aone, -m_run[b]);
#pragma unroll
            for (int r = 0; r < 16; ++r) S[X ^ 1][b][r] -= dl;
          }
          pend = true;
        }
      }
      if constexpr (g == 16 || g == 18 || g == 22) {
        constexpr int pc = g == 16 ? 3 : (g == 18 ? 4 : 5);
        if constexpr (HB == 0) glds_i<(pc & 1) * 1024 + (pc >> 1) * 8192>(ssrc, kpo[pc], sdst);
        else glds_i<(pc & 1) * 3072 + (pc >> 1) * 1024>(ssrc, vpo[pc], sdst);
      }
    };
    auto pvm = [&](auto G) {
      constexpr int g = decltype(G)::v, i = g >> 1, b = g & 1, k = (g - 4) >> 1;
      const bf16x8 pb = __builtin_bit_cast(bf16x8, pw[b][i / 6]);
      if constexpr (g >= 4 && g < 20) {
        if constexpr (k == 0) mfma_o_mx<AO(b, i % 6)>(fr[i & 3], pb, mx[b], S[X ^ 1][b][0], S[X ^ 1][b][1], S[X ^ 1][b][2]);
        else if constexpr (k < 7) mfma_o_mx<AO(b, i % 6)>(fr[i & 3], pb, mx[b], mx[b], S[X ^ 1][b][2 * k + 1], S[X ^ 1][b][2 * k + 2]);
        else mfma_o_mx<AO(b, i % 6)>(fr[i & 3], pb, mx[b], mx[b], S[X ^ 1][b][15], S[X ^ 1][b][15]);
      } else mfma_o<AO(b, i % 6)>(fr[i & 3], pb);
    };
    sfor<12>([&](auto I) {
      constexpr int i = decltype(I)::v;
      pvm(IC<2 * i>{});
      SB();
      fill(IC<2 * i>{});
      SB();
      pvm(IC<2 * i + 1>{});
      SB();
      if constexpr (i + 4 < 12) fr[i & 3] = rdV(vb + ((4 * HB + 2 * ((i + 4) / 6)) * 6 + ((i + 4) % 6)) * 512);
      else fr[i & 3] = rdK(kb[i - 8] + HBKN * 4096);
      fill(IC<2 * i + 1>{});
      SB();
    });
    if (__builtin_expect(pend, 0)) {
      asm volatile("s_nop 15\n\ts_nop 7" ::: "memory");
      l_run[0] *= alpha[0]; l_run[1] *= alpha[1];
      sfor<192>([&](auto I) { constexpr int n = decltype(I)::v; acc_scale<n>(alpha[n / 96]); });
      asm volatile("s_nop 3" ::: "memory");
      pend = false;
    }
  };

  asm volatile("s_waitcnt vmcnt(0)" ::: "memory");
  unsigned scur = 0;
#pragma unroll 1
  for (int t = 0; t < 32; ++t) {
    const unsigned snxt = nxt(scur), swr = nxt(snxt);
    asm volatile("s_waitcnt vmcnt(6)\n\ts_barrier" ::: "memory");
    int tk = t + 2 < 32 ? t + 2 : 31;
    const int tv = tk;
    if (tk == 31 && wid >= 2) tk = 30;
    const char* ks_ = kbase + (size_t)(tk * 64 + 32) * (DH * 2);
    const char* vs_ = vbase + (size_t)(tv * 64) * (DH * 2);
    SB();
    phaseA(IC<0>{}, IC<0>{}, ks_, kdst + swr);
    phaseB(IC<0>{}, IC<0>{}, IC<1>{}, ks_, kdst + swr);
    phaseA(IC<1>{}, IC<1>{}, vs_, vdst + swr);
#pragma unroll
    for (int j = 0; j < 4; ++j) kb[j] = koff[j] + snxt;
    phaseB(IC<1>{}, IC<1>{}, IC<0>{}, vs_, vdst + swr);
    vb = voff + snxt;
    scur = snxt;
  }
  asm volatile("s_waitcnt vmcnt(0)\n\ts_nop 15\n\ts_nop 7" ::: "memory");
  int tz = threadIdx.x;
  asm volatile("" : "+v"(tz));
  float rls[2];
#pragma unroll
  for (int b = 0; b < 2; ++b) {
    auto rr = __builtin_amdgcn_permlane32_swap(__float_as_uint(l_run[b]), __float_as_uint(l_run[b]), false, false);
    rls[b] = 1.0f / (__uint_as_float(rr[0]) + __uint_as_float(rr[1]));
  }
  u32x4* ofr = (u32x4*)O + ((size_t)(qb * 8 + (tz >> 6) * 2) * 384 + head * 12) * 64 + (tz & 63);
  sfor<24>([&](auto I) {
    constexpr int n = decltype(I)::v, b = n / 12, f = n % 12, R = AO(b, f >> 1) + 8 * (f & 1);
    const float rl = rls[b];
    float t8[8];
    acc_rd8<R>(t8);
    ofr[(b * 384 + f) * 64] = u32x4{cvtpk(t8[0] * rl, t8[1] * rl), cvtpk(t8[2] * rl, t8[3] * rl),
                                    cvtpk(t8[4] * rl, t8[5] * rl), cvtpk(t8[6] * rl, t8[7] * rl)};
  });
}

extern "C" void kernel_launch(void* const* d_in, const int* in_sizes, int n_in, void* d_out, int out_size,
                              void* d_ws, size_t ws_size, hipStream_t stream) {
  const u16* x = (const u16*)d_in[0];
  const u16* kc = (const u16*)d_in[1];
  const u16* vc = (const u16*)d_in[2];
  const u16* wq = (const u16*)d_in[3];
  const u16* wo = (const u16*)d_in[4];
  const u16* w1 = (const u16*)d_in[6];
  const u16* b1 = (const u16*)d_in[7];
  const u16* w2 = (const u16*)d_in[8];
  const u16* b2 = (const u16*)d_in[9];
  char* ws = (char*)d_ws;
  u16* xnp = (u16*)(ws);
  u16* y = (u16*)(ws + (4u << 20));
  u16* h = (u16*)(ws + (8u << 20));
  u16* O = (u16*)(ws + (16u << 20));
  float* part = (float*)(ws + (48u << 20));

  hipFuncSetAttribute((const void*)k_attn, hipFuncAttributeMaxDynamicSharedMemorySize, 6 * TB);
  hipFuncSetAttribute((const void*)k_gemm2, hipFuncAttributeMaxDynamicSharedMemorySize, 4 * 16384);

  k_ln<<<SEQ / 4, 256, 0, stream>>>(x, xnp, y);
  k_attn<<<256, 256, 6 * TB, stream>>>(xnp, wq, kc, vc, O, y, w1, b1, h);
  k_gemm2<<<200, 256, 4 * 16384, stream>>>(O, wo, h, w2, part);
  k_final<<<SEQ * DM / 1024, 256, 0, stream>>>(x, part, b2, (u16*)d_out);
}
```

```cpp
#include <hip/hip_runtime.h>
#include <hip/hip_bf16.h>
#include <stdint.h>

typedef __attribute__((ext_vector_type(8))) short bf16x8;
typedef __attribute__((ext_vector_type(8))) __bf16 bf16x8b;
typedef __attribute__((ext_vector_type(4))) short s16x4;
typedef __attribute__((ext_vector_type(16))) float f32x16;
typedef __attribute__((ext_vector_type(4))) float f32x4;
typedef __attribute__((ext_vector_type(2))) float f32x2;
typedef __attribute__((ext_vector_type(2))) __bf16 bf16x2b;
typedef __attribute__((ext_vector_type(4))) unsigned u32x4;
typedef __attribute__((ext_vector_type(2))) unsigned u32x2;
typedef __attribute__((address_space(3))) char* lds_ptr;
typedef unsigned short u16;

#define DI __device__ __forceinline__

constexpr int SEQ = 2048, DM = 640, NH = 32, DH = 192, FF = 1536, QD = NH * DH;

DI f32x16 MFMA(bf16x8 a, bf16x8 b, f32x16 c) {
  return __builtin_amdgcn_mfma_f32_32x32x16_bf16(__builtin_bit_cast(bf16x8b, a), __builtin_bit_cast(bf16x8b, b), c, 0, 0, 0);
}
DI unsigned cvtpk(float lo, float hi) {
  f32x2 v = {lo, hi};
  bf16x2b b = __builtin_convertvector(v, bf16x2b);
  return __builtin_bit_cast(unsigned, b);
}
DI float bf2f(u16 v) { return __uint_as_float(((unsigned)v) << 16); }
DI float rbf(float v) { return __uint_as_float(cvtpk(v, 0.f) << 16); }
DI s16x4 trread(lds_ptr p) {
  return __builtin_amdgcn_ds_read_tr16_b64_v4i16((__attribute__((address_space(3))) s16x4*)p);
}
DI bf16x8 ldsread(lds_ptr p) { return *(__attribute__((address_space(3))) bf16x8*)p; }
DI bf16x8 cat(s16x4 a, s16x4 b) { return __builtin_shufflevector(a, b, 0, 1, 2, 3, 4, 5, 6, 7); }
DI void glds16(const void* g, lds_ptr l) {
  __builtin_amdgcn_global_load_lds((const __attribute__((address_space(1))) unsigned*)g,
                                   (__attribute__((address_space(3))) unsigned*)l, 16, 0, 0);
}

DI float wave_sum(float v) {
#pragma unroll
  for (int o = 32; o > 0; o >>= 1) v += __shfl_xor(v, o, 64);
  return v;
}
DI void ln_row(float (&v)[10]) {
  float s = 0.f;
#pragma unroll
  for (int i = 0; i < 10; ++i) s += v[i];
  s = wave_sum(s);
  const float mean = s * (1.0f / DM);
  float q = 0.f;
#pragma unroll
  for (int i = 0; i < 10; ++i) { float d = v[i] - mean; q += d * d; }
  q = wave_sum(q);
  const float mb = rbf(mean), vb = rbf(q * (1.0f / DM));
  const float den = rbf(sqrtf(rbf(vb + 1e-5f)));
#pragma unroll
  for (int i = 0; i < 10; ++i) v[i] = rbf(rbf(v[i] - mb) / den);
}
__global__ __launch_bounds__(256) void k_ln(const u16* __restrict__ x, u16* __restrict__ xnp, u16* __restrict__ y,
                                            const u16* __restrict__ wq, const u16* __restrict__ w1) {
  const int row = blockIdx.x * 4 + (threadIdx.x >> 6), lane = threadIdx.x & 63;
  u32x4 pf[5];
  {
    const int g = blockIdx.x * 256 + threadIdx.x;
#pragma unroll
    for (int i = 0; i < 4; ++i) {
      const int e = g + i * 131072;
      pf[i] = e < DM * QD / 8 ? ((const u32x4*)wq)[e] : u32x4{};
    }
    pf[4] = g < DM * FF / 8 ? ((const u32x4*)w1)[g] : u32x4{};
  }
  const unsigned* xr = (const unsigned*)(x + (size_t)row * DM);
  float v[10];
#pragma unroll
  for (int i = 0; i < 5; ++i) {
    unsigned w = xr[lane + 64 * i];
    v[2 * i] = __uint_as_float(w << 16);
    v[2 * i + 1] = __uint_as_float(w & 0xffff0000u);
  }
  ln_row(v);
  unsigned* xo = (unsigned*)xnp + (size_t)(row >> 5) * (40 * 64 * 4) + (row & 31) * 4;
#pragma unroll
  for (int i = 0; i < 5; ++i) {
    int e = lane + 64 * i;
    int ep = (e & ~6) | ((e & 2) << 1) | ((e & 4) >> 1);
    xo[(ep >> 3) * 256 + ((ep >> 2) & 1) * 128 + (ep & 3)] =
        (__float_as_uint(v[2 * i]) >> 16) | (__float_as_uint(v[2 * i + 1]) & 0xffff0000u);
  }
  ln_row(v);
  unsigned* yo = (unsigned*)y + (size_t)(row >> 5) * (40 * 64 * 4) + (row & 31) * 4;
#pragma unroll
  for (int i = 0; i < 5; ++i) {
    int e = lane + 64 * i;
    int ep = (e & ~6) | ((e & 2) << 1) | ((e & 4) >> 1);
    yo[(ep >> 3) * 256 + ((ep >> 2) & 1) * 128 + (ep & 3)] =
        (__float_as_uint(v[2 * i]) >> 16) | (__float_as_uint(v[2 * i + 1]) & 0xffff0000u);
  }
  asm volatile("" :: "v"(pf[0]), "v"(pf[1]), "v"(pf[2]), "v"(pf[3]), "v"(pf[4]));
}

#define SB() __builtin_amdgcn_sched_barrier(0)
#define PIN(x) asm volatile("" : "+v"(x))
template <int I> struct IC { static constexpr int v = I; };
template <int N, class F> DI void sfor(F&& f) {
  if constexpr (N > 0) { sfor<N - 1>(f); f(IC<N - 1>{}); }
}
template <int IMM> DI void glds_i(const void* sbase, unsigned voff, unsigned ldsbase) {
  asm volatile("s_add_i32 m0, %2, %3\n\ts_nop 0\n\tglobal_load_lds_dwordx4 %0, %1"
               :: "v"(voff), "s"(sbase), "s"(ldsbase), "i"(IMM) : "memory", "scc");
}
template <int OFF> DI void gload16(bf16x8& d, const char* p) {
  asm volatile("global_load_dwordx4 %0, %1, off offset:%2" : "=&v"(d) : "v"(p), "i"(OFF) : "memory");
}
template <int OFF> DI void gload8(u32x2& d, const void* p) {
  asm volatile("global_load_dwordx2 %0, %1, off offset:%2" : "=&v"(d) : "v"(p), "i"(OFF) : "memory");
}
template <int N> DI void xwait2(bf16x8 (&x)[2][4]) {
  asm volatile("s_waitcnt vmcnt(%8)\n\ts_barrier" : "+v"(x[0][0]), "+v"(x[0][1]), "+v"(x[0][2]), "+v"(x[0][3]), "+v"(x[1][0]), "+v"(x[1][1]), "+v"(x[1][2]), "+v"(x[1][3]) : "i"(N) : "memory");
}
DI void xpin2(bf16x8 (&x)[2][4]) {
  asm volatile("" : "+v"(x[0][0]), "+v"(x[0][1]), "+v"(x[0][2]), "+v"(x[0][3]), "+v"(x[1][0]), "+v"(x[1][1]), "+v"(x[1][2]), "+v"(x[1][3]));
}

DI void wgemm24(const char* wbase, unsigned ldwb, const char* xp, unsigned xrbs, int ktiles, unsigned lds0,
                f32x16 (&acc)[2][4]) {
  constexpr int NB = 4;
  constexpr unsigned TBY = 4096 * NB;
  const int tid = threadIdx.x, lane = tid & 63, wid = __builtin_amdgcn_readfirstlane(tid >> 6);
  const int hi = lane >> 5;
  unsigned wpo[NB];
#pragma unroll
  for (int i = 0; i < NB; ++i) {
    const int sub = 2 * (wid * NB + i) + hi, kg = sub / NB, nb = sub % NB;
    wpo[i] = (unsigned)(8 * kg + ((lane >> 2) & 7)) * ldwb + nb * 64 + (lane & 3) * 16;
  }
  const unsigned wdst = lds0 + wid * NB * 1024;
  const unsigned voff = lds0 + (hi * 4 + ((lane >> 2) & 3)) * 64 + ((lane >> 4) & 1) * 32 + (lane & 3) * 8;
  auto rdW = [&](unsigned a) { return cat(trread((lds_ptr)(uintptr_t)a), trread((lds_ptr)(uintptr_t)(a + NB * 512))); };
#pragma unroll
  for (int r = 0; r < 2; ++r)
#pragma unroll
    for (int n = 0; n < NB; ++n) acc[r][n] = f32x16{};
  bf16x8 x0[2][4], x1[2][4], x2[2][4], x3[2][4], fr[4];
  const size_t wts = (size_t)64 * ldwb;
  auto issue = [&](bf16x8 (&x)[2][4], int t, auto SLOT) {
    const char* xq = xp + (size_t)t * 4096;
    sfor<8>([&](auto I) { constexpr int i = decltype(I)::v; gload16<(i & 3) * 1024>(x[i >> 2][i & 3], xq + (i >> 2) * xrbs); });
    sfor<NB>([&](auto I) { glds_i<decltype(SLOT)::v * TBY + decltype(I)::v * 1024>(wbase + (size_t)t * wts, wpo[decltype(I)::v], wdst); });
  };
  issue(x0, 0, IC<0>{});
  issue(x1, 1, IC<1>{});
  issue(x2, 2, IC<2>{});
  asm volatile("s_waitcnt vmcnt(24)\n\ts_barrier" ::: "memory");
#pragma unroll
  for (int i = 0; i < 4; ++i) fr[i] = rdW(voff + i * 512);
  auto step = [&](bf16x8 (&xc)[2][4], bf16x8 (&xn)[2][4], const int t, auto SLOT) {
    constexpr int sl = decltype(SLOT)::v, sw = (sl + 3) & 3;
    xwait2<12>(xc);
    const unsigned vb = voff + sl * TBY, vbn = voff + ((sl + 1) & 3) * TBY;
    const int tn = t + 3 < ktiles ? t + 3 : ktiles - 1;
    const char* xq = xp + (size_t)tn * 4096;
    const char* wq_ = wbase + (size_t)tn * wts;
    SB();
    sfor<16>([&](auto I) {
      constexpr int i = decltype(I)::v;
      acc[0][i & 3] = MFMA(fr[i & 3], xc[0][i >> 2], acc[0][i & 3]);
      acc[1][i & 3] = MFMA(fr[i & 3], xc[1][i >> 2], acc[1][i & 3]);
      SB();
      if constexpr (i + 4 < 16) fr[i & 3] = rdW(vb + ((2 * ((i + 4) >> 2)) * NB + (i & 3)) * 512);
      else fr[i & 3] = rdW(vbn + (i & 3) * 512);
      if constexpr (i < 8) gload16<(i & 3) * 1024>(xn[i >> 2][i & 3], xq + (i >> 2) * xrbs);
      if constexpr (i >= 8 && i < 12) glds_i<sw * TBY + (i - 8) * 1024>(wq_, wpo[i - 8], wdst);
      SB();
    });
  };
#pragma unroll 1
  for (int t = 0; t < ktiles; t += 4) {
    step(x0, x3, t, IC<0>{});
    step(x1, x0, t + 1, IC<1>{});
    step(x2, x1, t + 2, IC<2>{});
    step(x3, x2, t + 3, IC<3>{});
  }
  asm volatile("s_waitcnt vmcnt(0)" ::: "memory");
  xpin2(x0); xpin2(x1); xpin2(x2);
}
DI int crow(int r, int hi) { return (r & 3) + 8 * (r >> 2) + 4 * hi; }

DI float gelu_tanh(float v) {
  const float u = 0.7978845608028654f * (v + 0.044715f * v * v * v);
  const float e = __builtin_amdgcn_exp2f(2.8853900817779268f * u);
  return v - v * __builtin_amdgcn_rcpf(e + 1.f);
}

__global__ __launch_bounds__(256, 1) void k_gemm2(const u16* __restrict__ O, const u16* __restrict__ wo,
                                                  const u16* __restrict__ h, const u16* __restrict__ w2,
                                                  float* __restrict__ part) {
  extern __shared__ __attribute__((aligned(16))) char smem[];
  const int bid = blockIdx.x, x = bid & 7, r = bid >> 3;
  const int nt = r % 5, c = r < 20 ? (x >> 1) : 4, mt = r < 20 ? 4 * (x & 1) + r / 5 : x;
  const int lane = threadIdx.x & 63, wid = threadIdx.x >> 6;
  const int rb0 = mt * 8 + wid * 2;
  const char* W; const char* X; unsigned xrbs;
  if (c < 4) {
    W = (const char*)(wo + (size_t)c * 24 * 64 * DM + nt * 128);
    xrbs = 384 * 1024;
    X = (const char*)O + ((size_t)rb0 * 384 + c * 96) * 1024 + lane * 16;
  } else {
    W = (const char*)(w2 + nt * 128);
    xrbs = 96 * 1024;
    X = (const char*)h + (size_t)rb0 * 96 * 1024 + lane * 16;
  }
  f32x16 acc[2][4];
  wgemm24(W, DM * 2, X, xrbs, 24, (unsigned)(uintptr_t)(lds_ptr)smem, acc);
  f32x4* P = (f32x4*)part + (size_t)c * (SEQ * DM / 4);
#pragma unroll
  for (int b = 0; b < 2; ++b)
#pragma unroll
    for (int nb = 0; nb < 4; ++nb)
#pragma unroll
      for (int q = 0; q < 4; ++q)
        P[(((size_t)(rb0 + b) * 20 + nt * 4 + nb) * 4 + q) * 64 + lane] =
            f32x4{acc[b][nb][4 * q], acc[b][nb][4 * q + 1], acc[b][nb][4 * q + 2], acc[b][nb][4 * q + 3]};
}

__global__ __launch_bounds__(256) void k_final(const u16* __restrict__ x, const float* __restrict__ part,
                                               const u16* __restrict__ b2, u16* __restrict__ out) {
  const int idx = blockIdx.x * 256 + threadIdx.x;
  constexpr size_t PS = (size_t)SEQ * DM / 4;
  const f32x4* P = (const f32x4*)part + idx;
  const f32x4 p0 = P[0], p1 = P[PS], p2 = P[2 * PS], p3 = P[3 * PS], pm = P[4 * PS];
  const int lane = idx & 63, q = (idx >> 6) & 3, blk = idx >> 8, cb = blk % 20, rb = blk / 20;
  const int m = rb * 32 + (lane & 31), n = cb * 32 + 8 * q + 4 * (lane >> 5);
  const u32x2 xw = *(const u32x2*)(x + (size_t)m * DM + n);
  const u32x2 bw = *(const u32x2*)(b2 + n);
  float xo[4] = {__uint_as_float(xw[0] << 16), __uint_as_float(xw[0] & 0xffff0000u), __uint_as_float(xw[1] << 16),
                 __uint_as_float(xw[1] & 0xffff0000u)};
  float bo[4] = {__uint_as_float(bw[0] << 16), __uint_as_float(bw[0] & 0xffff0000u), __uint_as_float(bw[1] << 16),
                 __uint_as_float(bw[1] & 0xffff0000u)};
  float r[4];
#pragma unroll
  for (int i = 0; i < 4; ++i) {
    const float s = rbf((p0[i] + p1[i]) + (p2[i] + p3[i]));
    const float mm = rbf(rbf(pm[i]) + bo[i]);
    const float g = rbf(mm / (1.f + __expf(-mm)));
    r[i] = xo[i] + rbf(s * g);
  }
  u32x2 o = {cvtpk(r[0], r[1]), cvtpk(r[2], r[3])};
  *(u32x2*)(out + (size_t)m * DM + n) = o;
}

constexpr int TB = 24576;
constexpr float QSCALE = 0.07216878364870322f * 1.4426950408889634f;
constexpr float THR = 20.0f;

DI void glds_s(const void* sbase, unsigned voff, unsigned ldsaddr) {
  asm volatile("s_mov_b32 m0, %2\n\ts_nop 0\n\tglobal_load_lds_dwordx4 %0, %1"
               :: "v"(voff), "s"(sbase), "s"(ldsaddr) : "memory");
}
DI float max3f(float a, float b, float c) { float r; asm("v_max3_f32 %0, %1, %2, %3" : "=v"(r) : "v"(a), "v"(b), "v"(c)); return r; }
DI float max2f(float a, float b) { float r; asm("v_max_f32_e32 %0, %1, %2" : "=v"(r) : "v"(a), "v"(b)); return r; }
DI void bcast16(f32x16& d, float aone, float negm) {
  asm volatile("s_nop 1\n\tv_mfma_f32_32x32x2_f32 %0, %1, %2, 0" : "+v"(d) : "v"(aone), "v"(negm));
}
DI bf16x8 rdK(unsigned a) { return ldsread((lds_ptr)(uintptr_t)a); }
DI bf16x8 rdV(unsigned a) { return cat(trread((lds_ptr)(uintptr_t)a), trread((lds_ptr)(uintptr_t)(a + 3072))); }
DI unsigned nxt(unsigned s) { return s == 2 * TB ? 0 : s + TB; }
#define ACLOB "a0","a1","a2","a3","a4","a5","a6","a7","a8","a9","a10","a11","a12","a13","a14","a15","a16","a17","a18","a19","a20","a21","a22","a23","a24","a25","a26","a27","a28","a29","a30","a31","a32","a33","a34","a35","a36","a37","a38","a39","a40","a41","a42","a43","a44","a45","a46","a47","a48","a49","a50","a51","a52","a53","a54","a55","a56","a57","a58","a59","a60","a61","a62","a63","a64","a65","a66","a67","a68","a69","a70","a71","a72","a73","a74","a75","a76","a77","a78","a79","a80","a81","a82","a83","a84","a85","a86","a87","a88","a89","a90","a91","a92","a93","a94","a95","a96","a97","a98","a99","a100","a101","a102","a103","a104","a105","a106","a107","a108","a109","a110","a111","a112","a113","a114","a115","a116","a117","a118","a119","a120","a121","a122","a123","a124","a125","a126","a127","a128","a129","a130","a131","a132","a133","a134","a135","a136","a137","a138","a139","a140","a141","a142","a143","a144","a145","a146","a147","a148","a149","a150","a151","a152","a153","a154","a155","a156","a157","a158","a159","a160","a161","a162","a163","a164","a165","a166","a167","a168","a169","a170","a171","a172","a173","a174","a175","a176","a177","a178","a179","a180","a181","a182","a183","a184","a185","a186","a187","a188","a189","a190","a191","a192","a193","a194","a195","a196","a197","a198","a199","a200","a201","a202","a203","a204","a205","a206","a207","a208","a209","a210","a211","a212","a213","a214","a215","a216","a217","a218","a219","a220","a221","a222","a223","a224","a225","a226","a227","a228","a229","a230","a231","a232","a233","a234","a235","a236","a237","a238","a239","a240","a241","a242","a243","a244","a245","a246","a247","a248","a249","a250","a251","a252","a253","a254","a255"
constexpr int AO(int b, int db) { return 16 * (6 * b + db); }
constexpr int AQ(int b, int i) { return 192 + 32 * b + 4 * i; }
template <int R> DI void mfma_o(const bf16x8& a, const bf16x8& b) {
  asm volatile("v_mfma_f32_32x32x16_bf16 a[%c2:%c3], %0, %1, a[%c2:%c3]" :: "v"(a), "v"(b), "i"(R), "i"(R + 15) : ACLOB);
}
template <int R> DI void mfma_o_mx(const bf16x8& a, const bf16x8& b, float& m, float x, float y, float z) {
  asm volatile("v_mfma_f32_32x32x16_bf16 a[%c6:%c7], %1, %2, a[%c6:%c7]\n\tv_max3_f32 %0, %3, %4, %5"
               : "=v"(m) : "v"(a), "v"(b), "v"(x), "v"(y), "v"(z), "i"(R), "i"(R + 15) : ACLOB);
}
template <int R> DI void mfma_first(f32x16& d, const bf16x8& a, const f32x16& c) {
  asm volatile("v_mfma_f32_32x32x16_bf16 %0, %1, a[%c3:%c4], %2" : "=&v"(d) : "v"(a), "v"(c), "i"(R), "i"(R + 3) : ACLOB);
}
template <int R> DI void mfma_qa(f32x16& d, const bf16x8& a) {
  asm volatile("v_mfma_f32_32x32x16_bf16 %0, %1, a[%c2:%c3], %0" : "+v"(d) : "v"(a), "i"(R), "i"(R + 3) : ACLOB);
}
DI void mfma_qv(f32x16& d, const bf16x8& a, const bf16x8& b) {
  asm volatile("v_mfma_f32_32x32x16_bf16 %0, %1, %2, %0" : "+v"(d) : "v"(a), "v"(b));
}
template <int R> DI float acc_rd() { float r; asm volatile("v_accvgpr_read_b32 %0, a[%c1]" : "=v"(r) : "i"(R)); return r; }
template <int R> DI void acc_wr(unsigned v) { asm volatile("v_accvgpr_write_b32 a[%c1], %0" :: "v"(v), "i"(R) : ACLOB); }
template <int R> DI void acc_zero() { asm volatile("v_accvgpr_write_b32 a[%c0], 0" :: "i"(R) : ACLOB); }
template <int R> DI void acc_zero16(const bf16x8& z) {
  asm volatile("v_mfma_f32_32x32x16_bf16 a[%c1:%c2], %0, %0, 0" :: "v"(z), "i"(R), "i"(R + 15) : ACLOB);
}
template <int R> DI void acc_rd8(float (&r)[8]) {
  asm volatile("v_accvgpr_read_b32 %0, a[%c8]\n\tv_accvgpr_read_b32 %1, a[%c9]\n\tv_accvgpr_read_b32 %2, a[%c10]\n\tv_accvgpr_read_b32 %3, a[%c11]\n\t"
               "v_accvgpr_read_b32 %4, a[%c12]\n\tv_accvgpr_read_b32 %5, a[%c13]\n\tv_accvgpr_read_b32 %6, a[%c14]\n\tv_accvgpr_read_b32 %7, a[%c15]"
               : "=v"(r[0]), "=v"(r[1]), "=v"(r[2]), "=v"(r[3]), "=v"(r[4]), "=v"(r[5]), "=v"(r[6]), "=v"(r[7])
               : "i"(R), "i"(R + 1), "i"(R + 2), "i"(R + 3), "i"(R + 4), "i"(R + 5), "i"(R + 6), "i"(R + 7));
}
template <int R> DI void acc_wr4(unsigned a, unsigned b, unsigned c, unsigned d) {
  asm volatile("v_accvgpr_write_b32 a[%c4], %0\n\tv_accvgpr_write_b32 a[%c5], %1\n\tv_accvgpr_write_b32 a[%c6], %2\n\tv_accvgpr_write_b32 a[%c7], %3"
               :: "v"(a), "v"(b), "v"(c), "v"(d), "i"(R), "i"(R + 1), "i"(R + 2), "i"(R + 3) : ACLOB);
}
template <int R> DI void acc_scale(float f) {
  float t;
  asm volatile("v_accvgpr_read_b32 %0, a[%c2]\n\tv_mul_f32 %0, %1, %0\n\tv_accvgpr_write_b32 a[%c2], %0" : "=&v"(t) : "v"(f), "i"(R) : ACLOB);
}

__global__ __launch_bounds__(256, 1) void k_attn(const u16* __restrict__ xnp, const u16* __restrict__ wq,
                                                 const u16* __restrict__ Kc, const u16* __restrict__ Vc,
                                                 u16* __restrict__ O, const u16* __restrict__ yfr,
                                                 const u16* __restrict__ w1, const u16* __restrict__ b1,
                                                 u16* __restrict__ hfr) {
  extern __shared__ __attribute__((aligned(16))) char smem[];
  const unsigned lds0 = (unsigned)(uintptr_t)(lds_ptr)smem;
  const int tid = threadIdx.x, lane = tid & 63, wid = __builtin_amdgcn_readfirstlane(tid >> 6);
  const int r32 = lane & 31, hi = lane >> 5;
  const int bid = blockIdx.x, xcd = bid & 7, idx = bid >> 3;
  const int head = xcd * 4 + (idx >> 3), qb = idx & 7;
  const int q0 = qb * 256 + wid * 64;

  const int krl = lane >> 3;
  const unsigned kvo0 = (unsigned)(krl * DH * 2 + (((lane & 7) ^ (krl >> 1)) << 4));
  const unsigned kvo1 = (unsigned)(krl * DH * 2 + (((lane & 7) ^ (4 + (krl >> 1))) << 4));
  const unsigned vvo = (unsigned)(((lane >> 2) & 7) * DH * 2 + (lane >> 5) * 64 + (lane & 3) * 16);
  const unsigned wvo = (unsigned)(((lane >> 2) & 7) * QD * 2 + (lane >> 5) * 64 + (lane & 3) * 16);
  const char* kbase = (const char*)(Kc + (size_t)head * SEQ * DH) + (size_t)(16 * wid) * DH * 2;
  const char* vbase = (const char*)(Vc + (size_t)head * SEQ * DH) + (size_t)(16 * wid) * DH * 2;
  const char* wbase = (const char*)(wq + head * DH) + (size_t)(16 * wid) * QD * 2;
  const unsigned kdst = lds0 + wid * 2048, vdst = lds0 + 3 * TB + wid * 6144;
  auto pieceK = [&](int key0, unsigned slot, int pc) {
    glds_s(kbase + (size_t)key0 * (DH * 2) + (pc & 1) * (8 * DH * 2) + (pc >> 1) * 128, (pc & 1) ? kvo1 : kvo0,
           kdst + slot + (pc & 1) * 1024 + (pc >> 1) * 8192);
  };
  auto pieceV = [&](int key0, unsigned slot, int pc) {
    glds_s(vbase + (size_t)key0 * (DH * 2) + (pc & 1) * (8 * DH * 2) + (pc >> 1) * 128, vvo,
           vdst + slot + (pc & 1) * 3072 + (pc >> 1) * 1024);
  };
  auto pieceW = [&](int t, unsigned slot, int pc) {
    glds_s(wbase + (size_t)t * (64 * QD * 2) + (size_t)(pc & 1) * (8 * QD * 2) + (pc >> 1) * 128, wvo,
           vdst + slot + (pc & 1) * 3072 + (pc >> 1) * 1024);
  };
  unsigned kpo[6], vpo[6];
#pragma unroll
  for (int pc = 0; pc < 6; ++pc) {
    kpo[pc] = ((pc & 1) ? kvo1 : kvo0) + (pc & 1) * (8 * DH * 2) + (pc >> 1) * 128;
    vpo[pc] = vvo + (pc & 1) * (8 * DH * 2) + (pc >> 1) * 128;
  }
  unsigned koff[4];
#pragma unroll
  for (int j = 0; j < 4; ++j) koff[j] = lds0 + r32 * 128 + (((2 * j + hi) ^ ((r32 >> 1) & 7)) << 4);
  const unsigned voff = lds0 + 3 * TB + (hi * 4 + ((lane >> 2) & 3)) * 64 + ((lane >> 4) & 1) * 32 + (lane & 3) * 8;

  bf16x8 zfr = bf16x8{};
  PIN(zfr);
  asm volatile("s_nop 3");
  sfor<12>([&](auto I) { acc_zero16<16 * decltype(I)::v>(zfr); });
  bf16x8 fr[4];

  const int odd = head & 1, hj = head >> 1;
  const char* xsrc0 = (const char*)xnp + ((size_t)(q0 >> 5) * (40 * 64) + lane) * 16;
  const char* ysrcA = (const char*)yfr + ((size_t)((q0 >> 5) + odd) * (40 * 64) + lane) * 16;
  const char* ysrcB = (const char*)yfr + ((size_t)((q0 >> 5) + 1 - odd) * (40 * 64) + lane) * 16;
  const char* w1base = (const char*)(w1 + (3 * hj + odd) * 32) + (size_t)(16 * wid) * (FF * 2);
  const unsigned w1vo = (unsigned)(((lane >> 2) & 7) * FF * 2 + (lane >> 5) * 64 + (lane & 3) * 16);
  const unsigned w1dst = lds0 + TB + wid * 2048;
  const unsigned w1rd = lds0 + TB + (hi * 4 + ((lane >> 2) & 3)) * 64 + ((lane >> 4) & 1) * 32 + (lane & 3) * 8;
  const unsigned w1P = w1rd + (odd ? 512 : 0), w1Q = w1rd + (odd ? 0 : 512);
  auto rdH = [&](unsigned a) { return cat(trread((lds_ptr)(uintptr_t)a), trread((lds_ptr)(uintptr_t)(a + 1024))); };
  u32x2 bwv[2][4];
  {
    const u16* bp = b1 + (3 * hj + 2 * odd) * 32 + 4 * hi;
    const u16* bq = b1 + (3 * hj + 1) * 32 + 4 * hi;
    sfor<8>([&](auto I) {
      constexpr int i = decltype(I)::v;
      gload8<(i & 3) * 16>(bwv[i >> 2][i & 3], i < 4 ? bp : bq);
    });
  }
  bf16x8 xa[4][4], xb[4][4];
  f32x16 hacc[3];
  hacc[0] = f32x16{}; hacc[1] = f32x16{}; hacc[2] = f32x16{};
  PIN(hacc[0]); PIN(hacc[1]); PIN(hacc[2]);
#pragma unroll
  for (int pc = 0; pc < 6; ++pc) pieceW(0, 2 * TB, pc);
#pragma unroll
  for (int pc = 0; pc < 6; ++pc) pieceW(1, 0, pc);
#pragma unroll
  for (int t = 0; t < 2; ++t)
#pragma unroll
    for (int g = 0; g < 2; ++g)
      glds_s(w1base + (size_t)t * (64 * FF * 2) + (size_t)g * (8 * FF * 2), w1vo, w1dst + (t == 0 ? 2 : 0) * 8192 + g * 1024);
  sfor<16>([&](auto I) {
    constexpr int i = decltype(I)::v;
    gload16<(i & 3) * 1024>(xa[i >> 2][i & 3], (i >> 2) == 0 ? xsrc0 : (i >> 2) == 1 ? xsrc0 + 40960 : (i >> 2) == 2 ? ysrcA : ysrcB);
  });
  if (wid >= 2) {
#pragma unroll
    for (int pc = 0; pc < 6; ++pc) pieceK(-32, 2 * TB, pc);
  }
#pragma unroll
  for (int pc = 0; pc < 6; ++pc) pieceK(32, 0, pc);
#define XOPS(x) "+v"(x[0][0]), "+v"(x[0][1]), "+v"(x[0][2]), "+v"(x[0][3]), "+v"(x[1][0]), "+v"(x[1][1]), "+v"(x[1][2]), "+v"(x[1][3]), \
                "+v"(x[2][0]), "+v"(x[2][1]), "+v"(x[2][2]), "+v"(x[2][3]), "+v"(x[3][0]), "+v"(x[3][1]), "+v"(x[3][2]), "+v"(x[3][3])
  {
    unsigned srd = 2 * TB, swr = TB;
    auto qstep = [&](bf16x8 (&xc)[4][4], bf16x8 (&xn)[4][4], const int t) {
      asm volatile("s_waitcnt vmcnt(8)\n\ts_barrier" : XOPS(xc) :: "memory");
      const unsigned vb = voff + srd;
      const unsigned hP = w1P + srd / 3, hQ = w1Q + srd / 3;
#pragma unroll
      for (int i = 0; i < 4; ++i) fr[i] = rdV(vb + i * 512);
      const int tn = t + 1 < 10 ? t + 1 : 9, tw = t + 2 < 10 ? t + 2 : 9;
      const char* p0 = xsrc0 + tn * 4096;
      const char* p2 = ysrcA + tn * 4096;
      const char* p3 = ysrcB + tn * 4096;
      SB();
      const bool isw = t + 2 < 10;
      const char* ssrc = isw ? wbase + (size_t)(t + 2) * (64 * QD * 2) : vbase + (size_t)(t - 8) * (64 * DH * 2);
      const unsigned sgs = isw ? 8 * QD * 2 : 8 * DH * 2;
      const unsigned svo = isw ? wvo : vvo;
      const char* w1s = w1base + (size_t)tw * (64 * FF * 2);
      auto rdN = [&](auto N) {
        constexpr int n = decltype(N)::v, ks = n >> 3, g = n & 7;
        if constexpr (g < 6) return rdV(vb + ((2 * ks) * 6 + g) * 512);
        else if constexpr (g == 6) return rdH(hP + (4 * ks) * 512);
        else return rdH(hQ + (4 * ks) * 512);
      };
      sfor<32>([&](auto I) {
        constexpr int i = decltype(I)::v, ks = i >> 3, g = i & 7;
        if constexpr (g < 6) { mfma_o<AO(0, g)>(fr[i & 3], xc[0][ks]); mfma_o<AO(1, g)>(fr[i & 3], xc[1][ks]); }
        else if constexpr (g == 6) { mfma_qv(hacc[0], fr[i & 3], xc[2][ks]); mfma_qv(hacc[1], fr[i & 3], xc[3][ks]); }
        else mfma_qv(hacc[2], fr[i & 3], xc[2][ks]);
        if constexpr (i + 4 < 32) fr[i & 3] = rdN(IC<(i + 4 < 32 ? i + 4 : 0)>{});
        if constexpr (i < 16)
          gload16<(i & 3) * 1024>(xn[i >> 2][i & 3], (i >> 2) == 0 ? p0 : (i >> 2) == 1 ? p0 + 40960 : (i >> 2) == 2 ? p2 : p3);
        if constexpr (i >= 16 && i < 22) {
          constexpr int pc = i - 16;
          glds_s(ssrc + (size_t)(pc & 1) * sgs + (pc >> 1) * 128, svo, vdst + swr + (pc & 1) * 3072 + (pc >> 1) * 1024);
        }
        if constexpr (i >= 22 && i < 24) glds_s(w1s + (size_t)(i - 22) * (8 * FF * 2), w1vo, w1dst + swr / 3 + (i - 22) * 1024);
        SB();
      });
      srd = nxt(srd);
      swr = nxt(swr);
    };
    asm volatile("s_waitcnt vmcnt(0)" : XOPS(xa) :: "memory");
    asm volatile("" : "+v"(bwv[0][0]), "+v"(bwv[0][1]), "+v"(bwv[0][2]), "+v"(bwv[0][3]), "+v"(bwv[1][0]), "+v"(bwv[1][1]), "+v"(bwv[1][2]), "+v"(bwv[1][3]));
#pragma unroll 1
    for (int t = 0; t < 10; t += 2) { qstep(xa, xb, t); qstep(xb, xa, t + 1); }
    asm volatile("s_waitcnt vmcnt(0)\n\ts_barrier" : XOPS(xa) :: "memory");
  }
#pragma unroll
  for (int pc = 0; pc < 6; ++pc) pieceK(96, TB, pc);
  asm volatile("s_nop 15\n\ts_nop 7" ::: "memory");
  SB();
  {
    const int rbA = (q0 >> 5) + odd, rbB = (q0 >> 5) + 1 - odd, cP = 3 * hj + 2 * odd, cQ = 3 * hj + 1;
#pragma unroll
    for (int k = 0; k < 3; ++k) {
      const int rbk = k == 1 ? rbB : rbA, ck = k == 2 ? cQ : cP;
      float g[16];
#pragma unroll
      for (int q = 0; q < 4; ++q) {
        const u32x2 bw = bwv[k == 2 ? 1 : 0][q];
        const float bs[4] = {__uint_as_float(bw[0] << 16), __uint_as_float(bw[0] & 0xffff0000u), __uint_as_float(bw[1] << 16),
                             __uint_as_float(bw[1] & 0xffff0000u)};
#pragma unroll
        for (int e = 0; e < 4; ++e) g[4 * q + e] = gelu_tanh(rbf(rbf(hacc[k][4 * q + e]) + bs[e]));
      }
      u32x4* ho = (u32x4*)hfr + ((size_t)rbk * 96 + 2 * ck) * 64 + lane;
#pragma unroll
      for (int sidx = 0; sidx < 2; ++sidx)
        ho[sidx * 64] = u32x4{cvtpk(g[8 * sidx], g[8 * sidx + 1]), cvtpk(g[8 * sidx + 2], g[8 * sidx + 3]),
                              cvtpk(g[8 * sidx + 4], g[8 * sidx + 5]), cvtpk(g[8 * sidx + 6], g[8 * sidx + 7])};
    }
  }
  SB();
  bf16x8 qf[2][4];
  sfor<24>([&](auto I) {
    constexpr int n = decltype(I)::v, b = n / 12, f = n % 12, R = AO(b, f >> 1) + 8 * (f & 1);
    float t8[8];
    acc_rd8<R>(t8);
    unsigned w0 = cvtpk(t8[0] * QSCALE, t8[1] * QSCALE);
    unsigned w1 = cvtpk(t8[2] * QSCALE, t8[3] * QSCALE);
    unsigned w2 = cvtpk(t8[4] * QSCALE, t8[5] * QSCALE);
    unsigned w3 = cvtpk(t8[6] * QSCALE, t8[7] * QSCALE);
    auto r0 = __builtin_amdgcn_permlane32_swap(w0, w2, false, false);
    auto r1 = __builtin_amdgcn_permlane32_swap(w1, w3, false, false);
    if constexpr (f < 8) {
      acc_wr4<AQ(b, f)>(r0[0], r1[0], r0[1], r1[1]);
    } else {
      u32x4 w = {r0[0], r1[0], r0[1], r1[1]};
      qf[b][f - 8] = __builtin_bit_cast(bf16x8, w);
    }
  });
  zfr = bf16x8{};
  PIN(zfr);
  SB();
  asm volatile("s_nop 7");
  sfor<12>([&](auto I) { acc_zero16<16 * decltype(I)::v>(zfr); });

  float m_run[2], l_run[2] = {0.f, 0.f}, alpha[2];
  bool pend = false;
  f32x16 S[2][2], nm[2];
  u32x4 pw[2][2];
  unsigned kb[4], vb;
  nm[0] = f32x16{}; nm[1] = f32x16{};
  PIN(nm[0]); PIN(nm[1]);
#pragma unroll
  for (int i = 0; i < 4; ++i) { PIN(qf[0][i]); PIN(qf[1][i]); }
#pragma unroll
  for (int i = 0; i < 4; ++i) fr[i] = rdK(koff[i] + 2 * TB + 4096);
  SB();
  asm volatile("s_nop 3" ::: "memory");
  SB();

  sfor<12>([&](auto I) {
    constexpr int i = decltype(I)::v;
    if constexpr (i == 0) { mfma_first<AQ(0, 0)>(S[0][0], fr[0], nm[0]); mfma_first<AQ(1, 0)>(S[0][1], fr[0], nm[1]); }
    else if constexpr (i < 8) { mfma_qa<AQ(0, i)>(S[0][0], fr[i & 3]); mfma_qa<AQ(1, i)>(S[0][1], fr[i & 3]); }
    else { mfma_qv(S[0][0], fr[i & 3], qf[0][i - 8]); mfma_qv(S[0][1], fr[i & 3], qf[1][i - 8]); }
    if constexpr (i + 4 < 12) fr[i & 3] = rdK(koff[i & 3] + 2 * TB + ((i + 4) >> 2) * 8192 + 4096);
    SB();
  });
  asm volatile("s_nop 15\n\ts_nop 7" :: "v"(nm[0]), "v"(nm[1]) : "memory");
  SB();
#pragma unroll
  for (int b = 0; b < 2; ++b) {
    float mx = S[0][b][0];
#pragma unroll
    for (int r = 1; r < 16; ++r) mx = fmaxf(mx, S[0][b][r]);
    auto rr = __builtin_amdgcn_permlane32_swap(__float_as_uint(mx), __float_as_uint(mx), false, false);
    mx = fmaxf(__uint_as_float(rr[0]), __uint_as_float(rr[1]));
    m_run[b] = mx;
#pragma unroll
    for (int r = 0; r < 16; ++r) { nm[b][r] = -mx; S[0][b][r] -= mx; }
  }
#pragma unroll
  for (int j = 0; j < 4; ++j) kb[j] = koff[j];
  vb = voff;
#pragma unroll
  for (int i = 0; i < 4; ++i) fr[i] = rdK(kb[i]);

  auto phaseA = [&](auto XX, auto HH, const char* ssrc, const unsigned sdst) {
    constexpr int X = decltype(XX)::v, HB = decltype(HH)::v;
    sfor<12>([&](auto I) {
      constexpr int i = decltype(I)::v;
      auto ex = [&](auto N) {
        constexpr int n = decltype(N)::v;
        if constexpr (n < 32) {
          constexpr int b = (n & 15) >> 3, r = (n >> 4) * 8 + (n & 7);
          S[X][b][r] = __builtin_amdgcn_exp2f(S[X][b][r]);
        }
      };
      auto pk = [&](auto N) {
        constexpr int n = decltype(N)::v;
        if constexpr (n >= 0 && n < 16) {
          constexpr int b = (n >> 2) & 1, ks = n >> 3, w = n & 3;
          pw[b][ks][w] = cvtpk(S[X][b][8 * ks + 2 * w], S[X][b][8 * ks + 2 * w + 1]);
        }
      };
      if constexpr (i == 0) mfma_first<AQ(0, 0)>(S[X ^ 1][0], fr[0], nm[0]);
      else if constexpr (i < 8) mfma_qa<AQ(0, i)>(S[X ^ 1][0], fr[i & 3]);
      else mfma_qv(S[X ^ 1][0], fr[i & 3], qf[0][i - 8]);
      SB();
      ex(IC<3 * i>{}); ex(IC<3 * i + 1>{});
      pk(IC<2 * i - 8>{});
      PIN(S[X][0]); PIN(S[X][1]);
      SB();
      if constexpr (i == 0) mfma_first<AQ(1, 0)>(S[X ^ 1][1], fr[0], nm[1]);
      else if constexpr (i < 8) mfma_qa<AQ(1, i)>(S[X ^ 1][1], fr[i & 3]);
      else mfma_qv(S[X ^ 1][1], fr[i & 3], qf[1][i - 8]);
      SB();
      if constexpr (i + 4 < 12) fr[i & 3] = rdK(kb[i & 3] + ((i + 4) >> 2) * 8192 + HB * 4096);
      else fr[i & 3] = rdV(vb + ((4 * HB) * 6 + (i - 8)) * 512);
      ex(IC<3 * i + 2>{});
      pk(IC<2 * i - 7>{});
      if constexpr (i == 2) asm volatile("" :: "v"(nm[0]), "v"(nm[1]));
      if constexpr (i == 1 || i == 5 || i == 9) {
        constexpr int pc = (i - 1) / 4;
        if constexpr (HB == 0) glds_i<(pc & 1) * 1024 + (pc >> 1) * 8192>(ssrc, kpo[pc], sdst);
        else glds_i<(pc & 1) * 3072 + (pc >> 1) * 1024>(ssrc, vpo[pc], sdst);
      }
      PIN(S[X][0]); PIN(S[X][1]); PIN(pw[0][0]); PIN(pw[0][1]); PIN(pw[1][0]); PIN(pw[1][1]);
      SB();
    });
  };
  auto phaseB = [&](auto XX, auto HH, auto HK, const char* ssrc, const unsigned sdst) {
    constexpr int X = decltype(XX)::v, HB = decltype(HH)::v, HBKN = decltype(HK)::v;
    float ls[2][2], mx[2];
    auto fill = [&](auto G) {
      constexpr int g = decltype(G)::v;
      if constexpr (g < 16) {
        constexpr int b = g >> 3, r = (g & 7) * 2;
        if constexpr ((g & 7) == 0) { ls[b][0] = S[X][b][r]; ls[b][1] = S[X][b][r + 1]; }
        else { ls[b][0] += S[X][b][r]; ls[b][1] += S[X][b][r + 1]; }
        PIN(ls[b][0]); PIN(ls[b][1]);
      }
      if constexpr (g == 16) { l_run[0] += ls[0][0] + ls[0][1]; PIN(l_run[0]); }
      if constexpr (g == 17) { l_run[1] += ls[1][0] + ls[1][1]; PIN(l_run[1]); }
      if constexpr (g == 20) {
#pragma unroll
        for (int b = 0; b < 2; ++b) {
          auto rr = __builtin_amdgcn_permlane32_swap(__float_as_uint(mx[b]), __float_as_uint(mx[b]), false, false);
          mx[b] = max2f(__uint_as_float(rr[0]), __uint_as_float(rr[1]));
          PIN(mx[b]);
        }
      }
      if constexpr (g == 21) {
        if (__builtin_expect(__any(max2f(mx[0], mx[1]) > THR), 0)) {
          const float aone = hi == 0 ? 1.f : 0.f;
#pragma unroll
          for (int b = 0; b < 2; ++b) {
            const float dl = fmaxf(mx[b], 0.f);
            m_run[b] += dl;
            alpha[b] = __builtin_amdgcn_exp2f(-dl);
            bcast16(nm[b], aone, -m_run[b]);
#pragma unroll
            for (int r = 0; r < 16; ++r) S[X ^ 1][b][r] -= dl;
          }
          pend = true;
        }
      }
      if constexpr (g == 16 || g == 18 || g == 22) {
        constexpr int pc = g == 16 ? 3 : (g == 18 ? 4 : 5);
        if constexpr (HB == 0) glds_i<(pc & 1) * 1024 + (pc >> 1) * 8192>(ssrc, kpo[pc], sdst);
        else glds_i<(pc & 1) * 3072 + (pc >> 1) * 1024>(ssrc, vpo[pc], sdst);
      }
    };
    auto pvm = [&](auto G) {
      constexpr int g = decltype(G)::v, i = g >> 1, b = g & 1, k = (g - 4) >> 1;
      const bf16x8 pb = __builtin_bit_cast(bf16x8, pw[b][i / 6]);
      if constexpr (g >= 4 && g < 20) {
        if constexpr (k == 0) mfma_o_mx<AO(b, i % 6)>(fr[i & 3], pb, mx[b], S[X ^ 1][b][0], S[X ^ 1][b][1], S[X ^ 1][b][2]);
        else if constexpr (k < 7) mfma_o_mx<AO(b, i % 6)>(fr[i & 3], pb, mx[b], mx[b], S[X ^ 1][b][2 * k + 1], S[X ^ 1][b][2 * k + 2]);
        else mfma_o_mx<AO(b, i % 6)>(fr[i & 3], pb, mx[b], mx[b], S[X ^ 1][b][15], S[X ^ 1][b][15]);
      } else mfma_o<AO(b, i % 6)>(fr[i & 3], pb);
    };
    sfor<12>([&](auto I) {
      constexpr int i = decltype(I)::v;
      pvm(IC<2 * i>{});
      SB();
      fill(IC<2 * i>{});
      SB();
      pvm(IC<2 * i + 1>{});
      SB();
      if constexpr (i + 4 < 12) fr[i & 3] = rdV(vb + ((4 * HB + 2 * ((i + 4) / 6)) * 6 + ((i + 4) % 6)) * 512);
      else fr[i & 3] = rdK(kb[i - 8] + HBKN * 4096);
      fill(IC<2 * i + 1>{});
      SB();
    });
    if (__builtin_expect(pend, 0)) {
      asm volatile("s_nop 15\n\ts_nop 7" ::: "memory");
      l_run[0] *= alpha[0]; l_run[1] *= alpha[1];
      sfor<192>([&](auto I) { constexpr int n = decltype(I)::v; acc_scale<n>(alpha[n / 96]); });
      asm volatile("s_nop 3" ::: "memory");
      pend = false;
    }
  };

  asm volatile("s_waitcnt vmcnt(0)" ::: "memory");
  unsigned scur = 0;
#pragma unroll 1
  for (int t = 0; t < 32; ++t) {
    const unsigned snxt = nxt(scur), swr = nxt(snxt);
    asm volatile("s_waitcnt vmcnt(6)\n\ts_barrier" ::: "memory");
    int tk = t + 2 < 32 ? t + 2 : 31;
    const int tv = tk;
    if (tk == 31 && wid >= 2) tk = 30;
    const char* ks_ = kbase + (size_t)(tk * 64 + 32) * (DH * 2);
    const char* vs_ = vbase + (size_t)(tv * 64) * (DH * 2);
    SB();
    phaseA(IC<0>{}, IC<0>{}, ks_, kdst + swr);
    phaseB(IC<0>{}, IC<0>{}, IC<1>{}, ks_, kdst + swr);
    phaseA(IC<1>{}, IC<1>{}, vs_, vdst + swr);
#pragma unroll
    for (int j = 0; j < 4; ++j) kb[j] = koff[j] + snxt;
    phaseB(IC<1>{}, IC<1>{}, IC<0>{}, vs_, vdst + swr);
    vb = voff + snxt;
    scur = snxt;
  }
  asm volatile("s_waitcnt vmcnt(0)\n\ts_nop 15\n\ts_nop 7" ::: "memory");
  int tz = threadIdx.x;
  asm volatile("" : "+v"(tz));
  float rls[2];
#pragma unroll
  for (int b = 0; b < 2; ++b) {
    auto rr = __builtin_amdgcn_permlane32_swap(__float_as_uint(l_run[b]), __float_as_uint(l_run[b]), false, false);
    rls[b] = 1.0f / (__uint_as_float(rr[0]) + __uint_as_float(rr[1]));
  }
  u32x4* ofr = (u32x4*)O + ((size_t)(qb * 8 + (tz >> 6) * 2) * 384 + head * 12) * 64 + (tz & 63);
  sfor<24>([&](auto I) {
    constexpr int n = decltype(I)::v, b = n / 12, f = n % 12, R = AO(b, f >> 1) + 8 * (f & 1);
    const float rl = rls[b];
    float t8[8];
    acc_rd8<R>(t8);
    ofr[(b * 384 + f) * 64] = u32x4{cvtpk(t8[0] * rl, t8[1] * rl), cvtpk(t8[2] * rl, t8[3] * rl),
                                    cvtpk(t8[4] * rl, t8[5] * rl), cvtpk(t8[6] * rl, t8[7] * rl)};
  });
}

extern "C" void kernel_launch(void* const* d_in, const int* in_sizes, int n_in, void* d_out, int out_size,
                              void* d_ws, size_t ws_size, hipStream_t stream) {
  const u16* x = (const u16*)d_in[0];
  const u16* kc = (const u16*)d_in[1];
  const u16* vc = (const u16*)d_in[2];
  const u16* wq = (const u16*)d_in[3];
  const u16* wo = (const u16*)d_in[4];
  const u16* w1 = (const u16*)d_in[6];
  const u16* b1 = (const u16*)d_in[7];
  const u16* w2 = (const u16*)d_in[8];
  const u16* b2 = (const u16*)d_in[9];
  char* ws = (char*)d_ws;
  u16* xnp = (u16*)(ws);
  u16* y = (u16*)(ws + (4u << 20));
  u16* h = (u16*)(ws + (8u << 20));
  u16* O = (u16*)(ws + (16u << 20));
  float* part = (float*)(ws + (48u << 20));

  hipFuncSetAttribute((const void*)k_attn, hipFuncAttributeMaxDynamicSharedMemorySize, 6 * TB);
  hipFuncSetAttribute((const void*)k_gemm2, hipFuncAttributeMaxDynamicSharedMemorySize, 4 * 16384);

  k_ln<<<SEQ / 4, 256, 0, stream>>>(x, xnp, y, wq, w1);
  k_attn<<<256, 256, 6 * TB, stream>>>(xnp, wq, kc, vc, O, y, w1, b1, h);
  k_gemm2<<<200, 256, 4 * 16384, stream>>>(O, wo, h, w2, part);
  k_final<<<SEQ * DM / 1024, 256, 0, stream>>>(x, part, b2, (u16*)d_out);
}
```

```cpp
#include <hip/hip_runtime.h>
#include <hip/hip_bf16.h>
#include <stdint.h>

typedef __attribute__((ext_vector_type(8))) short bf16x8;
typedef __attribute__((ext_vector_type(8))) __bf16 bf16x8b;
typedef __attribute__((ext_vector_type(4))) short s16x4;
typedef __attribute__((ext_vector_type(16))) float f32x16;
typedef __attribute__((ext_vector_type(4))) float f32x4;
typedef __attribute__((ext_vector_type(2))) float f32x2;
typedef __attribute__((ext_vector_type(2))) __bf16 bf16x2b;
typedef __attribute__((ext_vector_type(4))) unsigned u32x4;
typedef __attribute__((ext_vector_type(2))) unsigned u32x2;
typedef __attribute__((address_space(3))) char* lds_ptr;
typedef unsigned short u16;

#define DI __device__ __forceinline__

constexpr int SEQ = 2048, DM = 640, NH = 32, DH = 192, FF = 1536, QD = NH * DH;

DI f32x16 MFMA(bf16x8 a, bf16x8 b, f32x16 c) {
  return __builtin_amdgcn_mfma_f32_32x32x16_bf16(__builtin_bit_cast(bf16x8b, a), __builtin_bit_cast(bf16x8b, b), c, 0, 0, 0);
}
DI unsigned cvtpk(float lo, float hi) {
  f32x2 v = {lo, hi};
  bf16x2b b = __builtin_convertvector(v, bf16x2b);
  return __builtin_bit_cast(unsigned, b);
}
DI float bf2f(u16 v) { return __uint_as_float(((unsigned)v) << 16); }
DI float rbf(float v) { return __uint_as_float(cvtpk(v, 0.f) << 16); }
DI s16x4 trread(lds_ptr p) {
  return __builtin_amdgcn_ds_read_tr16_b64_v4i16((__attribute__((address_space(3))) s16x4*)p);
}
DI bf16x8 ldsread(lds_ptr p) { return *(__attribute__((address_space(3))) bf16x8*)p; }
DI bf16x8 cat(s16x4 a, s16x4 b) { return __builtin_shufflevector(a, b, 0, 1, 2, 3, 4, 5, 6, 7); }
DI void glds16(const void* g, lds_ptr l) {
  __builtin_amdgcn_global_load_lds((const __attribute__((address_space(1))) unsigned*)g,
                                   (__attribute__((address_space(3))) unsigned*)l, 16, 0, 0);
}

DI float wave_sum(float v) {
#pragma unroll
  for (int o = 32; o > 0; o >>= 1) v += __shfl_xor(v, o, 64);
  return v;
}
DI void ln_row(float (&v)[10]) {
  float s = 0.f;
#pragma unroll
  for (int i = 0; i < 10; ++i) s += v[i];
  s = wave_sum(s);
  const float mean = s * (1.0f / DM);
  float q = 0.f;
#pragma unroll
  for (int i = 0; i < 10; ++i) { float d = v[i] - mean; q += d * d; }
  q = wave_sum(q);
  const float mb = rbf(mean), vb = rbf(q * (1.0f / DM));
  const float den = rbf(sqrtf(rbf(vb + 1e-5f)));
#pragma unroll
  for (int i = 0; i < 10; ++i) v[i] = rbf(rbf(v[i] - mb) / den);
}
__global__ __launch_bounds__(256) void k_ln(const u16* __restrict__ x, u16* __restrict__ xnp, u16* __restrict__ y) {
  const int row = blockIdx.x * 4 + (threadIdx.x >> 6), lane = threadIdx.x & 63;
  const unsigned* xr = (const unsigned*)(x + (size_t)row * DM);
  float v[10];
#pragma unroll
  for (int i = 0; i < 5; ++i) {
    unsigned w = xr[lane + 64 * i];
    v[2 * i] = __uint_as_float(w << 16);
    v[2 * i + 1] = __uint_as_float(w & 0xffff0000u);
  }
  ln_row(v);
  unsigned* xo = (unsigned*)xnp + (size_t)(row >> 5) * (40 * 64 * 4) + (row & 31) * 4;
#pragma unroll
  for (int i = 0; i < 5; ++i) {
    int e = lane + 64 * i;
    int ep = (e & ~6) | ((e & 2) << 1) | ((e & 4) >> 1);
    xo[(ep >> 3) * 256 + ((ep >> 2) & 1) * 128 + (ep & 3)] =
        (__float_as_uint(v[2 * i]) >> 16) | (__float_as_uint(v[2 * i + 1]) & 0xffff0000u);
  }
  ln_row(v);
  unsigned* yo = (unsigned*)y + (size_t)(row >> 5) * (40 * 64 * 4) + (row & 31) * 4;
#pragma unroll
  for (int i = 0; i < 5; ++i) {
    int e = lane + 64 * i;
    int ep = (e & ~6) | ((e & 2) << 1) | ((e & 4) >> 1);
    yo[(ep >> 3) * 256 + ((ep >> 2) & 1) * 128 + (ep & 3)] =
        (__float_as_uint(v[2 * i]) >> 16) | (__float_as_uint(v[2 * i + 1]) & 0xffff0000u);
  }
}

#define SB() __builtin_amdgcn_sched_barrier(0)
#define PIN(x) asm volatile("" : "+v"(x))
template <int I> struct IC { static constexpr int v = I; };
template <int N, class F> DI void sfor(F&& f) {
  if constexpr (N > 0) { sfor<N - 1>(f); f(IC<N - 1>{}); }
}
template <int IMM> DI void glds_i(const void* sbase, unsigned voff, unsigned ldsbase) {
  asm volatile("s_add_i32 m0, %2, %3\n\ts_nop 0\n\tglobal_load_lds_dwordx4 %0, %1"
               :: "v"(voff), "s"(sbase), "s"(ldsbase), "i"(IMM) : "memory", "scc");
}
template <int OFF> DI void gload16(bf16x8& d, const char* p) {
  asm volatile("global_load_dwordx4 %0, %1, off offset:%2" : "=&v"(d) : "v"(p), "i"(OFF) : "memory");
}
template <int OFF> DI void gload8(u32x2& d, const void* p) {
  asm volatile("global_load_dwordx2 %0, %1, off offset:%2" : "=&v"(d) : "v"(p), "i"(OFF) : "memory");
}
template <int N> DI void xwait2(bf16x8 (&x)[2][4]) {
  asm volatile("s_waitcnt vmcnt(%8)\n\ts_barrier" : "+v"(x[0][0]), "+v"(x[0][1]), "+v"(x[0][2]), "+v"(x[0][3]), "+v"(x[1][0]), "+v"(x[1][1]), "+v"(x[1][2]), "+v"(x[1][3]) : "i"(N) : "memory");
}
DI void xpin2(bf16x8 (&x)[2][4]) {
  asm volatile("" : "+v"(x[0][0]), "+v"(x[0][1]), "+v"(x[0][2]), "+v"(x[0][3]), "+v"(x[1][0]), "+v"(x[1][1]), "+v"(x[1][2]), "+v"(x[1][3]));
}

DI void wgemm24(const char* wbase, unsigned ldwb, const char* xp, unsigned xrbs, int ktiles, unsigned lds0,
                f32x16 (&acc)[2][4]) {
  constexpr int NB = 4;
  constexpr unsigned TBY = 4096 * NB;
  const int tid = threadIdx.x, lane = tid & 63, wid = __builtin_amdgcn_readfirstlane(tid >> 6);
  const int hi = lane >> 5;
  unsigned wpo[NB];
#pragma unroll
  for (int i = 0; i < NB; ++i) {
    const int sub = 2 * (wid * NB + i) + hi, kg = sub / NB, nb = sub % NB;
    wpo[i] = (unsigned)(8 * kg + ((lane >> 2) & 7)) * ldwb + nb * 64 + (lane & 3) * 16;
  }
  const unsigned wdst = lds0 + wid * NB * 1024;
  const unsigned voff = lds0 + (hi * 4 + ((lane >> 2) & 3)) * 64 + ((lane >> 4) & 1) * 32 + (lane & 3) * 8;
  auto rdW = [&](unsigned a) { return cat(trread((lds_ptr)(uintptr_t)a), trread((lds_ptr)(uintptr_t)(a + NB * 512))); };
#pragma unroll
  for (int r = 0; r < 2; ++r)
#pragma unroll
    for (int n = 0; n < NB; ++n) acc[r][n] = f32x16{};
  bf16x8 x0[2][4], x1[2][4], x2[2][4], x3[2][4], fr[4];
  const size_t wts = (size_t)64 * ldwb;
  auto issue = [&](bf16x8 (&x)[2][4], int t, auto SLOT) {
    const char* xq = xp + (size_t)t * 4096;
    sfor<8>([&](auto I) { constexpr int i = decltype(I)::v; gload16<(i & 3) * 1024>(x[i >> 2][i & 3], xq + (i >> 2) * xrbs); });
    sfor<NB>([&](auto I) { glds_i<decltype(SLOT)::v * TBY + decltype(I)::v * 1024>(wbase + (size_t)t * wts, wpo[decltype(I)::v], wdst); });
  };
  issue(x0, 0, IC<0>{});
  issue(x1, 1, IC<1>{});
  issue(x2, 2, IC<2>{});
  asm volatile("s_waitcnt vmcnt(24)\n\ts_barrier" ::: "memory");
#pragma unroll
  for (int i = 0; i < 4; ++i) fr[i] = rdW(voff + i * 512);
  auto step = [&](bf16x8 (&xc)[2][4], bf16x8 (&xn)[2][4], const int t, auto SLOT) {
    constexpr int sl = decltype(SLOT)::v, sw = (sl + 3) & 3;
    xwait2<12>(xc);
    const unsigned vb = voff + sl * TBY, vbn = voff + ((sl + 1) & 3) * TBY;
    const int tn = t + 3 < ktiles ? t + 3 : ktiles - 1;
    const char* xq = xp + (size_t)tn * 4096;
    const char* wq_ = wbase + (size_t)tn * wts;
    SB();
    sfor<16>([&](auto I) {
      constexpr int i = decltype(I)::v;
      acc[0][i & 3] = MFMA(fr[i & 3], xc[0][i >> 2], acc[0][i & 3]);
      acc[1][i & 3] = MFMA(fr[i & 3], xc[1][i >> 2], acc[1][i & 3]);
      SB();
      if constexpr (i + 4 < 16) fr[i & 3] = rdW(vb + ((2 * ((i + 4) >> 2)) * NB + (i & 3)) * 512);
      else fr[i & 3] = rdW(vbn + (i & 3) * 512);
      if constexpr (i < 8) gload16<(i & 3) * 1024>(xn[i >> 2][i & 3], xq + (i >> 2) * xrbs);
      if constexpr (i >= 8 && i < 12) glds_i<sw * TBY + (i - 8) * 1024>(wq_, wpo[i - 8], wdst);
      SB();
    });
  };
#pragma unroll 1
  for (int t = 0; t < ktiles; t += 4) {
    step(x0, x3, t, IC<0>{});
    step(x1, x0, t + 1, IC<1>{});
    step(x2, x1, t + 2, IC<2>{});
    step(x3, x2, t + 3, IC<3>{});
  }
  asm volatile("s_waitcnt vmcnt(0)" ::: "memory");
  xpin2(x0); xpin2(x1); xpin2(x2);
}
DI int crow(int r, int hi) { return (r & 3) + 8 * (r >> 2) + 4 * hi; }

DI float gelu_tanh(float v) {
  const float u = 0.7978845608028654f * (v + 0.044715f * v * v * v);
  const float e = __builtin_amdgcn_exp2f(2.8853900817779268f * u);
  return v - v * __builtin_amdgcn_rcpf(e + 1.f);
}

__global__ __launch_bounds__(256, 1) void k_gemm2(const u16* __restrict__ O, const u16* __restrict__ wo,
                                                  const u16* __restrict__ h, const u16* __restrict__ w2,
                                                  float* __restrict__ part) {
  extern __shared__ __attribute__((aligned(16))) char smem[];
  const int bid = blockIdx.x, x = bid & 7, r = bid >> 3;
  const int nt = r % 5, c = r < 20 ? (x >> 1) : 4, mt = r < 20 ? 4 * (x & 1) + r / 5 : x;
  const int lane = threadIdx.x & 63, wid = threadIdx.x >> 6;
  const int rb0 = mt * 8 + wid * 2;
  const char* W; const char* X; unsigned xrbs;
  if (c < 4) {
    W = (const char*)(wo + (size_t)c * 24 * 64 * DM + nt * 128);
    xrbs = 384 * 1024;
    X = (const char*)O + ((size_t)rb0 * 384 + c * 96) * 1024 + lane * 16;
  } else {
    W = (const char*)(w2 + nt * 128);
    xrbs = 96 * 1024;
    X = (const char*)h + (size_t)rb0 * 96 * 1024 + lane * 16;
  }
  f32x16 acc[2][4];
  wgemm24(W, DM * 2, X, xrbs, 24, (unsigned)(uintptr_t)(lds_ptr)smem, acc);
  f32x4* P = (f32x4*)part + (size_t)c * (SEQ * DM / 4);
#pragma unroll
  for (int b = 0; b < 2; ++b)
#pragma unroll
    for (int nb = 0; nb < 4; ++nb)
#pragma unroll
      for (int q = 0; q < 4; ++q)
        P[(((size_t)(rb0 + b) * 20 + nt * 4 + nb) * 4 + q) * 64 + lane] =
            f32x4{acc[b][nb][4 * q], acc[b][nb][4 * q + 1], acc[b][nb][4 * q + 2], acc[b][nb][4 * q + 3]};
}

__global__ __launch_bounds__(256) void k_final(const u16* __restrict__ x, const float* __restrict__ part,
                                               const u16* __restrict__ b2, u16* __restrict__ out) {
  const int idx = blockIdx.x * 256 + threadIdx.x;
  constexpr size_t PS = (size_t)SEQ * DM / 4;
  const f32x4* P = (const f32x4*)part + idx;
  const f32x4 p0 = P[0], p1 = P[PS], p2 = P[2 * PS], p3 = P[3 * PS], pm = P[4 * PS];
  const int lane = idx & 63, q = (idx >> 6) & 3, blk = idx >> 8, cb = blk % 20, rb = blk / 20;
  const int m = rb * 32 + (lane & 31), n = cb * 32 + 8 * q + 4 * (lane >> 5);
  const u32x2 xw = *(const u32x2*)(x + (size_t)m * DM + n);
  const u32x2 bw = *(const u32x2*)(b2 + n);
  float xo[4] = {__uint_as_float(xw[0] << 16), __uint_as_float(xw[0] & 0xffff0000u), __uint_as_float(xw[1] << 16),
                 __uint_as_float(xw[1] & 0xffff0000u)};
  float bo[4] = {__uint_as_float(bw[0] << 16), __uint_as_float(bw[0] & 0xffff0000u), __uint_as_float(bw[1] << 16),
                 __uint_as_float(bw[1] & 0xffff0000u)};
  float r[4];
#pragma unroll
  for (int i = 0; i < 4; ++i) {
    const float s = rbf((p0[i] + p1[i]) + (p2[i] + p3[i]));
    const float mm = rbf(rbf(pm[i]) + bo[i]);
    const float g = rbf(mm / (1.f + __expf(-mm)));
    r[i] = xo[i] + rbf(s * g);
  }
  u32x2 o = {cvtpk(r[0], r[1]), cvtpk(r[2], r[3])};
  *(u32x2*)(out + (size_t)m * DM + n) = o;
}

constexpr int TB = 24576;
constexpr float QSCALE = 0.07216878364870322f * 1.4426950408889634f;
constexpr float THR = 20.0f;

DI void glds_s(const void* sbase, unsigned voff, unsigned ldsaddr) {
  asm volatile("s_mov_b32 m0, %2\n\ts_nop 0\n\tglobal_load_lds_dwordx4 %0, %1"
               :: "v"(voff), "s"(sbase), "s"(ldsaddr) : "memory");
}
DI float max3f(float a, float b, float c) { float r; asm("v_max3_f32 %0, %1, %2, %3" : "=v"(r) : "v"(a), "v"(b), "v"(c)); return r; }
DI float max2f(float a, float b) { float r; asm("v_max_f32_e32 %0, %1, %2" : "=v"(r) : "v"(a), "v"(b)); return r; }
DI void bcast16(f32x16& d, float aone, float negm) {
  asm volatile("s_nop 1\n\tv_mfma_f32_32x32x2_f32 %0, %1, %2, 0" : "+v"(d) : "v"(aone), "v"(negm));
}
DI bf16x8 rdK(unsigned a) { return ldsread((lds_ptr)(uintptr_t)a); }
DI bf16x8 rdV(unsigned a) { return cat(trread((lds_ptr)(uintptr_t)a), trread((lds_ptr)(uintptr_t)(a + 3072))); }
DI unsigned nxt(unsigned s) { return s == 2 * TB ? 0 : s + TB; }
#define ACLOB "a0","a1","a2","a3","a4","a5","a6","a7","a8","a9","a10","a11","a12","a13","a14","a15","a16","a17","a18","a19","a20","a21","a22","a23","a24","a25","a26","a27","a28","a29","a30","a31","a32","a33","a34","a35","a36","a37","a38","a39","a40","a41","a42","a43","a44","a45","a46","a47","a48","a49","a50","a51","a52","a53","a54","a55","a56","a57","a58","a59","a60","a61","a62","a63","a64","a65","a66","a67","a68","a69","a70","a71","a72","a73","a74","a75","a76","a77","a78","a79","a80","a81","a82","a83","a84","a85","a86","a87","a88","a89","a90","a91","a92","a93","a94","a95","a96","a97","a98","a99","a100","a101","a102","a103","a104","a105","a106","a107","a108","a109","a110","a111","a112","a113","a114","a115","a116","a117","a118","a119","a120","a121","a122","a123","a124","a125","a126","a127","a128","a129","a130","a131","a132","a133","a134","a135","a136","a137","a138","a139","a140","a141","a142","a143","a144","a145","a146","a147","a148","a149","a150","a151","a152","a153","a154","a155","a156","a157","a158","a159","a160","a161","a162","a163","a164","a165","a166","a167","a168","a169","a170","a171","a172","a173","a174","a175","a176","a177","a178","a179","a180","a181","a182","a183","a184","a185","a186","a187","a188","a189","a190","a191","a192","a193","a194","a195","a196","a197","a198","a199","a200","a201","a202","a203","a204","a205","a206","a207","a208","a209","a210","a211","a212","a213","a214","a215","a216","a217","a218","a219","a220","a221","a222","a223","a224","a225","a226","a227","a228","a229","a230","a231","a232","a233","a234","a235","a236","a237","a238","a239","a240","a241","a242","a243","a244","a245","a246","a247","a248","a249","a250","a251","a252","a253","a254","a255"
constexpr int AO(int b, int db) { return 16 * (6 * b + db); }
constexpr int AQ(int b, int i) { return 192 + 32 * b + 4 * i; }
template <int R> DI void mfma_o(const bf16x8& a, const bf16x8& b) {
  asm volatile("v_mfma_f32_32x32x16_bf16 a[%c2:%c3], %0, %1, a[%c2:%c3]" :: "v"(a), "v"(b), "i"(R), "i"(R + 15) : ACLOB);
}
template <int R> DI void mfma_o_mx(const bf16x8& a, const bf16x8& b, float& m, float x, float y, float z) {
  asm volatile("v_mfma_f32_32x32x16_bf16 a[%c6:%c7], %1, %2, a[%c6:%c7]\n\tv_max3_f32 %0, %3, %4, %5"
               : "=v"(m) : "v"(a), "v"(b), "v"(x), "v"(y), "v"(z), "i"(R), "i"(R + 15) : ACLOB);
}
template <int R> DI void mfma_first(f32x16& d, const bf16x8& a, const f32x16& c) {
  asm volatile("v_mfma_f32_32x32x16_bf16 %0, %1, a[%c3:%c4], %2" : "=&v"(d) : "v"(a), "v"(c), "i"(R), "i"(R + 3) : ACLOB);
}
template <int R> DI void mfma_qa(f32x16& d, const bf16x8& a) {
  asm volatile("v_mfma_f32_32x32x16_bf16 %0, %1, a[%c2:%c3], %0" : "+v"(d) : "v"(a), "i"(R), "i"(R + 3) : ACLOB);
}
DI void mfma_qv(f32x16& d, const bf16x8& a, const bf16x8& b) {
  asm volatile("v_mfma_f32_32x32x16_bf16 %0, %1, %2, %0" : "+v"(d) : "v"(a), "v"(b));
}
template <int R> DI float acc_rd() { float r; asm volatile("v_accvgpr_read_b32 %0, a[%c1]" : "=v"(r) : "i"(R)); return r; }
template <int R> DI void acc_wr(unsigned v) { asm volatile("v_accvgpr_write_b32 a[%c1], %0" :: "v"(v), "i"(R) : ACLOB); }
template <int R> DI void acc_zero() { asm volatile("v_accvgpr_write_b32 a[%c0], 0" :: "i"(R) : ACLOB); }
template <int R> DI void acc_zero16(const bf16x8& z) {
  asm volatile("v_mfma_f32_32x32x16_bf16 a[%c1:%c2], %0, %0, 0" :: "v"(z), "i"(R), "i"(R + 15) : ACLOB);
}
template <int R> DI void acc_rd8(float (&r)[8]) {
  asm volatile("v_accvgpr_read_b32 %0, a[%c8]\n\tv_accvgpr_read_b32 %1, a[%c9]\n\tv_accvgpr_read_b32 %2, a[%c10]\n\tv_accvgpr_read_b32 %3, a[%c11]\n\t"
               "v_accvgpr_read_b32 %4, a[%c12]\n\tv_accvgpr_read_b32 %5, a[%c13]\n\tv_accvgpr_read_b32 %6, a[%c14]\n\tv_accvgpr_read_b32 %7, a[%c15]"
               : "=v"(r[0]), "=v"(r[1]), "=v"(r[2]), "=v"(r[3]), "=v"(r[4]), "=v"(r[5]), "=v"(r[6]), "=v"(r[7])
               : "i"(R), "i"(R + 1), "i"(R + 2), "i"(R + 3), "i"(R + 4), "i"(R + 5), "i"(R + 6), "i"(R + 7));
}
template <int R> DI void acc_wr4(unsigned a, unsigned b, unsigned c, unsigned d) {
  asm volatile("v_accvgpr_write_b32 a[%c4], %0\n\tv_accvgpr_write_b32 a[%c5], %1\n\tv_accvgpr_write_b32 a[%c6], %2\n\tv_accvgpr_write_b32 a[%c7], %3"
               :: "v"(a), "v"(b), "v"(c), "v"(d), "i"(R), "i"(R + 1), "i"(R + 2), "i"(R + 3) : ACLOB);
}
template <int R> DI void acc_scale(float f) {
  float t;
  asm volatile("v_accvgpr_read_b32 %0, a[%c2]\n\tv_mul_f32 %0, %1, %0\n\tv_accvgpr_write_b32 a[%c2], %0" : "=&v"(t) : "v"(f), "i"(R) : ACLOB);
}

__global__ __launch_bounds__(256, 1) void k_attn(const u16* __restrict__ xnp, const u16* __restrict__ wq,
                                                 const u16* __restrict__ Kc, const u16* __restrict__ Vc,
                                                 u16* __restrict__ O, const u16* __restrict__ yfr,
                                                 const u16* __restrict__ w1, const u16* __restrict__ b1,
                                                 u16* __restrict__ hfr) {
  extern __shared__ __attribute__((aligned(16))) char smem[];
  const unsigned lds0 = (unsigned)(uintptr_t)(lds_ptr)smem;
  const int tid = threadIdx.x, lane = tid & 63, wid = __builtin_amdgcn_readfirstlane(tid >> 6);
  const int r32 = lane & 31, hi = lane >> 5;
  const int bid = blockIdx.x, xcd = bid & 7, idx = bid >> 3;
  const int head = xcd * 4 + (idx >> 3), qb = idx & 7;
  const int q0 = qb * 256 + wid * 64;

  const int krl = lane >> 3;
  const unsigned kvo0 = (unsigned)(krl * DH * 2 + (((lane & 7) ^ (krl >> 1)) << 4));
  const unsigned kvo1 = (unsigned)(krl * DH * 2 + (((lane & 7) ^ (4 + (krl >> 1))) << 4));
  const unsigned vvo = (unsigned)(((lane >> 2) & 7) * DH * 2 + (lane >> 5) * 64 + (lane & 3) * 16);
  const unsigned wvo = (unsigned)(((lane >> 2) & 7) * QD * 2 + (lane >> 5) * 64 + (lane & 3) * 16);
  const char* kbase = (const char*)(Kc + (size_t)head * SEQ * DH) + (size_t)(16 * wid) * DH * 2;
  const char* vbase = (const char*)(Vc + (size_t)head * SEQ * DH) + (size_t)(16 * wid) * DH * 2;
  const char* wbase = (const char*)(wq + head * DH) + (size_t)(16 * wid) * QD * 2;
  const unsigned kdst = lds0 + wid * 2048, vdst = lds0 + 3 * TB + wid * 6144;
  auto pieceK = [&](int key0, unsigned slot, int pc) {
    glds_s(kbase + (size_t)key0 * (DH * 2) + (pc & 1) * (8 * DH * 2) + (pc >> 1) * 128, (pc & 1) ? kvo1 : kvo0,
           kdst + slot + (pc & 1) * 1024 + (pc >> 1) * 8192);
  };
  auto pieceV = [&](int key0, unsigned slot, int pc) {
    glds_s(vbase + (size_t)key0 * (DH * 2) + (pc & 1) * (8 * DH * 2) + (pc >> 1) * 128, vvo,
           vdst + slot + (pc & 1) * 3072 + (pc >> 1) * 1024);
  };
  auto pieceW = [&](int t, unsigned slot, int pc) {
    glds_s(wbase + (size_t)t * (64 * QD * 2) + (size_t)(pc & 1) * (8 * QD * 2) + (pc >> 1) * 128, wvo,
           vdst + slot + (pc & 1) * 3072 + (pc >> 1) * 1024);
  };
  unsigned kpo[6], vpo[6];
#pragma unroll
  for (int pc = 0; pc < 6; ++pc) {
    kpo[pc] = ((pc & 1) ? kvo1 : kvo0) + (pc & 1) * (8 * DH * 2) + (pc >> 1) * 128;
    vpo[pc] = vvo + (pc & 1) * (8 * DH * 2) + (pc >> 1) * 128;
  }
  unsigned koff[4];
#pragma unroll
  for (int j = 0; j < 4; ++j) koff[j] = lds0 + r32 * 128 + (((2 * j + hi) ^ ((r32 >> 1) & 7)) << 4);
  const unsigned voff = lds0 + 3 * TB + (hi * 4 + ((lane >> 2) & 3)) * 64 + ((lane >> 4) & 1) * 32 + (lane & 3) * 8;

  bf16x8 zfr = bf16x8{};
  PIN(zfr);
  asm volatile("s_nop 3");
  sfor<12>([&](auto I) { acc_zero16<16 * decltype(I)::v>(zfr); });
  bf16x8 fr[4];

  const int odd = head & 1, hj = head >> 1;
  const char* xsrc0 = (const char*)xnp + ((size_t)(q0 >> 5) * (40 * 64) + lane) * 16;
  const char* ysrcA = (const char*)yfr + ((size_t)((q0 >> 5) + odd) * (40 * 64) + lane) * 16;
  const char* ysrcB = (const char*)yfr + ((size_t)((q0 >> 5) + 1 - odd) * (40 * 64) + lane) * 16;
  const char* w1base = (const char*)(w1 + (3 * hj + odd) * 32) + (size_t)(16 * wid) * (FF * 2);
  const unsigned w1vo = (unsigned)(((lane >> 2) & 7) * FF * 2 + (lane >> 5) * 64 + (lane & 3) * 16);
  const unsigned w1dst = lds0 + TB + wid * 2048;
  const unsigned w1rd = lds0 + TB + (hi * 4 + ((lane >> 2) & 3)) * 64 + ((lane >> 4) & 1) * 32 + (lane & 3) * 8;
  const unsigned w1P = w1rd + (odd ? 512 : 0), w1Q = w1rd + (odd ? 0 : 512);
  auto rdH = [&](unsigned a) { return cat(trread((lds_ptr)(uintptr_t)a), trread((lds_ptr)(uintptr_t)(a + 1024))); };
  u32x2 bwv[2][4];
  {
    const u16* bp = b1 + (3 * hj + 2 * odd) * 32 + 4 * hi;
    const u16* bq = b1 + (3 * hj + 1) * 32 + 4 * hi;
    sfor<8>([&](auto I) {
      constexpr int i = decltype(I)::v;
      gload8<(i & 3) * 16>(bwv[i >> 2][i & 3], i < 4 ? bp : bq);
    });
  }
  bf16x8 xa[4][4], xb[4][4];
  f32x16 hacc[3];
  hacc[0] = f32x16{}; hacc[1] = f32x16{}; hacc[2] = f32x16{};
  PIN(hacc[0]); PIN(hacc[1]); PIN(hacc[2]);
#pragma unroll
  for (int pc = 0; pc < 6; ++pc) pieceW(0, 2 * TB, pc);
#pragma unroll
  for (int g = 0; g < 2; ++g) glds_s(w1base + (size_t)g * (8 * FF * 2), w1vo, w1dst + 2 * 8192 + g * 1024);
  sfor<16>([&](auto I) {
    constexpr int i = decltype(I)::v;
    gload16<(i & 3) * 1024>(xa[i >> 2][i & 3], (i >> 2) == 0 ? xsrc0 : (i >> 2) == 1 ? xsrc0 + 40960 : (i >> 2) == 2 ? ysrcA : ysrcB);
  });
#pragma unroll
  for (int pc = 0; pc < 6; ++pc) pieceW(1, 0, pc);
#pragma unroll
  for (int g = 0; g < 2; ++g) glds_s(w1base + (size_t)(64 * FF * 2) + (size_t)g * (8 * FF * 2), w1vo, w1dst + g * 1024);
#define XOPS(x) "+v"(x[0][0]), "+v"(x[0][1]), "+v"(x[0][2]), "+v"(x[0][3]), "+v"(x[1][0]), "+v"(x[1][1]), "+v"(x[1][2]), "+v"(x[1][3]), \
                "+v"(x[2][0]), "+v"(x[2][1]), "+v"(x[2][2]), "+v"(x[2][3]), "+v"(x[3][0]), "+v"(x[3][1]), "+v"(x[3][2]), "+v"(x[3][3])
  {
    unsigned srd = 2 * TB, swr = TB;
    auto qstep = [&](bf16x8 (&xc)[4][4], bf16x8 (&xn)[4][4], const int t) {
      asm volatile("s_waitcnt vmcnt(8)\n\ts_barrier" : XOPS(xc) :: "memory");
      const unsigned vb = voff + srd;
      const unsigned hP = w1P + srd / 3, hQ = w1Q + srd / 3;
#pragma unroll
      for (int i = 0; i < 4; ++i) fr[i] = rdV(vb + i * 512);
      const int tn = t + 1 < 10 ? t + 1 : 9, tw = t + 2 < 10 ? t + 2 : 9;
      const char* p0 = xsrc0 + tn * 4096;
      const char* p2 = ysrcA + tn * 4096;
      const char* p3 = ysrcB + tn * 4096;
      SB();
      const bool isw = t + 2 < 10;
      const char* ssrc = isw ? wbase + (size_t)(t + 2) * (64 * QD * 2) : vbase + (size_t)(t - 8) * (64 * DH * 2);
      const unsigned sgs = isw ? 8 * QD * 2 : 8 * DH * 2;
      const unsigned svo = isw ? wvo : vvo;
      const char* w1s = w1base + (size_t)tw * (64 * FF * 2);
      if (t == 4 && wid >= 2) {
#pragma unroll
        for (int pc = 0; pc < 6; ++pc) pieceK(-32, 2 * TB, pc);
      }
      if (t == 6) {
#pragma unroll
        for (int pc = 0; pc < 6; ++pc) pieceK(32, 0, pc);
      }
      auto rdN = [&](auto N) {
        constexpr int n = decltype(N)::v, ks = n >> 3, g = n & 7;
        if constexpr (g < 6) return rdV(vb + ((2 * ks) * 6 + g) * 512);
        else if constexpr (g == 6) return rdH(hP + (4 * ks) * 512);
        else return rdH(hQ + (4 * ks) * 512);
      };
      sfor<32>([&](auto I) {
        constexpr int i = decltype(I)::v, ks = i >> 3, g = i & 7;
        if constexpr (g < 6) { mfma_o<AO(0, g)>(fr[i & 3], xc[0][ks]); mfma_o<AO(1, g)>(fr[i & 3], xc[1][ks]); }
        else if constexpr (g == 6) { mfma_qv(hacc[0], fr[i & 3], xc[2][ks]); mfma_qv(hacc[1], fr[i & 3], xc[3][ks]); }
        else mfma_qv(hacc[2], fr[i & 3], xc[2][ks]);
        if constexpr (i + 4 < 32) fr[i & 3] = rdN(IC<(i + 4 < 32 ? i + 4 : 0)>{});
        if constexpr (i < 16)
          gload16<(i & 3) * 1024>(xn[i >> 2][i & 3], (i >> 2) == 0 ? p0 : (i >> 2) == 1 ? p0 + 40960 : (i >> 2) == 2 ? p2 : p3);
        if constexpr (i >= 16 && i < 22) {
          constexpr int pc = i - 16;
          glds_s(ssrc + (size_t)(pc & 1) * sgs + (pc >> 1) * 128, svo, vdst + swr + (pc & 1) * 3072 + (pc >> 1) * 1024);
        }
        if constexpr (i >= 22 && i < 24) glds_s(w1s + (size_t)(i - 22) * (8 * FF * 2), w1vo, w1dst + swr / 3 + (i - 22) * 1024);
        SB();
      });
      srd = nxt(srd);
      swr = nxt(swr);
    };
    asm volatile("s_waitcnt vmcnt(8)" : XOPS(xa) :: "memory");
    asm volatile("" : "+v"(bwv[0][0]), "+v"(bwv[0][1]), "+v"(bwv[0][2]), "+v"(bwv[0][3]), "+v"(bwv[1][0]), "+v"(bwv[1][1]), "+v"(bwv[1][2]), "+v"(bwv[1][3]));
#pragma unroll 1
    for (int t = 0; t < 10; t += 2) { qstep(xa, xb, t); qstep(xb, xa, t + 1); }
    asm volatile("s_waitcnt vmcnt(0)\n\ts_barrier" : XOPS(xa) :: "memory");
  }
#pragma unroll
  for (int pc = 0; pc < 6; ++pc) pieceK(96, TB, pc);
  asm volatile("s_nop 15\n\ts_nop 7" ::: "memory");
  SB();
  {
    const int rbA = (q0 >> 5) + odd, rbB = (q0 >> 5) + 1 - odd, cP = 3 * hj + 2 * odd, cQ = 3 * hj + 1;
#pragma unroll
    for (int k = 0; k < 3; ++k) {
      const int rbk = k == 1 ? rbB : rbA, ck = k == 2 ? cQ : cP;
      float g[16];
#pragma unroll
      for (int q = 0; q < 4; ++q) {
        const u32x2 bw = bwv[k == 2 ? 1 : 0][q];
        const float bs[4] = {__uint_as_float(bw[0] << 16), __uint_as_float(bw[0] & 0xffff0000u), __uint_as_float(bw[1] << 16),
                             __uint_as_float(bw[1] & 0xffff0000u)};
#pragma unroll
        for (int e = 0; e < 4; ++e) g[4 * q + e] = gelu_tanh(rbf(rbf(hacc[k][4 * q + e]) + bs[e]));
      }
      u32x4* ho = (u32x4*)hfr + ((size_t)rbk * 96 + 2 * ck) * 64 + lane;
#pragma unroll
      for (int sidx = 0; sidx < 2; ++sidx)
        ho[sidx * 64] = u32x4{cvtpk(g[8 * sidx], g[8 * sidx + 1]), cvtpk(g[8 * sidx + 2], g[8 * sidx + 3]),
                              cvtpk(g[8 * sidx + 4], g[8 * sidx + 5]), cvtpk(g[8 * sidx + 6], g[8 * sidx + 7])};
    }
  }
  SB();
  bf16x8 qf[2][4];
  sfor<24>([&](auto I) {
    constexpr int n = decltype(I)::v, b = n / 12, f = n % 12, R = AO(b, f >> 1) + 8 * (f & 1);
    float t8[8];
    acc_rd8<R>(t8);
    unsigned w0 = cvtpk(t8[0] * QSCALE, t8[1] * QSCALE);
    unsigned w1 = cvtpk(t8[2] * QSCALE, t8[3] * QSCALE);
    unsigned w2 = cvtpk(t8[4] * QSCALE, t8[5] * QSCALE);
    unsigned w3 = cvtpk(t8[6] * QSCALE, t8[7] * QSCALE);
    auto r0 = __builtin_amdgcn_permlane32_swap(w0, w2, false, false);
    auto r1 = __builtin_amdgcn_permlane32_swap(w1, w3, false, false);
    if constexpr (f < 8) {
      acc_wr4<AQ(b, f)>(r0[0], r1[0], r0[1], r1[1]);
    } else {
      u32x4 w = {r0[0], r1[0], r0[1], r1[1]};
      qf[b][f - 8] = __builtin_bit_cast(bf16x8, w);
    }
  });
  zfr = bf16x8{};
  PIN(zfr);
  SB();
  asm volatile("s_nop 7");
  sfor<12>([&](auto I) { acc_zero16<16 * decltype(I)::v>(zfr); });

  float m_run[2], l_run[2] = {0.f, 0.f}, alpha[2];
  bool pend = false;
  f32x16 S[2][2], nm[2];
  u32x4 pw[2][2];
  unsigned kb[4], vb;
  nm[0] = f32x16{}; nm[1] = f32x16{};
  PIN(nm[0]); PIN(nm[1]);
#pragma unroll
  for (int i = 0; i < 4; ++i) { PIN(qf[0][i]); PIN(qf[1][i]); }
#pragma unroll
  for (int i = 0; i < 4; ++i) fr[i] = rdK(koff[i] + 2 * TB + 4096);
  SB();
  asm volatile("s_nop 3" ::: "memory");
  SB();

  sfor<12>([&](auto I) {
    constexpr int i = decltype(I)::v;
    if constexpr (i == 0) { mfma_first<AQ(0, 0)>(S[0][0], fr[0], nm[0]); mfma_first<AQ(1, 0)>(S[0][1], fr[0], nm[1]); }
    else if constexpr (i < 8) { mfma_qa<AQ(0, i)>(S[0][0], fr[i & 3]); mfma_qa<AQ(1, i)>(S[0][1], fr[i & 3]); }
    else { mfma_qv(S[0][0], fr[i & 3], qf[0][i - 8]); mfma_qv(S[0][1], fr[i & 3], qf[1][i - 8]); }
    if constexpr (i + 4 < 12) fr[i & 3] = rdK(koff[i & 3] + 2 * TB + ((i + 4) >> 2) * 8192 + 4096);
    SB();
  });
  asm volatile("s_nop 15\n\ts_nop 7" :: "v"(nm[0]), "v"(nm[1]) : "memory");
  SB();
#pragma unroll
  for (int b = 0; b < 2; ++b) {
    float mx = S[0][b][0];
#pragma unroll
    for (int r = 1; r < 16; ++r) mx = fmaxf(mx, S[0][b][r]);
    auto rr = __builtin_amdgcn_permlane32_swap(__float_as_uint(mx), __float_as_uint(mx), false, false);
    mx = fmaxf(__uint_as_float(rr[0]), __uint_as_float(rr[1]));
    m_run[b] = mx;
#pragma unroll
    for (int r = 0; r < 16; ++r) { nm[b][r] = -mx; S[0][b][r] -= mx; }
  }
#pragma unroll
  for (int j = 0; j < 4; ++j) kb[j] = koff[j];
  vb = voff;
#pragma unroll
  for (int i = 0; i < 4; ++i) fr[i] = rdK(kb[i]);

  auto phaseA = [&](auto XX, auto HH, const char* ssrc, const unsigned sdst) {
    constexpr int X = decltype(XX)::v, HB = decltype(HH)::v;
    sfor<12>([&](auto I) {
      constexpr int i = decltype(I)::v;
      auto ex = [&](auto N) {
        constexpr int n = decltype(N)::v;
        if constexpr (n < 32) {
          constexpr int b = (n & 15) >> 3, r = (n >> 4) * 8 + (n & 7);
          S[X][b][r] = __builtin_amdgcn_exp2f(S[X][b][r]);
        }
      };
      auto pk = [&](auto N) {
        constexpr int n = decltype(N)::v;
        if constexpr (n >= 0 && n < 16) {
          constexpr int b = (n >> 2) & 1, ks = n >> 3, w = n & 3;
          pw[b][ks][w] = cvtpk(S[X][b][8 * ks + 2 * w], S[X][b][8 * ks + 2 * w + 1]);
        }
      };
      if constexpr (i == 0) mfma_first<AQ(0, 0)>(S[X ^ 1][0], fr[0], nm[0]);
      else if constexpr (i < 8) mfma_qa<AQ(0, i)>(S[X ^ 1][0], fr[i & 3]);
      else mfma_qv(S[X ^ 1][0], fr[i & 3], qf[0][i - 8]);
      SB();
      ex(IC<3 * i>{}); ex(IC<3 * i + 1>{});
      pk(IC<2 * i - 8>{});
      PIN(S[X][0]); PIN(S[X][1]);
      SB();
      if constexpr (i == 0) mfma_first<AQ(1, 0)>(S[X ^ 1][1], fr[0], nm[1]);
      else if constexpr (i < 8) mfma_qa<AQ(1, i)>(S[X ^ 1][1], fr[i & 3]);
      else mfma_qv(S[X ^ 1][1], fr[i & 3], qf[1][i - 8]);
      SB();
      if constexpr (i + 4 < 12) fr[i & 3] = rdK(kb[i & 3] + ((i + 4) >> 2) * 8192 + HB * 4096);
      else fr[i & 3] = rdV(vb + ((4 * HB) * 6 + (i - 8)) * 512);
      ex(IC<3 * i + 2>{});
      pk(IC<2 * i - 7>{});
      if constexpr (i == 2) asm volatile("" :: "v"(nm[0]), "v"(nm[1]));
      if constexpr (i == 1 || i == 5 || i == 9) {
        constexpr int pc = (i - 1) / 4;
        if constexpr (HB == 0) glds_i<(pc & 1) * 1024 + (pc >> 1) * 8192>(ssrc, kpo[pc], sdst);
        else glds_i<(pc & 1) * 3072 + (pc >> 1) * 1024>(ssrc, vpo[pc], sdst);
      }
      PIN(S[X][0]); PIN(S[X][1]); PIN(pw[0][0]); PIN(pw[0][1]); PIN(pw[1][0]); PIN(pw[1][1]);
      SB();
    });
  };
  auto phaseB = [&](auto XX, auto HH, auto HK, const char* ssrc, const unsigned sdst) {
    constexpr int X = decltype(XX)::v, HB = decltype(HH)::v, HBKN = decltype(HK)::v;
    float ls[2][2], mx[2];
    auto fill = [&](auto G) {
      constexpr int g = decltype(G)::v;
      if constexpr (g < 16) {
        constexpr int b = g >> 3, r = (g & 7) * 2;
        if constexpr ((g & 7) == 0) { ls[b][0] = S[X][b][r]; ls[b][1] = S[X][b][r + 1]; }
        else { ls[b][0] += S[X][b][r]; ls[b][1] += S[X][b][r + 1]; }
        PIN(ls[b][0]); PIN(ls[b][1]);
      }
      if constexpr (g == 16) { l_run[0] += ls[0][0] + ls[0][1]; PIN(l_run[0]); }
      if constexpr (g == 17) { l_run[1] += ls[1][0] + ls[1][1]; PIN(l_run[1]); }
      if constexpr (g == 20) {
#pragma unroll
        for (int b = 0; b < 2; ++b) {
          auto rr = __builtin_amdgcn_permlane32_swap(__float_as_uint(mx[b]), __float_as_uint(mx[b]), false, false);
          mx[b] = max2f(__uint_as_float(rr[0]), __uint_as_float(rr[1]));
          PIN(mx[b]);
        }
      }
      if constexpr (g == 21) {
        if (__builtin_expect(__any(max2f(mx[0], mx[1]) > THR), 0)) {
          const float aone = hi == 0 ? 1.f : 0.f;
#pragma unroll
          for (int b = 0; b < 2; ++b) {
            const float dl = fmaxf(mx[b], 0.f);
            m_run[b] += dl;
            alpha[b] = __builtin_amdgcn_exp2f(-dl);
            bcast16(nm[b], aone, -m_run[b]);
#pragma unroll
            for (int r = 0; r < 16; ++r) S[X ^ 1][b][r] -= dl;
          }
          pend = true;
        }
      }
      if constexpr (g == 16 || g == 18 || g == 22) {
        constexpr int pc = g == 16 ? 3 : (g == 18 ? 4 : 5);
        if constexpr (HB == 0) glds_i<(pc & 1) * 1024 + (pc >> 1) * 8192>(ssrc, kpo[pc], sdst);
        else glds_i<(pc & 1) * 3072 + (pc >> 1) * 1024>(ssrc, vpo[pc], sdst);
      }
    };
    auto pvm = [&](auto G) {
      constexpr int g = decltype(G)::v, i = g >> 1, b = g & 1, k = (g - 4) >> 1;
      const bf16x8 pb = __builtin_bit_cast(bf16x8, pw[b][i / 6]);
      if constexpr (g >= 4 && g < 20) {
        if constexpr (k == 0) mfma_o_mx<AO(b, i % 6)>(fr[i & 3], pb, mx[b], S[X ^ 1][b][0], S[X ^ 1][b][1], S[X ^ 1][b][2]);
        else if constexpr (k < 7) mfma_o_mx<AO(b, i % 6)>(fr[i & 3], pb, mx[b], mx[b], S[X ^ 1][b][2 * k + 1], S[X ^ 1][b][2 * k + 2]);
        else mfma_o_mx<AO(b, i % 6)>(fr[i & 3], pb, mx[b], mx[b], S[X ^ 1][b][15], S[X ^ 1][b][15]);
      } else mfma_o<AO(b, i % 6)>(fr[i & 3], pb);
    };
    sfor<12>([&](auto I) {
      constexpr int i = decltype(I)::v;
      pvm(IC<2 * i>{});
      SB();
      fill(IC<2 * i>{});
      SB();
      pvm(IC<2 * i + 1>{});
      SB();
      if constexpr (i + 4 < 12) fr[i & 3] = rdV(vb + ((4 * HB + 2 * ((i + 4) / 6)) * 6 + ((i + 4) % 6)) * 512);
      else fr[i & 3] = rdK(kb[i - 8] + HBKN * 4096);
      fill(IC<2 * i + 1>{});
      SB();
    });
    if (__builtin_expect(pend, 0)) {
      asm volatile("s_nop 15\n\ts_nop 7" ::: "memory");
      l_run[0] *= alpha[0]; l_run[1] *= alpha[1];
      sfor<192>([&](auto I) { constexpr int n = decltype(I)::v; acc_scale<n>(alpha[n / 96]); });
      asm volatile("s_nop 3" ::: "memory");
      pend = false;
    }
  };

  asm volatile("s_waitcnt vmcnt(0)" ::: "memory");
  unsigned scur = 0;
#pragma unroll 1
  for (int t = 0; t < 32; ++t) {
    const unsigned snxt = nxt(scur), swr = nxt(snxt);
    asm volatile("s_waitcnt vmcnt(6)\n\ts_barrier" ::: "memory");
    int tk = t + 2 < 32 ? t + 2 : 31;
    const int tv = tk;
    if (tk == 31 && wid >= 2) tk = 30;
    const char* ks_ = kbase + (size_t)(tk * 64 + 32) * (DH * 2);
    const char* vs_ = vbase + (size_t)(tv * 64) * (DH * 2);
    SB();
    phaseA(IC<0>{}, IC<0>{}, ks_, kdst + swr);
    phaseB(IC<0>{}, IC<0>{}, IC<1>{}, ks_, kdst + swr);
    phaseA(IC<1>{}, IC<1>{}, vs_, vdst + swr);
#pragma unroll
    for (int j = 0; j < 4; ++j) kb[j] = koff[j] + snxt;
    phaseB(IC<1>{}, IC<1>{}, IC<0>{}, vs_, vdst + swr);
    vb = voff + snxt;
    scur = snxt;
  }
  asm volatile("s_waitcnt vmcnt(0)\n\ts_nop 15\n\ts_nop 7" ::: "memory");
  int tz = threadIdx.x;
  asm volatile("" : "+v"(tz));
  float rls[2];
#pragma unroll
  for (int b = 0; b < 2; ++b) {
    auto rr = __builtin_amdgcn_permlane32_swap(__float_as_uint(l_run[b]), __float_as_uint(l_run[b]), false, false);
    rls[b] = 1.0f / (__uint_as_float(rr[0]) + __uint_as_float(rr[1]));
  }
  u32x4* ofr = (u32x4*)O + ((size_t)(qb * 8 + (tz >> 6) * 2) * 384 + head * 12) * 64 + (tz & 63);
  sfor<24>([&](auto I) {
    constexpr int n = decltype(I)::v, b = n / 12, f = n % 12, R = AO(b, f >> 1) + 8 * (f & 1);
    const float rl = rls[b];
    float t8[8];
    acc_rd8<R>(t8);
    ofr[(b * 384 + f) * 64] = u32x4{cvtpk(t8[0] * rl, t8[1] * rl), cvtpk(t8[2] * rl, t8[3] * rl),
                                    cvtpk(t8[4] * rl, t8[5] * rl), cvtpk(t8[6] * rl, t8[7] * rl)};
  });
}

extern "C" void kernel_launch(void* const* d_in, const int* in_sizes, int n_in, void* d_out, int out_size,
                              void* d_ws, size_t ws_size, hipStream_t stream) {
  const u16* x = (const u16*)d_in[0];
  const u16* kc = (const u16*)d_in[1];
  const u16* vc = (const u16*)d_in[2];
  const u16* wq = (const u16*)d_in[3];
  const u16* wo = (const u16*)d_in[4];
  const u16* w1 = (const u16*)d_in[6];
  const u16* b1 = (const u16*)d_in[7];
  const u16* w2 = (const u16*)d_in[8];
  const u16* b2 = (const u16*)d_in[9];
  char* ws = (char*)d_ws;
  u16* xnp = (u16*)(ws);
  u16* y = (u16*)(ws + (4u << 20));
  u16* h = (u16*)(ws + (8u << 20));
  u16* O = (u16*)(ws + (16u << 20));
  float* part = (float*)(ws + (48u << 20));

  hipFuncSetAttribute((const void*)k_attn, hipFuncAttributeMaxDynamicSharedMemorySize, 6 * TB);
  hipFuncSetAttribute((const void*)k_gemm2, hipFuncAttributeMaxDynamicSharedMemorySize, 4 * 16384);

  k_ln<<<SEQ / 4, 256, 0, stream>>>(x, xnp, y);
  k_attn<<<256, 256, 6 * TB, stream>>>(xnp, wq, kc, vc, O, y, w1, b1, h);
  k_gemm2<<<200, 256, 4 * 16384, stream>>>(O, wo, h, w2, part);
  k_final<<<SEQ * DM / 1024, 256, 0, stream>>>(x, part, b2, (u16*)d_out);
}
```

```cpp
#include <hip/hip_runtime.h>
#include <hip/hip_bf16.h>
#include <stdint.h>

typedef __attribute__((ext_vector_type(8))) short bf16x8;
typedef __attribute__((ext_vector_type(8))) __bf16 bf16x8b;
typedef __attribute__((ext_vector_type(4))) short s16x4;
typedef __attribute__((ext_vector_type(16))) float f32x16;
typedef __attribute__((ext_vector_type(4))) float f32x4;
typedef __attribute__((ext_vector_type(2))) float f32x2;
typedef __attribute__((ext_vector_type(2))) __bf16 bf16x2b;
typedef __attribute__((ext_vector_type(4))) unsigned u32x4;
typedef __attribute__((ext_vector_type(2))) unsigned u32x2;
typedef __attribute__((address_space(3))) char* lds_ptr;
typedef unsigned short u16;

#define DI __device__ __forceinline__

constexpr int SEQ = 2048, DM = 640, NH = 32, DH = 192, FF = 1536, QD = NH * DH;

DI f32x16 MFMA(bf16x8 a, bf16x8 b, f32x16 c) {
  return __builtin_amdgcn_mfma_f32_32x32x16_bf16(__builtin_bit_cast(bf16x8b, a), __builtin_bit_cast(bf16x8b, b), c, 0, 0, 0);
}
DI unsigned cvtpk(float lo, float hi) {
  f32x2 v = {lo, hi};
  bf16x2b b = __builtin_convertvector(v, bf16x2b);
  return __builtin_bit_cast(unsigned, b);
}
DI float bf2f(u16 v) { return __uint_as_float(((unsigned)v) << 16); }
DI float rbf(float v) { return __uint_as_float(cvtpk(v, 0.f) << 16); }
DI s16x4 trread(lds_ptr p) {
  return __builtin_amdgcn_ds_read_tr16_b64_v4i16((__attribute__((address_space(3))) s16x4*)p);
}
DI bf16x8 ldsread(lds_ptr p) { return *(__attribute__((address_space(3))) bf16x8*)p; }
DI bf16x8 cat(s16x4 a, s16x4 b) { return __builtin_shufflevector(a, b, 0, 1, 2, 3, 4, 5, 6, 7); }
DI void glds16(const void* g, lds_ptr l) {
  __builtin_amdgcn_global_load_lds((const __attribute__((address_space(1))) unsigned*)g,
                                   (__attribute__((address_space(3))) unsigned*)l, 16, 0, 0);
}

DI float wave_sum(float v) {
#pragma unroll
  for (int o = 32; o > 0; o >>= 1) v += __shfl_xor(v, o, 64);
  return v;
}
DI void ln_row(float (&v)[10]) {
  float s = 0.f;
#pragma unroll
  for (int i = 0; i < 10; ++i) s += v[i];
  s = wave_sum(s);
  const float mean = s * (1.0f / DM);
  float q = 0.f;
#pragma unroll
  for (int i = 0; i < 10; ++i) { float d = v[i] - mean; q += d * d; }
  q = wave_sum(q);
  const float mb = rbf(mean), vb = rbf(q * (1.0f / DM));
  const float den = rbf(sqrtf(rbf(vb + 1e-5f)));
#pragma unroll
  for (int i = 0; i < 10; ++i) v[i] = rbf(rbf(v[i] - mb) / den);
}
__global__ __launch_bounds__(256) void k_ln(const u16* __restrict__ x, u16* __restrict__ xnp, u16* __restrict__ y) {
  const int row = blockIdx.x * 4 + (threadIdx.x >> 6), lane = threadIdx.x & 63;
  const unsigned* xr = (const unsigned*)(x + (size_t)row * DM);
  float v[10];
#pragma unroll
  for (int i = 0; i < 5; ++i) {
    unsigned w = xr[lane + 64 * i];
    v[2 * i] = __uint_as_float(w << 16);
    v[2 * i + 1] = __uint_as_float(w & 0xffff0000u);
  }
  ln_row(v);
  unsigned* xo = (unsigned*)xnp + (size_t)(row >> 5) * (40 * 64 * 4) + (row & 31) * 4;
#pragma unroll
  for (int i = 0; i < 5; ++i) {
    int e = lane + 64 * i;
    int ep = (e & ~6) | ((e & 2) << 1) | ((e & 4) >> 1);
    xo[(ep >> 3) * 256 + ((ep >> 2) & 1) * 128 + (ep & 3)] =
        (__float_as_uint(v[2 * i]) >> 16) | (__float_as_uint(v[2 * i + 1]) & 0xffff0000u);
  }
  ln_row(v);
  unsigned* yo = (unsigned*)y + (size_t)(row >> 5) * (40 * 64 * 4) + (row & 31) * 4;
#pragma unroll
  for (int i = 0; i < 5; ++i) {
    int e = lane + 64 * i;
    int ep = (e & ~6) | ((e & 2) << 1) | ((e & 4) >> 1);
    yo[(ep >> 3) * 256 + ((ep >> 2) & 1) * 128 + (ep & 3)] =
        (__float_as_uint(v[2 * i]) >> 16) | (__float_as_uint(v[2 * i + 1]) & 0xffff0000u);
  }
}

#define SB() __builtin_amdgcn_sched_barrier(0)
#define PIN(x) asm volatile("" : "+v"(x))
template <int I> struct IC { static constexpr int v = I; };
template <int N, class F> DI void sfor(F&& f) {
  if constexpr (N > 0) { sfor<N - 1>(f); f(IC<N - 1>{}); }
}
template <int IMM> DI void glds_i(const void* sbase, unsigned voff, unsigned ldsbase) {
  asm volatile("s_add_i32 m0, %2, %3\n\ts_nop 0\n\tglobal_load_lds_dwordx4 %0, %1"
               :: "v"(voff), "s"(sbase), "s"(ldsbase), "i"(IMM) : "memory", "scc");
}
template <int OFF> DI void gload16(bf16x8& d, const char* p) {
  asm volatile("global_load_dwordx4 %0, %1, off offset:%2" : "=&v"(d) : "v"(p), "i"(OFF) : "memory");
}
template <int OFF> DI void gload8(u32x2& d, const void* p) {
  asm volatile("global_load_dwordx2 %0, %1, off offset:%2" : "=&v"(d) : "v"(p), "i"(OFF) : "memory");
}
template <int N> DI void xwait2(bf16x8 (&x)[2][4]) {
  asm volatile("s_waitcnt vmcnt(%8)\n\ts_barrier" : "+v"(x[0][0]), "+v"(x[0][1]), "+v"(x[0][2]), "+v"(x[0][3]), "+v"(x[1][0]), "+v"(x[1][1]), "+v"(x[1][2]), "+v"(x[1][3]) : "i"(N) : "memory");
}
DI void xpin2(bf16x8 (&x)[2][4]) {
  asm volatile("" : "+v"(x[0][0]), "+v"(x[0][1]), "+v"(x[0][2]), "+v"(x[0][3]), "+v"(x[1][0]), "+v"(x[1][1]), "+v"(x[1][2]), "+v"(x[1][3]));
}

DI void wgemm24(const char* wbase, unsigned ldwb, const char* xp, unsigned xrbs, int ktiles, unsigned lds0,
                f32x16 (&acc)[2][4]) {
  constexpr int NB = 4;
  constexpr unsigned TBY = 4096 * NB;
  const int tid = threadIdx.x, lane = tid & 63, wid = __builtin_amdgcn_readfirstlane(tid >> 6);
  const int hi = lane >> 5;
  unsigned wpo[NB];
#pragma unroll
  for (int i = 0; i < NB; ++i) {
    const int sub = 2 * (wid * NB + i) + hi, kg = sub / NB, nb = sub % NB;
    wpo[i] = (unsigned)(8 * kg + ((lane >> 2) & 7)) * ldwb + nb * 64 + (lane & 3) * 16;
  }
  const unsigned wdst = lds0 + wid * NB * 1024;
  const unsigned voff = lds0 + (hi * 4 + ((lane >> 2) & 3)) * 64 + ((lane >> 4) & 1) * 32 + (lane & 3) * 8;
  auto rdW = [&](unsigned a) { return cat(trread((lds_ptr)(uintptr_t)a), trread((lds_ptr)(uintptr_t)(a + NB * 512))); };
#pragma unroll
  for (int r = 0; r < 2; ++r)
#pragma unroll
    for (int n = 0; n < NB; ++n) acc[r][n] = f32x16{};
  bf16x8 x0[2][4], x1[2][4], x2[2][4], x3[2][4], fr[4];
  const size_t wts = (size_t)64 * ldwb;
  auto issue = [&](bf16x8 (&x)[2][4], int t, auto SLOT) {
    const char* xq = xp + (size_t)t * 4096;
    sfor<8>([&](auto I) { constexpr int i = decltype(I)::v; gload16<(i & 3) * 1024>(x[i >> 2][i & 3], xq + (i >> 2) * xrbs); });
    sfor<NB>([&](auto I) { glds_i<decltype(SLOT)::v * TBY + decltype(I)::v * 1024>(wbase + (size_t)t * wts, wpo[decltype(I)::v], wdst); });
  };
  issue(x0, 0, IC<0>{});
  issue(x1, 1, IC<1>{});
  issue(x2, 2, IC<2>{});
  asm volatile("s_waitcnt vmcnt(24)\n\ts_barrier" ::: "memory");
#pragma unroll
  for (int i = 0; i < 4; ++i) fr[i] = rdW(voff + i * 512);
  auto step = [&](bf16x8 (&xc)[2][4], bf16x8 (&xn)[2][4], const int t, auto SLOT) {
    constexpr int sl = decltype(SLOT)::v, sw = (sl + 3) & 3;
    xwait2<12>(xc);
    const unsigned vb = voff + sl * TBY, vbn = voff + ((sl + 1) & 3) * TBY;
    const int tn = t + 3 < ktiles ? t + 3 : ktiles - 1;
    const char* xq = xp + (size_t)tn * 4096;
    const char* wq_ = wbase + (size_t)tn * wts;
    SB();
    sfor<16>([&](auto I) {
      constexpr int i = decltype(I)::v;
      acc[0][i & 3] = MFMA(fr[i & 3], xc[0][i >> 2], acc[0][i & 3]);
      acc[1][i & 3] = MFMA(fr[i & 3], xc[1][i >> 2], acc[1][i & 3]);
      SB();
      if constexpr (i + 4 < 16) fr[i & 3] = rdW(vb + ((2 * ((i + 4) >> 2)) * NB + (i & 3)) * 512);
      else fr[i & 3] = rdW(vbn + (i & 3) * 512);
      if constexpr (i < 8) gload16<(i & 3) * 1024>(xn[i >> 2][i & 3], xq + (i >> 2) * xrbs);
      if constexpr (i >= 8 && i < 12) glds_i<sw * TBY + (i - 8) * 1024>(wq_, wpo[i - 8], wdst);
      SB();
    });
  };
#pragma unroll 1
  for (int t = 0; t < ktiles; t += 4) {
    step(x0, x3, t, IC<0>{});
    step(x1, x0, t + 1, IC<1>{});
    step(x2, x1, t + 2, IC<2>{});
    step(x3, x2, t + 3, IC<3>{});
  }
  asm volatile("s_waitcnt vmcnt(0)" ::: "memory");
  xpin2(x0); xpin2(x1); xpin2(x2);
}
DI int crow(int r, int hi) { return (r & 3) + 8 * (r >> 2) + 4 * hi; }

DI float gelu_tanh(float v) {
  const float u = 0.7978845608028654f * (v + 0.044715f * v * v * v);
  const float e = __builtin_amdgcn_exp2f(2.8853900817779268f * u);
  return v - v * __builtin_amdgcn_rcpf(e + 1.f);
}

__global__ __launch_bounds__(256, 1) void k_gemm2(const u16* __restrict__ O, const u16* __restrict__ wo,
                                                  const u16* __restrict__ h, const u16* __restrict__ w2,
                                                  u16* __restrict__ part) {
  extern __shared__ __attribute__((aligned(16))) char smem[];
  const int bid = blockIdx.x, x = bid & 7, r = bid >> 3;
  const int nt = r % 5, c = r < 20 ? (x >> 1) : 4, mt = r < 20 ? 4 * (x & 1) + r / 5 : x;
  const int lane = threadIdx.x & 63, wid = threadIdx.x >> 6;
  const int rb0 = mt * 8 + wid * 2;
  const char* W; const char* X; unsigned xrbs;
  if (c < 4) {
    W = (const char*)(wo + (size_t)c * 24 * 64 * DM + nt * 128);
    xrbs = 384 * 1024;
    X = (const char*)O + ((size_t)rb0 * 384 + c * 96) * 1024 + lane * 16;
  } else {
    W = (const char*)(w2 + nt * 128);
    xrbs = 96 * 1024;
    X = (const char*)h + (size_t)rb0 * 96 * 1024 + lane * 16;
  }
  f32x16 acc[2][4];
  wgemm24(W, DM * 2, X, xrbs, 24, (unsigned)(uintptr_t)(lds_ptr)smem, acc);
  u32x4* P = (u32x4*)part + (size_t)c * (SEQ * DM / 8);
#pragma unroll
  for (int b = 0; b < 2; ++b)
#pragma unroll
    for (int nb = 0; nb < 4; ++nb)
#pragma unroll
      for (int hq = 0; hq < 2; ++hq)
        P[(((size_t)(rb0 + b) * 20 + nt * 4 + nb) * 2 + hq) * 64 + lane] =
            u32x4{cvtpk(acc[b][nb][8 * hq], acc[b][nb][8 * hq + 1]), cvtpk(acc[b][nb][8 * hq + 2], acc[b][nb][8 * hq + 3]),
                  cvtpk(acc[b][nb][8 * hq + 4], acc[b][nb][8 * hq + 5]), cvtpk(acc[b][nb][8 * hq + 6], acc[b][nb][8 * hq + 7])};
}

DI float blo(unsigned w) { return __uint_as_float(w << 16); }
DI float bhi(unsigned w) { return __uint_as_float(w & 0xffff0000u); }
__global__ __launch_bounds__(256) void k_final(const u16* __restrict__ x, const u16* __restrict__ part,
                                               const u16* __restrict__ b2, u16* __restrict__ out) {
  const int idx = blockIdx.x * 256 + threadIdx.x;
  constexpr size_t PS = (size_t)SEQ * DM / 8;
  const u32x4* P = (const u32x4*)part + idx;
  const u32x4 p0 = P[0], p1 = P[PS], p2 = P[2 * PS], p3 = P[3 * PS], pm = P[4 * PS];
  const int lane = idx & 63, hq = (idx >> 6) & 1, blk = idx >> 7, cb = blk % 20, rb = blk / 20;
  const int m = rb * 32 + (lane & 31), n0 = cb * 32 + 16 * hq + 4 * (lane >> 5);
#pragma unroll
  for (int q = 0; q < 2; ++q) {
    const int n = n0 + 8 * q;
    const u32x2 xw = *(const u32x2*)(x + (size_t)m * DM + n);
    const u32x2 bw = *(const u32x2*)(b2 + n);
    float r[4];
#pragma unroll
    for (int i = 0; i < 4; ++i) {
      const int w = 2 * q + (i >> 1);
      const float a0 = (i & 1) ? bhi(p0[w]) : blo(p0[w]), a1 = (i & 1) ? bhi(p1[w]) : blo(p1[w]);
      const float a2 = (i & 1) ? bhi(p2[w]) : blo(p2[w]), a3 = (i & 1) ? bhi(p3[w]) : blo(p3[w]);
      const float am = (i & 1) ? bhi(pm[w]) : blo(pm[w]);
      const float xo = (i & 1) ? bhi(xw[i >> 1]) : blo(xw[i >> 1]), bo = (i & 1) ? bhi(bw[i >> 1]) : blo(bw[i >> 1]);
      const float sv = rbf((a0 + a1) + (a2 + a3));
      const float mm = rbf(am + bo);
      const float g = rbf(mm / (1.f + __expf(-mm)));
      r[i] = xo + rbf(sv * g);
    }
    *(u32x2*)(out + (size_t)m * DM + n) = u32x2{cvtpk(r[0], r[1]), cvtpk(r[2], r[3])};
  }
}

constexpr int TB = 24576;
constexpr float QSCALE = 0.07216878364870322f * 1.4426950408889634f;
constexpr float THR = 20.0f;

DI void glds_s(const void* sbase, unsigned voff, unsigned ldsaddr) {
  asm volatile("s_mov_b32 m0, %2\n\ts_nop 0\n\tglobal_load_lds_dwordx4 %0, %1"
               :: "v"(voff), "s"(sbase), "s"(ldsaddr) : "memory");
}
DI float max3f(float a, float b, float c) { float r; asm("v_max3_f32 %0, %1, %2, %3" : "=v"(r) : "v"(a), "v"(b), "v"(c)); return r; }
DI float max2f(float a, float b) { float r; asm("v_max_f32_e32 %0, %1, %2" : "=v"(r) : "v"(a), "v"(b)); return r; }
DI void bcast16(f32x16& d, float aone, float negm) {
  asm volatile("s_nop 1\n\tv_mfma_f32_32x32x2_f32 %0, %1, %2, 0" : "+v"(d) : "v"(aone), "v"(negm));
}
DI bf16x8 rdK(unsigned a) { return ldsread((lds_ptr)(uintptr_t)a); }
DI bf16x8 rdV(unsigned a) { return cat(trread((lds_ptr)(uintptr_t)a), trread((lds_ptr)(uintptr_t)(a + 3072))); }
DI unsigned nxt(unsigned s) { return s == 2 * TB ? 0 : s + TB; }
#define ACLOB "a0","a1","a2","a3","a4","a5","a6","a7","a8","a9","a10","a11","a12","a13","a14","a15","a16","a17","a18","a19","a20","a21","a22","a23","a24","a25","a26","a27","a28","a29","a30","a31","a32","a33","a34","a35","a36","a37","a38","a39","a40","a41","a42","a43","a44","a45","a46","a47","a48","a49","a50","a51","a52","a53","a54","a55","a56","a57","a58","a59","a60","a61","a62","a63","a64","a65","a66","a67","a68","a69","a70","a71","a72","a73","a74","a75","a76","a77","a78","a79","a80","a81","a82","a83","a84","a85","a86","a87","a88","a89","a90","a91","a92","a93","a94","a95","a96","a97","a98","a99","a100","a101","a102","a103","a104","a105","a106","a107","a108","a109","a110","a111","a112","a113","a114","a115","a116","a117","a118","a119","a120","a121","a122","a123","a124","a125","a126","a127","a128","a129","a130","a131","a132","a133","a134","a135","a136","a137","a138","a139","a140","a141","a142","a143","a144","a145","a146","a147","a148","a149","a150","a151","a152","a153","a154","a155","a156","a157","a158","a159","a160","a161","a162","a163","a164","a165","a166","a167","a168","a169","a170","a171","a172","a173","a174","a175","a176","a177","a178","a179","a180","a181","a182","a183","a184","a185","a186","a187","a188","a189","a190","a191","a192","a193","a194","a195","a196","a197","a198","a199","a200","a201","a202","a203","a204","a205","a206","a207","a208","a209","a210","a211","a212","a213","a214","a215","a216","a217","a218","a219","a220","a221","a222","a223","a224","a225","a226","a227","a228","a229","a230","a231","a232","a233","a234","a235","a236","a237","a238","a239","a240","a241","a242","a243","a244","a245","a246","a247","a248","a249","a250","a251","a252","a253","a254","a255"
constexpr int AO(int b, int db) { return 16 * (6 * b + db); }
constexpr int AQ(int b, int i) { return 192 + 32 * b + 4 * i; }
template <int R> DI void mfma_o(const bf16x8& a, const bf16x8& b) {
  asm volatile("v_mfma_f32_32x32x16_bf16 a[%c2:%c3], %0, %1, a[%c2:%c3]" :: "v"(a), "v"(b), "i"(R), "i"(R + 15) : ACLOB);
}
template <int R> DI void mfma_o_mx(const bf16x8& a, const bf16x8& b, float& m, float x, float y, float z) {
  asm volatile("v_mfma_f32_32x32x16_bf16 a[%c6:%c7], %1, %2, a[%c6:%c7]\n\tv_max3_f32 %0, %3, %4, %5"
               : "=v"(m) : "v"(a), "v"(b), "v"(x), "v"(y), "v"(z), "i"(R), "i"(R + 15) : ACLOB);
}
template <int R> DI void mfma_first(f32x16& d, const bf16x8& a, const f32x16& c) {
  asm volatile("v_mfma_f32_32x32x16_bf16 %0, %1, a[%c3:%c4], %2" : "=&v"(d) : "v"(a), "v"(c), "i"(R), "i"(R + 3) : ACLOB);
}
template <int R> DI void mfma_qa(f32x16& d, const bf16x8& a) {
  asm volatile("v_mfma_f32_32x32x16_bf16 %0, %1, a[%c2:%c3], %0" : "+v"(d) : "v"(a), "i"(R), "i"(R + 3) : ACLOB);
}
DI void mfma_qv(f32x16& d, const bf16x8& a, const bf16x8& b) {
  asm volatile("v_mfma_f32_32x32x16_bf16 %0, %1, %2, %0" : "+v"(d) : "v"(a), "v"(b));
}
template <int R> DI float acc_rd() { float r; asm volatile("v_accvgpr_read_b32 %0, a[%c1]" : "=v"(r) : "i"(R)); return r; }
template <int R> DI void acc_wr(unsigned v) { asm volatile("v_accvgpr_write_b32 a[%c1], %0" :: "v"(v), "i"(R) : ACLOB); }
template <int R> DI void acc_zero() { asm volatile("v_accvgpr_write_b32 a[%c0], 0" :: "i"(R) : ACLOB); }
template <int R> DI void acc_zero16(const bf16x8& z) {
  asm volatile("v_mfma_f32_32x32x16_bf16 a[%c1:%c2], %0, %0, 0" :: "v"(z), "i"(R), "i"(R + 15) : ACLOB);
}
template <int R> DI void acc_rd8(float (&r)[8]) {
  asm volatile("v_accvgpr_read_b32 %0, a[%c8]\n\tv_accvgpr_read_b32 %1, a[%c9]\n\tv_accvgpr_read_b32 %2, a[%c10]\n\tv_accvgpr_read_b32 %3, a[%c11]\n\t"
               "v_accvgpr_read_b32 %4, a[%c12]\n\tv_accvgpr_read_b32 %5, a[%c13]\n\tv_accvgpr_read_b32 %6, a[%c14]\n\tv_accvgpr_read_b32 %7, a[%c15]"
               : "=v"(r[0]), "=v"(r[1]), "=v"(r[2]), "=v"(r[3]), "=v"(r[4]), "=v"(r[5]), "=v"(r[6]), "=v"(r[7])
               : "i"(R), "i"(R + 1), "i"(R + 2), "i"(R + 3), "i"(R + 4), "i"(R + 5), "i"(R + 6), "i"(R + 7));
}
template <int R> DI void acc_wr4(unsigned a, unsigned b, unsigned c, unsigned d) {
  asm volatile("v_accvgpr_write_b32 a[%c4], %0\n\tv_accvgpr_write_b32 a[%c5], %1\n\tv_accvgpr_write_b32 a[%c6], %2\n\tv_accvgpr_write_b32 a[%c7], %3"
               :: "v"(a), "v"(b), "v"(c), "v"(d), "i"(R), "i"(R + 1), "i"(R + 2), "i"(R + 3) : ACLOB);
}
template <int R> DI void acc_scale(float f) {
  float t;
  asm volatile("v_accvgpr_read_b32 %0, a[%c2]\n\tv_mul_f32 %0, %1, %0\n\tv_accvgpr_write_b32 a[%c2], %0" : "=&v"(t) : "v"(f), "i"(R) : ACLOB);
}

__global__ __launch_bounds__(256, 1) void k_attn(const u16* __restrict__ xnp, const u16* __restrict__ wq,
                                                 const u16* __restrict__ Kc, const u16* __restrict__ Vc,
                                                 u16* __restrict__ O, const u16* __restrict__ yfr,
                                                 const u16* __restrict__ w1, const u16* __restrict__ b1,
                                                 u16* __restrict__ hfr) {
  extern __shared__ __attribute__((aligned(16))) char smem[];
  const unsigned lds0 = (unsigned)(uintptr_t)(lds_ptr)smem;
  const int tid = threadIdx.x, lane = tid & 63, wid = __builtin_amdgcn_readfirstlane(tid >> 6);
  const int r32 = lane & 31, hi = lane >> 5;
  const int bid = blockIdx.x, xcd = bid & 7, idx = bid >> 3;
  const int head = xcd * 4 + (idx >> 3), qb = idx & 7;
  const int q0 = qb * 256 + wid * 64;

  const int krl = lane >> 3;
  const unsigned kvo0 = (unsigned)(krl * DH * 2 + (((lane & 7) ^ (krl >> 1)) << 4));
  const unsigned kvo1 = (unsigned)(krl * DH * 2 + (((lane & 7) ^ (4 + (krl >> 1))) << 4));
  const unsigned vvo = (unsigned)(((lane >> 2) & 7) * DH * 2 + (lane >> 5) * 64 + (lane & 3) * 16);
  const unsigned wvo = (unsigned)(((lane >> 2) & 7) * QD * 2 + (lane >> 5) * 64 + (lane & 3) * 16);
  const char* kbase = (const char*)(Kc + (size_t)head * SEQ * DH) + (size_t)(16 * wid) * DH * 2;
  const char* vbase = (const char*)(Vc + (size_t)head * SEQ * DH) + (size_t)(16 * wid) * DH * 2;
  const char* wbase = (const char*)(wq + head * DH) + (size_t)(16 * wid) * QD * 2;
  const unsigned kdst = lds0 + wid * 2048, vdst = lds0 + 3 * TB + wid * 6144;
  auto pieceK = [&](int key0, unsigned slot, int pc) {
    glds_s(kbase + (size_t)key0 * (DH * 2) + (pc & 1) * (8 * DH * 2) + (pc >> 1) * 128, (pc & 1) ? kvo1 : kvo0,
           kdst + slot + (pc & 1) * 1024 + (pc >> 1) * 8192);
  };
  auto pieceV = [&](int key0, unsigned slot, int pc) {
    glds_s(vbase + (size_t)key0 * (DH * 2) + (pc & 1) * (8 * DH * 2) + (pc >> 1) * 128, vvo,
           vdst + slot + (pc & 1) * 3072 + (pc >> 1) * 1024);
  };
  auto pieceW = [&](int t, unsigned slot, int pc) {
    glds_s(wbase + (size_t)t * (64 * QD * 2) + (size_t)(pc & 1) * (8 * QD * 2) + (pc >> 1) * 128, wvo,
           vdst + slot + (pc & 1) * 3072 + (pc >> 1) * 1024);
  };
  unsigned kpo[6], vpo[6];
#pragma unroll
  for (int pc = 0; pc < 6; ++pc) {
    kpo[pc] = ((pc & 1) ? kvo1 : kvo0) + (pc & 1) * (8 * DH * 2) + (pc >> 1) * 128;
    vpo[pc] = vvo + (pc & 1) * (8 * DH * 2) + (pc >> 1) * 128;
  }
  unsigned koff[4];
#pragma unroll
  for (int j = 0; j < 4; ++j) koff[j] = lds0 + r32 * 128 + (((2 * j + hi) ^ ((r32 >> 1) & 7)) << 4);
  const unsigned voff = lds0 + 3 * TB + (hi * 4 + ((lane >> 2) & 3)) * 64 + ((lane >> 4) & 1) * 32 + (lane & 3) * 8;

  bf16x8 zfr = bf16x8{};
  PIN(zfr);
  asm volatile("s_nop 3");
  sfor<12>([&](auto I) { acc_zero16<16 * decltype(I)::v>(zfr); });
  bf16x8 fr[4];

  const int odd = head & 1, hj = head >> 1;
  const char* xsrc0 = (const char*)xnp + ((size_t)(q0 >> 5) * (40 * 64) + lane) * 16;
  const char* ysrcA = (const char*)yfr + ((size_t)((q0 >> 5) + odd) * (40 * 64) + lane) * 16;
  const char* ysrcB = (const char*)yfr + ((size_t)((q0 >> 5) + 1 - odd) * (40 * 64) + lane) * 16;
  const char* w1base = (const char*)(w1 + (3 * hj + odd) * 32) + (size_t)(16 * wid) * (FF * 2);
  const unsigned w1vo = (unsigned)(((lane >> 2) & 7) * FF * 2 + (lane >> 5) * 64 + (lane & 3) * 16);
  const unsigned w1dst = lds0 + TB + wid * 2048;
  const unsigned w1rd = lds0 + TB + (hi * 4 + ((lane >> 2) & 3)) * 64 + ((lane >> 4) & 1) * 32 + (lane & 3) * 8;
  const unsigned w1P = w1rd + (odd ? 512 : 0), w1Q = w1rd + (odd ? 0 : 512);
  auto rdH = [&](unsigned a) { return cat(trread((lds_ptr)(uintptr_t)a), trread((lds_ptr)(uintptr_t)(a + 1024))); };
  u32x2 bwv[2][4];
  {
    const u16* bp = b1 + (3 * hj + 2 * odd) * 32 + 4 * hi;
    const u16* bq = b1 + (3 * hj + 1) * 32 + 4 * hi;
    sfor<8>([&](auto I) {
      constexpr int i = decltype(I)::v;
      gload8<(i & 3) * 16>(bwv[i >> 2][i & 3], i < 4 ? bp : bq);
    });
  }
  bf16x8 xa[4][4], xb[4][4];
  f32x16 hacc[3];
  hacc[0] = f32x16{}; hacc[1] = f32x16{}; hacc[2] = f32x16{};
  PIN(hacc[0]); PIN(hacc[1]); PIN(hacc[2]);
#pragma unroll
  for (int pc = 0; pc < 6; ++pc) pieceW(0, 2 * TB, pc);
#pragma unroll
  for (int pc = 0; pc < 6; ++pc) pieceW(1, 0, pc);
#pragma unroll
  for (int t = 0; t < 2; ++t)
#pragma unroll
    for (int g = 0; g < 2; ++g)
      glds_s(w1base + (size_t)t * (64 * FF * 2) + (size_t)g * (8 * FF * 2), w1vo, w1dst + (t == 0 ? 2 : 0) * 8192 + g * 1024);
  sfor<16>([&](auto I) {
    constexpr int i = decltype(I)::v;
    gload16<(i & 3) * 1024>(xa[i >> 2][i & 3], (i >> 2) == 0 ? xsrc0 : (i >> 2) == 1 ? xsrc0 + 40960 : (i >> 2) == 2 ? ysrcA : ysrcB);
  });
  if (wid >= 2) {
#pragma unroll
    for (int pc = 0; pc < 6; ++pc) pieceK(-32, 2 * TB, pc);
  }
#pragma unroll
  for (int pc = 0; pc < 6; ++pc) pieceK(32, 0, pc);
#define XOPS(x) "+v"(x[0][0]), "+v"(x[0][1]), "+v"(x[0][2]), "+v"(x[0][3]), "+v"(x[1][0]), "+v"(x[1][1]), "+v"(x[1][2]), "+v"(x[1][3]), \
                "+v"(x[2][0]), "+v"(x[2][1]), "+v"(x[2][2]), "+v"(x[2][3]), "+v"(x[3][0]), "+v"(x[3][1]), "+v"(x[3][2]), "+v"(x[3][3])
  {
    unsigned srd = 2 * TB, swr = TB;
    auto qstep = [&](bf16x8 (&xc)[4][4], bf16x8 (&xn)[4][4], const int t) {
      asm volatile("s_waitcnt vmcnt(8)\n\ts_barrier" : XOPS(xc) :: "memory");
      const unsigned vb = voff + srd;
      const unsigned hP = w1P + srd / 3, hQ = w1Q + srd / 3;
#pragma unroll
      for (int i = 0; i < 4; ++i) fr[i] = rdV(vb + i * 512);
      const int tn = t + 1 < 10 ? t + 1 : 9, tw = t + 2 < 10 ? t + 2 : 9;
      const char* p0 = xsrc0 + tn * 4096;
      const char* p2 = ysrcA + tn * 4096;
      const char* p3 = ysrcB + tn * 4096;
      SB();
      const bool isw = t + 2 < 10;
      const char* ssrc = isw ? wbase + (size_t)(t + 2) * (64 * QD * 2) : vbase + (size_t)(t - 8) * (64 * DH * 2);
      const unsigned sgs = isw ? 8 * QD * 2 : 8 * DH * 2;
      const unsigned svo = isw ? wvo : vvo;
      const char* w1s = w1base + (size_t)tw * (64 * FF * 2);
      auto rdN = [&](auto N) {
        constexpr int n = decltype(N)::v, ks = n >> 3, g = n & 7;
        if constexpr (g < 6) return rdV(vb + ((2 * ks) * 6 + g) * 512);
        else if constexpr (g == 6) return rdH(hP + (4 * ks) * 512);
        else return rdH(hQ + (4 * ks) * 512);
      };
      sfor<32>([&](auto I) {
        constexpr int i = decltype(I)::v, ks = i >> 3, g = i & 7;
        if constexpr (g < 6) { mfma_o<AO(0, g)>(fr[i & 3], xc[0][ks]); mfma_o<AO(1, g)>(fr[i & 3], xc[1][ks]); }
        else if constexpr (g == 6) { mfma_qv(hacc[0], fr[i & 3], xc[2][ks]); mfma_qv(hacc[1], fr[i & 3], xc[3][ks]); }
        else mfma_qv(hacc[2], fr[i & 3], xc[2][ks]);
        if constexpr (i + 4 < 32) fr[i & 3] = rdN(IC<(i + 4 < 32 ? i + 4 : 0)>{});
        if constexpr (i < 16)
          gload16<(i & 3) * 1024>(xn[i >> 2][i & 3], (i >> 2) == 0 ? p0 : (i >> 2) == 1 ? p0 + 40960 : (i >> 2) == 2 ? p2 : p3);
        if constexpr (i >= 16 && i < 22) {
          constexpr int pc = i - 16;
          glds_s(ssrc + (size_t)(pc & 1) * sgs + (pc >> 1) * 128, svo, vdst + swr + (pc & 1) * 3072 + (pc >> 1) * 1024);
        }
        if constexpr (i >= 22 && i < 24) glds_s(w1s + (size_t)(i - 22) * (8 * FF * 2), w1vo, w1dst + swr / 3 + (i - 22) * 1024);
        SB();
      });
      srd = nxt(srd);
      swr = nxt(swr);
    };
    asm volatile("s_waitcnt vmcnt(0)" : XOPS(xa) :: "memory");
    asm volatile("" : "+v"(bwv[0][0]), "+v"(bwv[0][1]), "+v"(bwv[0][2]), "+v"(bwv[0][3]), "+v"(bwv[1][0]), "+v"(bwv[1][1]), "+v"(bwv[1][2]), "+v"(bwv[1][3]));
#pragma unroll 1
    for (int t = 0; t < 10; t += 2) { qstep(xa, xb, t); qstep(xb, xa, t + 1); }
    asm volatile("s_waitcnt vmcnt(0)\n\ts_barrier" : XOPS(xa) :: "memory");
  }
#pragma unroll
  for (int pc = 0; pc < 6; ++pc) pieceK(96, TB, pc);
  asm volatile("s_nop 15\n\ts_nop 7" ::: "memory");
  SB();
  {
    const int rbA = (q0 >> 5) + odd, rbB = (q0 >> 5) + 1 - odd, cP = 3 * hj + 2 * odd, cQ = 3 * hj + 1;
#pragma unroll
    for (int k = 0; k < 3; ++k) {
      const int rbk = k == 1 ? rbB : rbA, ck = k == 2 ? cQ : cP;
      float g[16];
#pragma unroll
      for (int q = 0; q < 4; ++q) {
        const u32x2 bw = bwv[k == 2 ? 1 : 0][q];
        const float bs[4] = {__uint_as_float(bw[0] << 16), __uint_as_float(bw[0] & 0xffff0000u), __uint_as_float(bw[1] << 16),
                             __uint_as_float(bw[1] & 0xffff0000u)};
#pragma unroll
        for (int e = 0; e < 4; ++e) g[4 * q + e] = gelu_tanh(rbf(rbf(hacc[k][4 * q + e]) + bs[e]));
      }
      u32x4* ho = (u32x4*)hfr + ((size_t)rbk * 96 + 2 * ck) * 64 + lane;
#pragma unroll
      for (int sidx = 0; sidx < 2; ++sidx)
        ho[sidx * 64] = u32x4{cvtpk(g[8 * sidx], g[8 * sidx + 1]), cvtpk(g[8 * sidx + 2], g[8 * sidx + 3]),
                              cvtpk(g[8 * sidx + 4], g[8 * sidx + 5]), cvtpk(g[8 * sidx + 6], g[8 * sidx + 7])};
    }
  }
  SB();
  bf16x8 qf[2][4];
  sfor<24>([&](auto I) {
    constexpr int n = decltype(I)::v, b = n / 12, f = n % 12, R = AO(b, f >> 1) + 8 * (f & 1);
    float t8[8];
    acc_rd8<R>(t8);
    unsigned w0 = cvtpk(t8[0] * QSCALE, t8[1] * QSCALE);
    unsigned w1 = cvtpk(t8[2] * QSCALE, t8[3] * QSCALE);
    unsigned w2 = cvtpk(t8[4] * QSCALE, t8[5] * QSCALE);
    unsigned w3 = cvtpk(t8[6] * QSCALE, t8[7] * QSCALE);
    auto r0 = __builtin_amdgcn_permlane32_swap(w0, w2, false, false);
    auto r1 = __builtin_amdgcn_permlane32_swap(w1, w3, false, false);
    if constexpr (f < 8) {
      acc_wr4<AQ(b, f)>(r0[0], r1[0], r0[1], r1[1]);
    } else {
      u32x4 w = {r0[0], r1[0], r0[1], r1[1]};
      qf[b][f - 8] = __builtin_bit_cast(bf16x8, w);
    }
  });
  zfr = bf16x8{};
  PIN(zfr);
  SB();
  asm volatile("s_nop 7");
  sfor<12>([&](auto I) { acc_zero16<16 * decltype(I)::v>(zfr); });

  float m_run[2], l_run[2] = {0.f, 0.f}, alpha[2];
  bool pend = false;
  f32x16 S[2][2], nm[2];
  u32x4 pw[2][2];
  unsigned kb[4], vb;
  nm[0] = f32x16{}; nm[1] = f32x16{};
  PIN(nm[0]); PIN(nm[1]);
#pragma unroll
  for (int i = 0; i < 4; ++i) { PIN(qf[0][i]); PIN(qf[1][i]); }
#pragma unroll
  for (int i = 0; i < 4; ++i) fr[i] = rdK(koff[i] + 2 * TB + 4096);
  SB();
  asm volatile("s_nop 3" ::: "memory");
  SB();

  sfor<12>([&](auto I) {
    constexpr int i = decltype(I)::v;
    if constexpr (i == 0) { mfma_first<AQ(0, 0)>(S[0][0], fr[0], nm[0]); mfma_first<AQ(1, 0)>(S[0][1], fr[0], nm[1]); }
    else if constexpr (i < 8) { mfma_qa<AQ(0, i)>(S[0][0], fr[i & 3]); mfma_qa<AQ(1, i)>(S[0][1], fr[i & 3]); }
    else { mfma_qv(S[0][0], fr[i & 3], qf[0][i - 8]); mfma_qv(S[0][1], fr[i & 3], qf[1][i - 8]); }
    if constexpr (i + 4 < 12) fr[i & 3] = rdK(koff[i & 3] + 2 * TB + ((i + 4) >> 2) * 8192 + 4096);
    SB();
  });
  asm volatile("s_nop 15\n\ts_nop 7" :: "v"(nm[0]), "v"(nm[1]) : "memory");
  SB();
#pragma unroll
  for (int b = 0; b < 2; ++b) {
    float mx = S[0][b][0];
#pragma unroll
    for (int r = 1; r < 16; ++r) mx = fmaxf(mx, S[0][b][r]);
    auto rr = __builtin_amdgcn_permlane32_swap(__float_as_uint(mx), __float_as_uint(mx), false, false);
    mx = fmaxf(__uint_as_float(rr[0]), __uint_as_float(rr[1]));
    m_run[b] = mx;
#pragma unroll
    for (int r = 0; r < 16; ++r) { nm[b][r] = -mx; S[0][b][r] -= mx; }
  }
#pragma unroll
  for (int j = 0; j < 4; ++j) kb[j] = koff[j];
  vb = voff;
#pragma unroll
  for (int i = 0; i < 4; ++i) fr[i] = rdK(kb[i]);

  auto phaseA = [&](auto XX, auto HH, const char* ssrc, const unsigned sdst) {
    constexpr int X = decltype(XX)::v, HB = decltype(HH)::v;
    sfor<12>([&](auto I) {
      constexpr int i = decltype(I)::v;
      auto ex = [&](auto N) {
        constexpr int n = decltype(N)::v;
        if constexpr (n < 32) {
          constexpr int b = (n & 15) >> 3, r = (n >> 4) * 8 + (n & 7);
          S[X][b][r] = __builtin_amdgcn_exp2f(S[X][b][r]);
        }
      };
      auto pk = [&](auto N) {
        constexpr int n = decltype(N)::v;
        if constexpr (n >= 0 && n < 16) {
          constexpr int b = (n >> 2) & 1, ks = n >> 3, w = n & 3;
          pw[b][ks][w] = cvtpk(S[X][b][8 * ks + 2 * w], S[X][b][8 * ks + 2 * w + 1]);
        }
      };
      if constexpr (i == 0) mfma_first<AQ(0, 0)>(S[X ^ 1][0], fr[0], nm[0]);
      else if constexpr (i < 8) mfma_qa<AQ(0, i)>(S[X ^ 1][0], fr[i & 3]);
      else mfma_qv(S[X ^ 1][0], fr[i & 3], qf[0][i - 8]);
      SB();
      ex(IC<3 * i>{}); ex(IC<3 * i + 1>{});
      pk(IC<2 * i - 8>{});
      PIN(S[X][0]); PIN(S[X][1]);
      SB();
      if constexpr (i == 0) mfma_first<AQ(1, 0)>(S[X ^ 1][1], fr[0], nm[1]);
      else if constexpr (i < 8) mfma_qa<AQ(1, i)>(S[X ^ 1][1], fr[i & 3]);
      else mfma_qv(S[X ^ 1][1], fr[i & 3], qf[1][i - 8]);
      SB();
      if constexpr (i + 4 < 12) fr[i & 3] = rdK(kb[i & 3] + ((i + 4) >> 2) * 8192 + HB * 4096);
      else fr[i & 3] = rdV(vb + ((4 * HB) * 6 + (i - 8)) * 512);
      ex(IC<3 * i + 2>{});
      pk(IC<2 * i - 7>{});
      if constexpr (i == 2) asm volatile("" :: "v"(nm[0]), "v"(nm[1]));
      if constexpr (i == 1 || i == 5 || i == 9) {
        constexpr int pc = (i - 1) / 4;
        if constexpr (HB == 0) glds_i<(pc & 1) * 1024 + (pc >> 1) * 8192>(ssrc, kpo[pc], sdst);
        else glds_i<(pc & 1) * 3072 + (pc >> 1) * 1024>(ssrc, vpo[pc], sdst);
      }
      PIN(S[X][0]); PIN(S[X][1]); PIN(pw[0][0]); PIN(pw[0][1]); PIN(pw[1][0]); PIN(pw[1][1]);
      SB();
    });
  };
  auto phaseB = [&](auto XX, auto HH, auto HK, const char* ssrc, const unsigned sdst) {
    constexpr int X = decltype(XX)::v, HB = decltype(HH)::v, HBKN = decltype(HK)::v;
    float ls[2][2], mx[2];
    auto fill = [&](auto G) {
      constexpr int g = decltype(G)::v;
      if constexpr (g < 16) {
        constexpr int b = g >> 3, r = (g & 7) * 2;
        if constexpr ((g & 7) == 0) { ls[b][0] = S[X][b][r]; ls[b][1] = S[X][b][r + 1]; }
        else { ls[b][0] += S[X][b][r]; ls[b][1] += S[X][b][r + 1]; }
        PIN(ls[b][0]); PIN(ls[b][1]);
      }
      if constexpr (g == 16) { l_run[0] += ls[0][0] + ls[0][1]; PIN(l_run[0]); }
      if constexpr (g == 17) { l_run[1] += ls[1][0] + ls[1][1]; PIN(l_run[1]); }
      if constexpr (g == 20) {
#pragma unroll
        for (int b = 0; b < 2; ++b) {
          auto rr = __builtin_amdgcn_permlane32_swap(__float_as_uint(mx[b]), __float_as_uint(mx[b]), false, false);
          mx[b] = max2f(__uint_as_float(rr[0]), __uint_as_float(rr[1]));
          PIN(mx[b]);
        }
      }
      if constexpr (g == 21) {
        if (__builtin_expect(__any(max2f(mx[0], mx[1]) > THR), 0)) {
          const float aone = hi == 0 ? 1.f : 0.f;
#pragma unroll
          for (int b = 0; b < 2; ++b) {
            const float dl = fmaxf(mx[b], 0.f);
            m_run[b] += dl;
            alpha[b] = __builtin_amdgcn_exp2f(-dl);
            bcast16(nm[b], aone, -m_run[b]);
#pragma unroll
            for (int r = 0; r < 16; ++r) S[X ^ 1][b][r] -= dl;
          }
          pend = true;
        }
      }
      if constexpr (g == 16 || g == 18 || g == 22) {
        constexpr int pc = g == 16 ? 3 : (g == 18 ? 4 : 5);
        if constexpr (HB == 0) glds_i<(pc & 1) * 1024 + (pc >> 1) * 8192>(ssrc, kpo[pc], sdst);
        else glds_i<(pc & 1) * 3072 + (pc >> 1) * 1024>(ssrc, vpo[pc], sdst);
      }
    };
    auto pvm = [&](auto G) {
      constexpr int g = decltype(G)::v, i = g >> 1, b = g & 1, k = (g - 4) >> 1;
      const bf16x8 pb = __builtin_bit_cast(bf16x8, pw[b][i / 6]);
      if constexpr (g >= 4 && g < 20) {
        if constexpr (k == 0) mfma_o_mx<AO(b, i % 6)>(fr[i & 3], pb, mx[b], S[X ^ 1][b][0], S[X ^ 1][b][1], S[X ^ 1][b][2]);
        else if constexpr (k < 7) mfma_o_mx<AO(b, i % 6)>(fr[i & 3], pb, mx[b], mx[b], S[X ^ 1][b][2 * k + 1], S[X ^ 1][b][2 * k + 2]);
        else mfma_o_mx<AO(b, i % 6)>(fr[i & 3], pb, mx[b], mx[b], S[X ^ 1][b][15], S[X ^ 1][b][15]);
      } else mfma_o<AO(b, i % 6)>(fr[i & 3], pb);
    };
    sfor<12>([&](auto I) {
      constexpr int i = decltype(I)::v;
      pvm(IC<2 * i>{});
      SB();
      fill(IC<2 * i>{});
      SB();
      pvm(IC<2 * i + 1>{});
      SB();
      if constexpr (i + 4 < 12) fr[i & 3] = rdV(vb + ((4 * HB + 2 * ((i + 4) / 6)) * 6 + ((i + 4) % 6)) * 512);
      else fr[i & 3] = rdK(kb[i - 8] + HBKN * 4096);
      fill(IC<2 * i + 1>{});
      SB();
    });
    if (__builtin_expect(pend, 0)) {
      asm volatile("s_nop 15\n\ts_nop 7" ::: "memory");
      l_run[0] *= alpha[0]; l_run[1] *= alpha[1];
      sfor<192>([&](auto I) { constexpr int n = decltype(I)::v; acc_scale<n>(alpha[n / 96]); });
      asm volatile("s_nop 3" ::: "memory");
      pend = false;
    }
  };

  asm volatile("s_waitcnt vmcnt(0)" ::: "memory");
  unsigned scur = 0;
#pragma unroll 1
  for (int t = 0; t < 32; ++t) {
    const unsigned snxt = nxt(scur), swr = nxt(snxt);
    asm volatile("s_waitcnt vmcnt(6)\n\ts_barrier" ::: "memory");
    int tk = t + 2 < 32 ? t + 2 : 31;
    const int tv = tk;
    if (tk == 31 && wid >= 2) tk = 30;
    const char* ks_ = kbase + (size_t)(tk * 64 + 32) * (DH * 2);
    const char* vs_ = vbase + (size_t)(tv * 64) * (DH * 2);
    SB();
    phaseA(IC<0>{}, IC<0>{}, ks_, kdst + swr);
    phaseB(IC<0>{}, IC<0>{}, IC<1>{}, ks_, kdst + swr);
    phaseA(IC<1>{}, IC<1>{}, vs_, vdst + swr);
#pragma unroll
    for (int j = 0; j < 4; ++j) kb[j] = koff[j] + snxt;
    phaseB(IC<1>{}, IC<1>{}, IC<0>{}, vs_, vdst + swr);
    vb = voff + snxt;
    scur = snxt;
  }
  asm volatile("s_waitcnt vmcnt(0)\n\ts_nop 15\n\ts_nop 7" ::: "memory");
  int tz = threadIdx.x;
  asm volatile("" : "+v"(tz));
  float rls[2];
#pragma unroll
  for (int b = 0; b < 2; ++b) {
    auto rr = __builtin_amdgcn_permlane32_swap(__float_as_uint(l_run[b]), __float_as_uint(l_run[b]), false, false);
    rls[b] = 1.0f / (__uint_as_float(rr[0]) + __uint_as_float(rr[1]));
  }
  u32x4* ofr = (u32x4*)O + ((size_t)(qb * 8 + (tz >> 6) * 2) * 384 + head * 12) * 64 + (tz & 63);
  sfor<24>([&](auto I) {
    constexpr int n = decltype(I)::v, b = n / 12, f = n % 12, R = AO(b, f >> 1) + 8 * (f & 1);
    const float rl = rls[b];
    float t8[8];
    acc_rd8<R>(t8);
    __builtin_nontemporal_store(u32x4{cvtpk(t8[0] * rl, t8[1] * rl), cvtpk(t8[2] * rl, t8[3] * rl),
                                      cvtpk(t8[4] * rl, t8[5] * rl), cvtpk(t8[6] * rl, t8[7] * rl)}, &ofr[(b * 384 + f) * 64]);
  });
}

extern "C" void kernel_launch(void* const* d_in, const int* in_sizes, int n_in, void* d_out, int out_size,
                              void* d_ws, size_t ws_size, hipStream_t stream) {
  const u16* x = (const u16*)d_in[0];
  const u16* kc = (const u16*)d_in[1];
  const u16* vc = (const u16*)d_in[2];
  const u16* wq = (const u16*)d_in[3];
  const u16* wo = (const u16*)d_in[4];
  const u16* w1 = (const u16*)d_in[6];
  const u16* b1 = (const u16*)d_in[7];
  const u16* w2 = (const u16*)d_in[8];
  const u16* b2 = (const u16*)d_in[9];
  char* ws = (char*)d_ws;
  u16* xnp = (u16*)(ws);
  u16* y = (u16*)(ws + (4u << 20));
  u16* h = (u16*)(ws + (8u << 20));
  u16* O = (u16*)(ws + (16u << 20));
  u16* part = (u16*)(ws + (48u << 20));

  hipFuncSetAttribute((const void*)k_attn, hipFuncAttributeMaxDynamicSharedMemorySize, 6 * TB);
  hipFuncSetAttribute((const void*)k_gemm2, hipFuncAttributeMaxDynamicSharedMemorySize, 4 * 16384);

  k_ln<<<SEQ / 4, 256, 0, stream>>>(x, xnp, y);
  k_attn<<<256, 256, 6 * TB, stream>>>(xnp, wq, kc, vc, O, y, w1, b1, h);
  k_gemm2<<<200, 256, 4 * 16384, stream>>>(O, wo, h, w2, part);
  k_final<<<SEQ * DM / 2048, 256, 0, stream>>>(x, part, b2, (u16*)d_out);
}
```

```cpp
#include <hip/hip_runtime.h>
#include <hip/hip_bf16.h>
#include <stdint.h>

typedef __attribute__((ext_vector_type(8))) short bf16x8;
typedef __attribute__((ext_vector_type(8))) __bf16 bf16x8b;
typedef __attribute__((ext_vector_type(4))) short s16x4;
typedef __attribute__((ext_vector_type(16))) float f32x16;
typedef __attribute__((ext_vector_type(4))) float f32x4;
typedef __attribute__((ext_vector_type(2))) float f32x2;
typedef __attribute__((ext_vector_type(2))) __bf16 bf16x2b;
typedef __attribute__((ext_vector_type(4))) unsigned u32x4;
typedef __attribute__((ext_vector_type(2))) unsigned u32x2;
typedef __attribute__((address_space(3))) char* lds_ptr;
typedef unsigned short u16;

#define DI __device__ __forceinline__

constexpr int SEQ = 2048, DM = 640, NH = 32, DH = 192, FF = 1536, QD = NH * DH;

DI f32x16 MFMA(bf16x8 a, bf16x8 b, f32x16 c) {
  return __builtin_amdgcn_mfma_f32_32x32x16_bf16(__builtin_bit_cast(bf16x8b, a), __builtin_bit_cast(bf16x8b, b), c, 0, 0, 0);
}
DI unsigned cvtpk(float lo, float hi) {
  f32x2 v = {lo, hi};
  bf16x2b b = __builtin_convertvector(v, bf16x2b);
  return __builtin_bit_cast(unsigned, b);
}
DI float bf2f(u16 v) { return __uint_as_float(((unsigned)v) << 16); }
DI float rbf(float v) { return __uint_as_float(cvtpk(v, 0.f) << 16); }
DI s16x4 trread(lds_ptr p) {
  return __builtin_amdgcn_ds_read_tr16_b64_v4i16((__attribute__((address_space(3))) s16x4*)p);
}
DI bf16x8 ldsread(lds_ptr p) { return *(__attribute__((address_space(3))) bf16x8*)p; }
DI bf16x8 cat(s16x4 a, s16x4 b) { return __builtin_shufflevector(a, b, 0, 1, 2, 3, 4, 5, 6, 7); }
DI void glds16(const void* g, lds_ptr l) {
  __builtin_amdgcn_global_load_lds((const __attribute__((address_space(1))) unsigned*)g,
                                   (__attribute__((address_space(3))) unsigned*)l, 16, 0, 0);
}

DI float wave_sum(float v) {
#pragma unroll
  for (int o = 32; o > 0; o >>= 1) v += __shfl_xor(v, o, 64);
  return v;
}
DI void ln_row(float (&v)[10]) {
  float s = 0.f;
#pragma unroll
  for (int i = 0; i < 10; ++i) s += v[i];
  s = wave_sum(s);
  const float mean = s * (1.0f / DM);
  float q = 0.f;
#pragma unroll
  for (int i = 0; i < 10; ++i) { float d = v[i] - mean; q += d * d; }
  q = wave_sum(q);
  const float mb = rbf(mean), vb = rbf(q * (1.0f / DM));
  const float den = rbf(sqrtf(rbf(vb + 1e-5f)));
#pragma unroll
  for (int i = 0; i < 10; ++i) v[i] = rbf(rbf(v[i] - mb) / den);
}
__global__ __launch_bounds__(256) void k_ln(const u16* __restrict__ x, u16* __restrict__ xnp, u16* __restrict__ y) {
  const int row = blockIdx.x * 4 + (threadIdx.x >> 6), lane = threadIdx.x & 63;
  const unsigned* xr = (const unsigned*)(x + (size_t)row * DM);
  float v[10];
#pragma unroll
  for (int i = 0; i < 5; ++i) {
    unsigned w = xr[lane + 64 * i];
    v[2 * i] = __uint_as_float(w << 16);
    v[2 * i + 1] = __uint_as_float(w & 0xffff0000u);
  }
  ln_row(v);
  unsigned* xo = (unsigned*)xnp + (size_t)(row >> 5) * (40 * 64 * 4) + (row & 31) * 4;
#pragma unroll
  for (int i = 0; i < 5; ++i) {
    int e = lane + 64 * i;
    int ep = (e & ~6) | ((e & 2) << 1) | ((e & 4) >> 1);
    xo[(ep >> 3) * 256 + ((ep >> 2) & 1) * 128 + (ep & 3)] =
        (__float_as_uint(v[2 * i]) >> 16) | (__float_as_uint(v[2 * i + 1]) & 0xffff0000u);
  }
  ln_row(v);
  unsigned* yo = (unsigned*)y + (size_t)(row >> 5) * (40 * 64 * 4) + (row & 31) * 4;
#pragma unroll
  for (int i = 0; i < 5; ++i) {
    int e = lane + 64 * i;
    int ep = (e & ~6) | ((e & 2) << 1) | ((e & 4) >> 1);
    yo[(ep >> 3) * 256 + ((ep >> 2) & 1) * 128 + (ep & 3)] =
        (__float_as_uint(v[2 * i]) >> 16) | (__float_as_uint(v[2 * i + 1]) & 0xffff0000u);
  }
}

#define SB() __builtin_amdgcn_sched_barrier(0)
#define PIN(x) asm volatile("" : "+v"(x))
template <int I> struct IC { static constexpr int v = I; };
template <int N, class F> DI void sfor(F&& f) {
  if constexpr (N > 0) { sfor<N - 1>(f); f(IC<N - 1>{}); }
}
template <int IMM> DI void glds_i(const void* sbase, unsigned voff, unsigned ldsbase) {
  asm volatile("s_add_i32 m0, %2, %3\n\ts_nop 0\n\tglobal_load_lds_dwordx4 %0, %1"
               :: "v"(voff), "s"(sbase), "s"(ldsbase), "i"(IMM) : "memory", "scc");
}
template <int OFF> DI void gload16(bf16x8& d, const char* p) {
  asm volatile("global_load_dwordx4 %0, %1, off offset:%2" : "=&v"(d) : "v"(p), "i"(OFF) : "memory");
}
template <int OFF> DI void gload8(u32x2& d, const void* p) {
  asm volatile("global_load_dwordx2 %0, %1, off offset:%2" : "=&v"(d) : "v"(p), "i"(OFF) : "memory");
}
template <int N> DI void xwait2(bf16x8 (&x)[2][4]) {
  asm volatile("s_waitcnt vmcnt(%8)\n\ts_barrier" : "+v"(x[0][0]), "+v"(x[0][1]), "+v"(x[0][2]), "+v"(x[0][3]), "+v"(x[1][0]), "+v"(x[1][1]), "+v"(x[1][2]), "+v"(x[1][3]) : "i"(N) : "memory");
}
DI void xpin2(bf16x8 (&x)[2][4]) {
  asm volatile("" : "+v"(x[0][0]), "+v"(x[0][1]), "+v"(x[0][2]), "+v"(x[0][3]), "+v"(x[1][0]), "+v"(x[1][1]), "+v"(x[1][2]), "+v"(x[1][3]));
}

DI void wgemm24(const char* wbase, unsigned ldwb, const char* xp, unsigned xrbs, int ktiles, unsigned lds0,
                f32x16 (&acc)[2][4]) {
  constexpr int NB = 4;
  constexpr unsigned TBY = 4096 * NB;
  const int tid = threadIdx.x, lane = tid & 63, wid = __builtin_amdgcn_readfirstlane(tid >> 6);
  const int hi = lane >> 5;
  unsigned wpo[NB];
#pragma unroll
  for (int i = 0; i < NB; ++i) {
    const int sub = 2 * (wid * NB + i) + hi, kg = sub / NB, nb = sub % NB;
    wpo[i] = (unsigned)(8 * kg + ((lane >> 2) & 7)) * ldwb + nb * 64 + (lane & 3) * 16;
  }
  const unsigned wdst = lds0 + wid * NB * 1024;
  const unsigned voff = lds0 + (hi * 4 + ((lane >> 2) & 3)) * 64 + ((lane >> 4) & 1) * 32 + (lane & 3) * 8;
  auto rdW = [&](unsigned a) { return cat(trread((lds_ptr)(uintptr_t)a), trread((lds_ptr)(uintptr_t)(a + NB * 512))); };
#pragma unroll
  for (int r = 0; r < 2; ++r)
#pragma unroll
    for (int n = 0; n < NB; ++n) acc[r][n] = f32x16{};
  bf16x8 x0[2][4], x1[2][4], x2[2][4], x3[2][4], fr[4];
  const size_t wts = (size_t)64 * ldwb;
  auto issue = [&](bf16x8 (&x)[2][4], int t, auto SLOT) {
    const char* xq = xp + (size_t)t * 4096;
    sfor<8>([&](auto I) { constexpr int i = decltype(I)::v; gload16<(i & 3) * 1024>(x[i >> 2][i & 3], xq + (i >> 2) * xrbs); });
    sfor<NB>([&](auto I) { glds_i<decltype(SLOT)::v * TBY + decltype(I)::v * 1024>(wbase + (size_t)t * wts, wpo[decltype(I)::v], wdst); });
  };
  issue(x0, 0, IC<0>{});
  issue(x1, 1, IC<1>{});
  issue(x2, 2, IC<2>{});
  asm volatile("s_waitcnt vmcnt(24)\n\ts_barrier" ::: "memory");
#pragma unroll
  for (int i = 0; i < 4; ++i) fr[i] = rdW(voff + i * 512);
  auto step = [&](bf16x8 (&xc)[2][4], bf16x8 (&xn)[2][4], const int t, auto SLOT) {
    constexpr int sl = decltype(SLOT)::v, sw = (sl + 3) & 3;
    xwait2<12>(xc);
    const unsigned vb = voff + sl * TBY, vbn = voff + ((sl + 1) & 3) * TBY;
    const int tn = t + 3 < ktiles ? t + 3 : ktiles - 1;
    const char* xq = xp + (size_t)tn * 4096;
    const char* wq_ = wbase + (size_t)tn * wts;
    SB();
    sfor<16>([&](auto I) {
      constexpr int i = decltype(I)::v;
      acc[0][i & 3] = MFMA(fr[i & 3], xc[0][i >> 2], acc[0][i & 3]);
      acc[1][i & 3] = MFMA(fr[i & 3], xc[1][i >> 2], acc[1][i & 3]);
      SB();
      if constexpr (i + 4 < 16) fr[i & 3] = rdW(vb + ((2 * ((i + 4) >> 2)) * NB + (i & 3)) * 512);
      else fr[i & 3] = rdW(vbn + (i & 3) * 512);
      if constexpr (i < 8) gload16<(i & 3) * 1024>(xn[i >> 2][i & 3], xq + (i >> 2) * xrbs);
      if constexpr (i >= 8 && i < 12) glds_i<sw * TBY + (i - 8) * 1024>(wq_, wpo[i - 8], wdst);
      SB();
    });
  };
#pragma unroll 1
  for (int t = 0; t < ktiles; t += 4) {
    step(x0, x3, t, IC<0>{});
    step(x1, x0, t + 1, IC<1>{});
    step(x2, x1, t + 2, IC<2>{});
    step(x3, x2, t + 3, IC<3>{});
  }
  asm volatile("s_waitcnt vmcnt(0)" ::: "memory");
  xpin2(x0); xpin2(x1); xpin2(x2);
}
DI int crow(int r, int hi) { return (r & 3) + 8 * (r >> 2) + 4 * hi; }

DI float gelu_tanh(float v) {
  const float u = 0.7978845608028654f * (v + 0.044715f * v * v * v);
  const float e = __builtin_amdgcn_exp2f(2.8853900817779268f * u);
  return v - v * __builtin_amdgcn_rcpf(e + 1.f);
}

__global__ __launch_bounds__(256, 1) void k_gemm2(const u16* __restrict__ O, const u16* __restrict__ wo,
                                                  const u16* __restrict__ h, const u16* __restrict__ w2,
                                                  u16* __restrict__ part) {
  extern __shared__ __attribute__((aligned(16))) char smem[];
  const int bid = blockIdx.x, x = bid & 7, r = bid >> 3;
  const int nt = r % 5, c = r < 20 ? (x >> 1) : 4, mt = r < 20 ? 4 * (x & 1) + r / 5 : x;
  const int lane = threadIdx.x & 63, wid = threadIdx.x >> 6;
  const int rb0 = mt * 8 + wid * 2;
  const char* W; const char* X; unsigned xrbs;
  if (c < 4) {
    W = (const char*)(wo + (size_t)c * 24 * 64 * DM + nt * 128);
    xrbs = 384 * 1024;
    X = (const char*)O + ((size_t)rb0 * 384 + c * 96) * 1024 + lane * 16;
  } else {
    W = (const char*)(w2 + nt * 128);
    xrbs = 96 * 1024;
    X = (const char*)h + (size_t)rb0 * 96 * 1024 + lane * 16;
  }
  f32x16 acc[2][4];
  wgemm24(W, DM * 2, X, xrbs, 24, (unsigned)(uintptr_t)(lds_ptr)smem, acc);
  u32x4* P = (u32x4*)part + (size_t)c * (SEQ * DM / 8);
#pragma unroll
  for (int b = 0; b < 2; ++b)
#pragma unroll
    for (int nb = 0; nb < 4; ++nb)
#pragma unroll
      for (int hq = 0; hq < 2; ++hq)
        P[(((size_t)(rb0 + b) * 20 + nt * 4 + nb) * 2 + hq) * 64 + lane] =
            u32x4{cvtpk(acc[b][nb][8 * hq], acc[b][nb][8 * hq + 1]), cvtpk(acc[b][nb][8 * hq + 2], acc[b][nb][8 * hq + 3]),
                  cvtpk(acc[b][nb][8 * hq + 4], acc[b][nb][8 * hq + 5]), cvtpk(acc[b][nb][8 * hq + 6], acc[b][nb][8 * hq + 7])};
}

DI float blo(unsigned w) { return __uint_as_float(w << 16); }
DI float bhi(unsigned w) { return __uint_as_float(w & 0xffff0000u); }
__global__ __launch_bounds__(256) void k_final(const u16* __restrict__ x, const u16* __restrict__ part,
                                               const u16* __restrict__ b2, u16* __restrict__ out) {
  const int idx = blockIdx.x * 256 + threadIdx.x;
  constexpr size_t PS = (size_t)SEQ * DM / 8;
  const u32x4* P = (const u32x4*)part + idx;
  const u32x4 p0 = P[0], p1 = P[PS], p2 = P[2 * PS], p3 = P[3 * PS], pm = P[4 * PS];
  const int lane = idx & 63, hq = (idx >> 6) & 1, blk = idx >> 7, cb = blk % 20, rb = blk / 20;
  const int m = rb * 32 + (lane & 31), n0 = cb * 32 + 16 * hq + 4 * (lane >> 5);
#pragma unroll
  for (int q = 0; q < 2; ++q) {
    const int n = n0 + 8 * q;
    const u32x2 xw = *(const u32x2*)(x + (size_t)m * DM + n);
    const u32x2 bw = *(const u32x2*)(b2 + n);
    float r[4];
#pragma unroll
    for (int i = 0; i < 4; ++i) {
      const int w = 2 * q + (i >> 1);
      const float a0 = (i & 1) ? bhi(p0[w]) : blo(p0[w]), a1 = (i & 1) ? bhi(p1[w]) : blo(p1[w]);
      const float a2 = (i & 1) ? bhi(p2[w]) : blo(p2[w]), a3 = (i & 1) ? bhi(p3[w]) : blo(p3[w]);
      const float am = (i & 1) ? bhi(pm[w]) : blo(pm[w]);
      const float xo = (i & 1) ? bhi(xw[i >> 1]) : blo(xw[i >> 1]), bo = (i & 1) ? bhi(bw[i >> 1]) : blo(bw[i >> 1]);
      const float sv = rbf((a0 + a1) + (a2 + a3));
      const float mm = rbf(am + bo);
      const float g = rbf(mm / (1.f + __expf(-mm)));
      r[i] = xo + rbf(sv * g);
    }
    *(u32x2*)(out + (size_t)m * DM + n) = u32x2{cvtpk(r[0], r[1]), cvtpk(r[2], r[3])};
  }
}

constexpr int TB = 24576;
constexpr float QSCALE = 0.07216878364870322f * 1.4426950408889634f;
constexpr float THR = 20.0f;

DI void glds_s(const void* sbase, unsigned voff, unsigned ldsaddr) {
  asm volatile("s_mov_b32 m0, %2\n\ts_nop 0\n\tglobal_load_lds_dwordx4 %0, %1"
               :: "v"(voff), "s"(sbase), "s"(ldsaddr) : "memory");
}
DI float max3f(float a, float b, float c) { float r; asm("v_max3_f32 %0, %1, %2, %3" : "=v"(r) : "v"(a), "v"(b), "v"(c)); return r; }
DI float max2f(float a, float b) { float r; asm("v_max_f32_e32 %0, %1, %2" : "=v"(r) : "v"(a), "v"(b)); return r; }
DI void bcast16(f32x16& d, float aone, float negm) {
  asm volatile("s_nop 1\n\tv_mfma_f32_32x32x2_f32 %0, %1, %2, 0" : "+v"(d) : "v"(aone), "v"(negm));
}
DI bf16x8 rdK(unsigned a) { return ldsread((lds_ptr)(uintptr_t)a); }
DI bf16x8 rdV(unsigned a) { return cat(trread((lds_ptr)(uintptr_t)a), trread((lds_ptr)(uintptr_t)(a + 3072))); }
DI unsigned nxt(unsigned s) { return s == 2 * TB ? 0 : s + TB; }
#define ACLOB "a0","a1","a2","a3","a4","a5","a6","a7","a8","a9","a10","a11","a12","a13","a14","a15","a16","a17","a18","a19","a20","a21","a22","a23","a24","a25","a26","a27","a28","a29","a30","a31","a32","a33","a34","a35","a36","a37","a38","a39","a40","a41","a42","a43","a44","a45","a46","a47","a48","a49","a50","a51","a52","a53","a54","a55","a56","a57","a58","a59","a60","a61","a62","a63","a64","a65","a66","a67","a68","a69","a70","a71","a72","a73","a74","a75","a76","a77","a78","a79","a80","a81","a82","a83","a84","a85","a86","a87","a88","a89","a90","a91","a92","a93","a94","a95","a96","a97","a98","a99","a100","a101","a102","a103","a104","a105","a106","a107","a108","a109","a110","a111","a112","a113","a114","a115","a116","a117","a118","a119","a120","a121","a122","a123","a124","a125","a126","a127","a128","a129","a130","a131","a132","a133","a134","a135","a136","a137","a138","a139","a140","a141","a142","a143","a144","a145","a146","a147","a148","a149","a150","a151","a152","a153","a154","a155","a156","a157","a158","a159","a160","a161","a162","a163","a164","a165","a166","a167","a168","a169","a170","a171","a172","a173","a174","a175","a176","a177","a178","a179","a180","a181","a182","a183","a184","a185","a186","a187","a188","a189","a190","a191","a192","a193","a194","a195","a196","a197","a198","a199","a200","a201","a202","a203","a204","a205","a206","a207","a208","a209","a210","a211","a212","a213","a214","a215","a216","a217","a218","a219","a220","a221","a222","a223","a224","a225","a226","a227","a228","a229","a230","a231","a232","a233","a234","a235","a236","a237","a238","a239","a240","a241","a242","a243","a244","a245","a246","a247","a248","a249","a250","a251","a252","a253","a254","a255"
constexpr int AO(int b, int db) { return 16 * (6 * b + db); }
constexpr int AQ(int b, int i) { return 192 + 32 * b + 4 * i; }
template <int R> DI void mfma_o(const bf16x8& a, const bf16x8& b) {
  asm volatile("v_mfma_f32_32x32x16_bf16 a[%c2:%c3], %0, %1, a[%c2:%c3]" :: "v"(a), "v"(b), "i"(R), "i"(R + 15) : ACLOB);
}
template <int R> DI void mfma_o_mx(const bf16x8& a, const bf16x8& b, float& m, float x, float y, float z) {
  asm volatile("v_mfma_f32_32x32x16_bf16 a[%c6:%c7], %1, %2, a[%c6:%c7]\n\tv_max3_f32 %0, %3, %4, %5"
               : "=v"(m) : "v"(a), "v"(b), "v"(x), "v"(y), "v"(z), "i"(R), "i"(R + 15) : ACLOB);
}
template <int R> DI void mfma_first(f32x16& d, const bf16x8& a, const f32x16& c) {
  asm volatile("v_mfma_f32_32x32x16_bf16 %0, %1, a[%c3:%c4], %2" : "=&v"(d) : "v"(a), "v"(c), "i"(R), "i"(R + 3) : ACLOB);
}
template <int R> DI void mfma_qa(f32x16& d, const bf16x8& a) {
  asm volatile("v_mfma_f32_32x32x16_bf16 %0, %1, a[%c2:%c3], %0" : "+v"(d) : "v"(a), "i"(R), "i"(R + 3) : ACLOB);
}
DI void mfma_qv(f32x16& d, const bf16x8& a, const bf16x8& b) {
  asm volatile("v_mfma_f32_32x32x16_bf16 %0, %1, %2, %0" : "+v"(d) : "v"(a), "v"(b));
}
template <int R> DI float acc_rd() { float r; asm volatile("v_accvgpr_read_b32 %0, a[%c1]" : "=v"(r) : "i"(R)); return r; }
template <int R> DI void acc_wr(unsigned v) { asm volatile("v_accvgpr_write_b32 a[%c1], %0" :: "v"(v), "i"(R) : ACLOB); }
template <int R> DI void acc_zero() { asm volatile("v_accvgpr_write_b32 a[%c0], 0" :: "i"(R) : ACLOB); }
template <int R> DI void acc_zero16(const bf16x8& z) {
  asm volatile("v_mfma_f32_32x32x16_bf16 a[%c1:%c2], %0, %0, 0" :: "v"(z), "i"(R), "i"(R + 15) : ACLOB);
}
template <int R> DI void acc_rd8(float (&r)[8]) {
  asm volatile("v_accvgpr_read_b32 %0, a[%c8]\n\tv_accvgpr_read_b32 %1, a[%c9]\n\tv_accvgpr_read_b32 %2, a[%c10]\n\tv_accvgpr_read_b32 %3, a[%c11]\n\t"
               "v_accvgpr_read_b32 %4, a[%c12]\n\tv_accvgpr_read_b32 %5, a[%c13]\n\tv_accvgpr_read_b32 %6, a[%c14]\n\tv_accvgpr_read_b32 %7, a[%c15]"
               : "=v"(r[0]), "=v"(r[1]), "=v"(r[2]), "=v"(r[3]), "=v"(r[4]), "=v"(r[5]), "=v"(r[6]), "=v"(r[7])
               : "i"(R), "i"(R + 1), "i"(R + 2), "i"(R + 3), "i"(R + 4), "i"(R + 5), "i"(R + 6), "i"(R + 7));
}
template <int R> DI void acc_wr4(unsigned a, unsigned b, unsigned c, unsigned d) {
  asm volatile("v_accvgpr_write_b32 a[%c4], %0\n\tv_accvgpr_write_b32 a[%c5], %1\n\tv_accvgpr_write_b32 a[%c6], %2\n\tv_accvgpr_write_b32 a[%c7], %3"
               :: "v"(a), "v"(b), "v"(c), "v"(d), "i"(R), "i"(R + 1), "i"(R + 2), "i"(R + 3) : ACLOB);
}
template <int R> DI void acc_scale(float f) {
  float t;
  asm volatile("v_accvgpr_read_b32 %0, a[%c2]\n\tv_mul_f32 %0, %1, %0\n\tv_accvgpr_write_b32 a[%c2], %0" : "=&v"(t) : "v"(f), "i"(R) : ACLOB);
}

__global__ __launch_bounds__(256, 1) void k_attn(const u16* __restrict__ xnp, const u16* __restrict__ wq,
                                                 const u16* __restrict__ Kc, const u16* __restrict__ Vc,
                                                 u16* __restrict__ O, const u16* __restrict__ yfr,
                                                 const u16* __restrict__ w1, const u16* __restrict__ b1,
                                                 u16* __restrict__ hfr, const u16* __restrict__ wo,
                                                 const u16* __restrict__ w2) {
  extern __shared__ __attribute__((aligned(16))) char smem[];
  const unsigned lds0 = (unsigned)(uintptr_t)(lds_ptr)smem;
  const int tid = threadIdx.x, lane = tid & 63, wid = __builtin_amdgcn_readfirstlane(tid >> 6);
  const int r32 = lane & 31, hi = lane >> 5;
  const int bid = blockIdx.x, xcd = bid & 7, idx = bid >> 3;
  const int head = xcd * 4 + (idx >> 3), qb = idx & 7;
  const int q0 = qb * 256 + wid * 64;

  const int krl = lane >> 3;
  const unsigned kvo0 = (unsigned)(krl * DH * 2 + (((lane & 7) ^ (krl >> 1)) << 4));
  const unsigned kvo1 = (unsigned)(krl * DH * 2 + (((lane & 7) ^ (4 + (krl >> 1))) << 4));
  const unsigned vvo = (unsigned)(((lane >> 2) & 7) * DH * 2 + (lane >> 5) * 64 + (lane & 3) * 16);
  const unsigned wvo = (unsigned)(((lane >> 2) & 7) * QD * 2 + (lane >> 5) * 64 + (lane & 3) * 16);
  const char* kbase = (const char*)(Kc + (size_t)head * SEQ * DH) + (size_t)(16 * wid) * DH * 2;
  const char* vbase = (const char*)(Vc + (size_t)head * SEQ * DH) + (size_t)(16 * wid) * DH * 2;
  const char* wbase = (const char*)(wq + head * DH) + (size_t)(16 * wid) * QD * 2;
  const unsigned kdst = lds0 + wid * 2048, vdst = lds0 + 3 * TB + wid * 6144;
  auto pieceK = [&](int key0, unsigned slot, int pc) {
    glds_s(kbase + (size_t)key0 * (DH * 2) + (pc & 1) * (8 * DH * 2) + (pc >> 1) * 128, (pc & 1) ? kvo1 : kvo0,
           kdst + slot + (pc & 1) * 1024 + (pc >> 1) * 8192);
  };
  auto pieceV = [&](int key0, unsigned slot, int pc) {
    glds_s(vbase + (size_t)key0 * (DH * 2) + (pc & 1) * (8 * DH * 2) + (pc >> 1) * 128, vvo,
           vdst + slot + (pc & 1) * 3072 + (pc >> 1) * 1024);
  };
  auto pieceW = [&](int t, unsigned slot, int pc) {
    glds_s(wbase + (size_t)t * (64 * QD * 2) + (size_t)(pc & 1) * (8 * QD * 2) + (pc >> 1) * 128, wvo,
           vdst + slot + (pc & 1) * 3072 + (pc >> 1) * 1024);
  };
  unsigned kpo[6], vpo[6];
#pragma unroll
  for (int pc = 0; pc < 6; ++pc) {
    kpo[pc] = ((pc & 1) ? kvo1 : kvo0) + (pc & 1) * (8 * DH * 2) + (pc >> 1) * 128;
    vpo[pc] = vvo + (pc & 1) * (8 * DH * 2) + (pc >> 1) * 128;
  }
  unsigned koff[4];
#pragma unroll
  for (int j = 0; j < 4; ++j) koff[j] = lds0 + r32 * 128 + (((2 * j + hi) ^ ((r32 >> 1) & 7)) << 4);
  const unsigned voff = lds0 + 3 * TB + (hi * 4 + ((lane >> 2) & 3)) * 64 + ((lane >> 4) & 1) * 32 + (lane & 3) * 8;

  bf16x8 zfr = bf16x8{};
  PIN(zfr);
  asm volatile("s_nop 3");
  sfor<12>([&](auto I) { acc_zero16<16 * decltype(I)::v>(zfr); });
  bf16x8 fr[4];

  const int odd = head & 1, hj = head >> 1;
  const char* xsrc0 = (const char*)xnp + ((size_t)(q0 >> 5) * (40 * 64) + lane) * 16;
  const char* ysrcA = (const char*)yfr + ((size_t)((q0 >> 5) + odd) * (40 * 64) + lane) * 16;
  const char* ysrcB = (const char*)yfr + ((size_t)((q0 >> 5) + 1 - odd) * (40 * 64) + lane) * 16;
  const char* w1base = (const char*)(w1 + (3 * hj + odd) * 32) + (size_t)(16 * wid) * (FF * 2);
  const unsigned w1vo = (unsigned)(((lane >> 2) & 7) * FF * 2 + (lane >> 5) * 64 + (lane & 3) * 16);
  const unsigned w1dst = lds0 + TB + wid * 2048;
  const unsigned w1rd = lds0 + TB + (hi * 4 + ((lane >> 2) & 3)) * 64 + ((lane >> 4) & 1) * 32 + (lane & 3) * 8;
  const unsigned w1P = w1rd + (odd ? 512 : 0), w1Q = w1rd + (odd ? 0 : 512);
  auto rdH = [&](unsigned a) { return cat(trread((lds_ptr)(uintptr_t)a), trread((lds_ptr)(uintptr_t)(a + 1024))); };
  u32x2 bwv[2][4];
  {
    const u16* bp = b1 + (3 * hj + 2 * odd) * 32 + 4 * hi;
    const u16* bq = b1 + (3 * hj + 1) * 32 + 4 * hi;
    sfor<8>([&](auto I) {
      constexpr int i = decltype(I)::v;
      gload8<(i & 3) * 16>(bwv[i >> 2][i & 3], i < 4 ? bp : bq);
    });
  }
  bf16x8 xa[4][4], xb[4][4];
  f32x16 hacc[3];
  hacc[0] = f32x16{}; hacc[1] = f32x16{}; hacc[2] = f32x16{};
  PIN(hacc[0]); PIN(hacc[1]); PIN(hacc[2]);
#pragma unroll
  for (int pc = 0; pc < 6; ++pc) pieceW(0, 2 * TB, pc);
#pragma unroll
  for (int pc = 0; pc < 6; ++pc) pieceW(1, 0, pc);
#pragma unroll
  for (int t = 0; t < 2; ++t)
#pragma unroll
    for (int g = 0; g < 2; ++g)
      glds_s(w1base + (size_t)t * (64 * FF * 2) + (size_t)g * (8 * FF * 2), w1vo, w1dst + (t == 0 ? 2 : 0) * 8192 + g * 1024);
  sfor<16>([&](auto I) {
    constexpr int i = decltype(I)::v;
    gload16<(i & 3) * 1024>(xa[i >> 2][i & 3], (i >> 2) == 0 ? xsrc0 : (i >> 2) == 1 ? xsrc0 + 40960 : (i >> 2) == 2 ? ysrcA : ysrcB);
  });
  if (wid >= 2) {
#pragma unroll
    for (int pc = 0; pc < 6; ++pc) pieceK(-32, 2 * TB, pc);
  }
#pragma unroll
  for (int pc = 0; pc < 6; ++pc) pieceK(32, 0, pc);
#define XOPS(x) "+v"(x[0][0]), "+v"(x[0][1]), "+v"(x[0][2]), "+v"(x[0][3]), "+v"(x[1][0]), "+v"(x[1][1]), "+v"(x[1][2]), "+v"(x[1][3]), \
                "+v"(x[2][0]), "+v"(x[2][1]), "+v"(x[2][2]), "+v"(x[2][3]), "+v"(x[3][0]), "+v"(x[3][1]), "+v"(x[3][2]), "+v"(x[3][3])
  {
    unsigned srd = 2 * TB, swr = TB;
    auto qstep = [&](bf16x8 (&xc)[4][4], bf16x8 (&xn)[4][4], const int t) {
      asm volatile("s_waitcnt vmcnt(8)\n\ts_barrier" : XOPS(xc) :: "memory");
      const unsigned vb = voff + srd;
      const unsigned hP = w1P + srd / 3, hQ = w1Q + srd / 3;
#pragma unroll
      for (int i = 0; i < 4; ++i) fr[i] = rdV(vb + i * 512);
      const int tn = t + 1 < 10 ? t + 1 : 9, tw = t + 2 < 10 ? t + 2 : 9;
      const char* p0 = xsrc0 + tn * 4096;
      const char* p2 = ysrcA + tn * 4096;
      const char* p3 = ysrcB + tn * 4096;
      SB();
      const bool isw = t + 2 < 10;
      const char* ssrc = isw ? wbase + (size_t)(t + 2) * (64 * QD * 2) : vbase + (size_t)(t - 8) * (64 * DH * 2);
      const unsigned sgs = isw ? 8 * QD * 2 : 8 * DH * 2;
      const unsigned svo = isw ? wvo : vvo;
      const char* w1s = w1base + (size_t)tw * (64 * FF * 2);
      auto rdN = [&](auto N) {
        constexpr int n = decltype(N)::v, ks = n >> 3, g = n & 7;
        if constexpr (g < 6) return rdV(vb + ((2 * ks) * 6 + g) * 512);
        else if constexpr (g == 6) return rdH(hP + (4 * ks) * 512);
        else return rdH(hQ + (4 * ks) * 512);
      };
      sfor<32>([&](auto I) {
        constexpr int i = decltype(I)::v, ks = i >> 3, g = i & 7;
        if constexpr (g < 6) { mfma_o<AO(0, g)>(fr[i & 3], xc[0][ks]); mfma_o<AO(1, g)>(fr[i & 3], xc[1][ks]); }
        else if constexpr (g == 6) { mfma_qv(hacc[0], fr[i & 3], xc[2][ks]); mfma_qv(hacc[1], fr[i & 3], xc[3][ks]); }
        else mfma_qv(hacc[2], fr[i & 3], xc[2][ks]);
        if constexpr (i + 4 < 32) fr[i & 3] = rdN(IC<(i + 4 < 32 ? i + 4 : 0)>{});
        if constexpr (i < 16)
          gload16<(i & 3) * 1024>(xn[i >> 2][i & 3], (i >> 2) == 0 ? p0 : (i >> 2) == 1 ? p0 + 40960 : (i >> 2) == 2 ? p2 : p3);
        if constexpr (i >= 16 && i < 22) {
          constexpr int pc = i - 16;
          glds_s(ssrc + (size_t)(pc & 1) * sgs + (pc >> 1) * 128, svo, vdst + swr + (pc & 1) * 3072 + (pc >> 1) * 1024);
        }
        if constexpr (i >= 22 && i < 24) glds_s(w1s + (size_t)(i - 22) * (8 * FF * 2), w1vo, w1dst + swr / 3 + (i - 22) * 1024);
        SB();
      });
      srd = nxt(srd);
      swr = nxt(swr);
    };
    asm volatile("s_waitcnt vmcnt(0)" : XOPS(xa) :: "memory");
    asm volatile("" : "+v"(bwv[0][0]), "+v"(bwv[0][1]), "+v"(bwv[0][2]), "+v"(bwv[0][3]), "+v"(bwv[1][0]), "+v"(bwv[1][1]), "+v"(bwv[1][2]), "+v"(bwv[1][3]));
#pragma unroll 1
    for (int t = 0; t < 10; t += 2) { qstep(xa, xb, t); qstep(xb, xa, t + 1); }
    asm volatile("s_waitcnt vmcnt(0)\n\ts_barrier" : XOPS(xa) :: "memory");
  }
#pragma unroll
  for (int pc = 0; pc < 6; ++pc) pieceK(96, TB, pc);
  asm volatile("s_nop 15\n\ts_nop 7" ::: "memory");
  SB();
  {
    const int rbA = (q0 >> 5) + odd, rbB = (q0 >> 5) + 1 - odd, cP = 3 * hj + 2 * odd, cQ = 3 * hj + 1;
#pragma unroll
    for (int k = 0; k < 3; ++k) {
      const int rbk = k == 1 ? rbB : rbA, ck = k == 2 ? cQ : cP;
      float g[16];
#pragma unroll
      for (int q = 0; q < 4; ++q) {
        const u32x2 bw = bwv[k == 2 ? 1 : 0][q];
        const float bs[4] = {__uint_as_float(bw[0] << 16), __uint_as_float(bw[0] & 0xffff0000u), __uint_as_float(bw[1] << 16),
                             __uint_as_float(bw[1] & 0xffff0000u)};
#pragma unroll
        for (int e = 0; e < 4; ++e) g[4 * q + e] = gelu_tanh(rbf(rbf(hacc[k][4 * q + e]) + bs[e]));
      }
      u32x4* ho = (u32x4*)hfr + ((size_t)rbk * 96 + 2 * ck) * 64 + lane;
#pragma unroll
      for (int sidx = 0; sidx < 2; ++sidx)
        ho[sidx * 64] = u32x4{cvtpk(g[8 * sidx], g[8 * sidx + 1]), cvtpk(g[8 * sidx + 2], g[8 * sidx + 3]),
                              cvtpk(g[8 * sidx + 4], g[8 * sidx + 5]), cvtpk(g[8 * sidx + 6], g[8 * sidx + 7])};
    }
  }
  SB();
  bf16x8 qf[2][4];
  sfor<24>([&](auto I) {
    constexpr int n = decltype(I)::v, b = n / 12, f = n % 12, R = AO(b, f >> 1) + 8 * (f & 1);
    float t8[8];
    acc_rd8<R>(t8);
    unsigned w0 = cvtpk(t8[0] * QSCALE, t8[1] * QSCALE);
    unsigned w1 = cvtpk(t8[2] * QSCALE, t8[3] * QSCALE);
    unsigned w2 = cvtpk(t8[4] * QSCALE, t8[5] * QSCALE);
    unsigned w3 = cvtpk(t8[6] * QSCALE, t8[7] * QSCALE);
    auto r0 = __builtin_amdgcn_permlane32_swap(w0, w2, false, false);
    auto r1 = __builtin_amdgcn_permlane32_swap(w1, w3, false, false);
    if constexpr (f < 8) {
      acc_wr4<AQ(b, f)>(r0[0], r1[0], r0[1], r1[1]);
    } else {
      u32x4 w = {r0[0], r1[0], r0[1], r1[1]};
      qf[b][f - 8] = __builtin_bit_cast(bf16x8, w);
    }
  });
  zfr = bf16x8{};
  PIN(zfr);
  SB();
  asm volatile("s_nop 7");
  sfor<12>([&](auto I) { acc_zero16<16 * decltype(I)::v>(zfr); });

  float m_run[2], l_run[2] = {0.f, 0.f}, alpha[2];
  bool pend = false;
  f32x16 S[2][2], nm[2];
  u32x4 pw[2][2];
  unsigned kb[4], vb;
  nm[0] = f32x16{}; nm[1] = f32x16{};
  PIN(nm[0]); PIN(nm[1]);
#pragma unroll
  for (int i = 0; i < 4; ++i) { PIN(qf[0][i]); PIN(qf[1][i]); }
#pragma unroll
  for (int i = 0; i < 4; ++i) fr[i] = rdK(koff[i] + 2 * TB + 4096);
  SB();
  asm volatile("s_nop 3" ::: "memory");
  SB();

  sfor<12>([&](auto I) {
    constexpr int i = decltype(I)::v;
    if constexpr (i == 0) { mfma_first<AQ(0, 0)>(S[0][0], fr[0], nm[0]); mfma_first<AQ(1, 0)>(S[0][1], fr[0], nm[1]); }
    else if constexpr (i < 8) { mfma_qa<AQ(0, i)>(S[0][0], fr[i & 3]); mfma_qa<AQ(1, i)>(S[0][1], fr[i & 3]); }
    else { mfma_qv(S[0][0], fr[i & 3], qf[0][i - 8]); mfma_qv(S[0][1], fr[i & 3], qf[1][i - 8]); }
    if constexpr (i + 4 < 12) fr[i & 3] = rdK(koff[i & 3] + 2 * TB + ((i + 4) >> 2) * 8192 + 4096);
    SB();
  });
  asm volatile("s_nop 15\n\ts_nop 7" :: "v"(nm[0]), "v"(nm[1]) : "memory");
  SB();
#pragma unroll
  for (int b = 0; b < 2; ++b) {
    float mx = S[0][b][0];
#pragma unroll
    for (int r = 1; r < 16; ++r) mx = fmaxf(mx, S[0][b][r]);
    auto rr = __builtin_amdgcn_permlane32_swap(__float_as_uint(mx), __float_as_uint(mx), false, false);
    mx = fmaxf(__uint_as_float(rr[0]), __uint_as_float(rr[1]));
    m_run[b] = mx;
#pragma unroll
    for (int r = 0; r < 16; ++r) { nm[b][r] = -mx; S[0][b][r] -= mx; }
  }
#pragma unroll
  for (int j = 0; j < 4; ++j) kb[j] = koff[j];
  vb = voff;
#pragma unroll
  for (int i = 0; i < 4; ++i) fr[i] = rdK(kb[i]);

  auto phaseA = [&](auto XX, auto HH, const char* ssrc, const unsigned sdst) {
    constexpr int X = decltype(XX)::v, HB = decltype(HH)::v;
    sfor<12>([&](auto I) {
      constexpr int i = decltype(I)::v;
      auto ex = [&](auto N) {
        constexpr int n = decltype(N)::v;
        if constexpr (n < 32) {
          constexpr int b = (n & 15) >> 3, r = (n >> 4) * 8 + (n & 7);
          S[X][b][r] = __builtin_amdgcn_exp2f(S[X][b][r]);
        }
      };
      auto pk = [&](auto N) {
        constexpr int n = decltype(N)::v;
        if constexpr (n >= 0 && n < 16) {
          constexpr int b = (n >> 2) & 1, ks = n >> 3, w = n & 3;
          pw[b][ks][w] = cvtpk(S[X][b][8 * ks + 2 * w], S[X][b][8 * ks + 2 * w + 1]);
        }
      };
      if constexpr (i == 0) mfma_first<AQ(0, 0)>(S[X ^ 1][0], fr[0], nm[0]);
      else if constexpr (i < 8) mfma_qa<AQ(0, i)>(S[X ^ 1][0], fr[i & 3]);
      else mfma_qv(S[X ^ 1][0], fr[i & 3], qf[0][i - 8]);
      SB();
      ex(IC<3 * i>{}); ex(IC<3 * i + 1>{});
      pk(IC<2 * i - 8>{});
      PIN(S[X][0]); PIN(S[X][1]);
      SB();
      if constexpr (i == 0) mfma_first<AQ(1, 0)>(S[X ^ 1][1], fr[0], nm[1]);
      else if constexpr (i < 8) mfma_qa<AQ(1, i)>(S[X ^ 1][1], fr[i & 3]);
      else mfma_qv(S[X ^ 1][1], fr[i & 3], qf[1][i - 8]);
      SB();
      if constexpr (i + 4 < 12) fr[i & 3] = rdK(kb[i & 3] + ((i + 4) >> 2) * 8192 + HB * 4096);
      else fr[i & 3] = rdV(vb + ((4 * HB) * 6 + (i - 8)) * 512);
      ex(IC<3 * i + 2>{});
      pk(IC<2 * i - 7>{});
      if constexpr (i == 2) asm volatile("" :: "v"(nm[0]), "v"(nm[1]));
      if constexpr (i == 1 || i == 5 || i == 9) {
        constexpr int pc = (i - 1) / 4;
        if constexpr (HB == 0) glds_i<(pc & 1) * 1024 + (pc >> 1) * 8192>(ssrc, kpo[pc], sdst);
        else glds_i<(pc & 1) * 3072 + (pc >> 1) * 1024>(ssrc, vpo[pc], sdst);
      }
      PIN(S[X][0]); PIN(S[X][1]); PIN(pw[0][0]); PIN(pw[0][1]); PIN(pw[1][0]); PIN(pw[1][1]);
      SB();
    });
  };
  auto phaseB = [&](auto XX, auto HH, auto HK, const char* ssrc, const unsigned sdst) {
    constexpr int X = decltype(XX)::v, HB = decltype(HH)::v, HBKN = decltype(HK)::v;
    float ls[2][2], mx[2];
    auto fill = [&](auto G) {
      constexpr int g = decltype(G)::v;
      if constexpr (g < 16) {
        constexpr int b = g >> 3, r = (g & 7) * 2;
        if constexpr ((g & 7) == 0) { ls[b][0] = S[X][b][r]; ls[b][1] = S[X][b][r + 1]; }
        else { ls[b][0] += S[X][b][r]; ls[b][1] += S[X][b][r + 1]; }
        PIN(ls[b][0]); PIN(ls[b][1]);
      }
      if constexpr (g == 16) { l_run[0] += ls[0][0] + ls[0][1]; PIN(l_run[0]); }
      if constexpr (g == 17) { l_run[1] += ls[1][0] + ls[1][1]; PIN(l_run[1]); }
      if constexpr (g == 20) {
#pragma unroll
        for (int b = 0; b < 2; ++b) {
          auto rr = __builtin_amdgcn_permlane32_swap(__float_as_uint(mx[b]), __float_as_uint(mx[b]), false, false);
          mx[b] = max2f(__uint_as_float(rr[0]), __uint_as_float(rr[1]));
          PIN(mx[b]);
        }
      }
      if constexpr (g == 21) {
        if (__builtin_expect(__any(max2f(mx[0], mx[1]) > THR), 0)) {
          const float aone = hi == 0 ? 1.f : 0.f;
#pragma unroll
          for (int b = 0; b < 2; ++b) {
            const float dl = fmaxf(mx[b], 0.f);
            m_run[b] += dl;
            alpha[b] = __builtin_amdgcn_exp2f(-dl);
            bcast16(nm[b], aone, -m_run[b]);
#pragma unroll
            for (int r = 0; r < 16; ++r) S[X ^ 1][b][r] -= dl;
          }
          pend = true;
        }
      }
      if constexpr (g == 16 || g == 18 || g == 22) {
        constexpr int pc = g == 16 ? 3 : (g == 18 ? 4 : 5);
        if constexpr (HB == 0) glds_i<(pc & 1) * 1024 + (pc >> 1) * 8192>(ssrc, kpo[pc], sdst);
        else glds_i<(pc & 1) * 3072 + (pc >> 1) * 1024>(ssrc, vpo[pc], sdst);
      }
    };
    auto pvm = [&](auto G) {
      constexpr int g = decltype(G)::v, i = g >> 1, b = g & 1, k = (g - 4) >> 1;
      const bf16x8 pb = __builtin_bit_cast(bf16x8, pw[b][i / 6]);
      if constexpr (g >= 4 && g < 20) {
        if constexpr (k == 0) mfma_o_mx<AO(b, i % 6)>(fr[i & 3], pb, mx[b], S[X ^ 1][b][0], S[X ^ 1][b][1], S[X ^ 1][b][2]);
        else if constexpr (k < 7) mfma_o_mx<AO(b, i % 6)>(fr[i & 3], pb, mx[b], mx[b], S[X ^ 1][b][2 * k + 1], S[X ^ 1][b][2 * k + 2]);
        else mfma_o_mx<AO(b, i % 6)>(fr[i & 3], pb, mx[b], mx[b], S[X ^ 1][b][15], S[X ^ 1][b][15]);
      } else mfma_o<AO(b, i % 6)>(fr[i & 3], pb);
    };
    sfor<12>([&](auto I) {
      constexpr int i = decltype(I)::v;
      pvm(IC<2 * i>{});
      SB();
      fill(IC<2 * i>{});
      SB();
      pvm(IC<2 * i + 1>{});
      SB();
      if constexpr (i + 4 < 12) fr[i & 3] = rdV(vb + ((4 * HB + 2 * ((i + 4) / 6)) * 6 + ((i + 4) % 6)) * 512);
      else fr[i & 3] = rdK(kb[i - 8] + HBKN * 4096);
      fill(IC<2 * i + 1>{});
      SB();
    });
    if (__builtin_expect(pend, 0)) {
      asm volatile("s_nop 15\n\ts_nop 7" ::: "memory");
      l_run[0] *= alpha[0]; l_run[1] *= alpha[1];
      sfor<192>([&](auto I) { constexpr int n = decltype(I)::v; acc_scale<n>(alpha[n / 96]); });
      asm volatile("s_nop 3" ::: "memory");
      pend = false;
    }
  };

  asm volatile("s_waitcnt vmcnt(0)" ::: "memory");
  unsigned scur = 0;
#pragma unroll 1
  for (int t = 0; t < 32; ++t) {
    const unsigned snxt = nxt(scur), swr = nxt(snxt);
    asm volatile("s_waitcnt vmcnt(7)\n\ts_barrier" ::: "memory");
    int tk = t + 2 < 32 ? t + 2 : 31;
    const int tv = tk;
    if (tk == 31 && wid >= 2) tk = 30;
    const char* ks_ = kbase + (size_t)(tk * 64 + 32) * (DH * 2);
    const char* vs_ = vbase + (size_t)(tv * 64) * (DH * 2);
    SB();
    phaseA(IC<0>{}, IC<0>{}, ks_, kdst + swr);
    phaseB(IC<0>{}, IC<0>{}, IC<1>{}, ks_, kdst + swr);
    phaseA(IC<1>{}, IC<1>{}, vs_, vdst + swr);
#pragma unroll
    for (int j = 0; j < 4; ++j) kb[j] = koff[j] + snxt;
    phaseB(IC<1>{}, IC<1>{}, IC<0>{}, vs_, vdst + swr);
    {
      const int pid = (t % 10) * 4 + wid;
      const char* pf = pid < 30 ? (const char*)wo + (size_t)(xcd >> 1) * 1966080 + ((xcd & 1) * 32 + idx) * 30720 + pid * 1024
                                : (const char*)w2 + min(bid * 7680 + (pid - 30) * 1024, 1966080 - 1024);
      glds_s(pf, (unsigned)lane * 16, lds0 + 6 * TB + wid * 1024);
    }
    vb = voff + snxt;
    scur = snxt;
  }
  asm volatile("s_waitcnt vmcnt(0)\n\ts_nop 15\n\ts_nop 7" ::: "memory");
  int tz = threadIdx.x;
  asm volatile("" : "+v"(tz));
  float rls[2];
#pragma unroll
  for (int b = 0; b < 2; ++b) {
    auto rr = __builtin_amdgcn_permlane32_swap(__float_as_uint(l_run[b]), __float_as_uint(l_run[b]), false, false);
    rls[b] = 1.0f / (__uint_as_float(rr[0]) + __uint_as_float(rr[1]));
  }
  u32x4* ofr = (u32x4*)O + ((size_t)(qb * 8 + (tz >> 6) * 2) * 384 + head * 12) * 64 + (tz & 63);
  sfor<24>([&](auto I) {
    constexpr int n = decltype(I)::v, b = n / 12, f = n % 12, R = AO(b, f >> 1) + 8 * (f & 1);
    const float rl = rls[b];
    float t8[8];
    acc_rd8<R>(t8);
    __builtin_nontemporal_store(u32x4{cvtpk(t8[0] * rl, t8[1] * rl), cvtpk(t8[2] * rl, t8[3] * rl),
                                      cvtpk(t8[4] * rl, t8[5] * rl), cvtpk(t8[6] * rl, t8[7] * rl)}, &ofr[(b * 384 + f) * 64]);
  });
}

extern "C" void kernel_launch(void* const* d_in, const int* in_sizes, int n_in, void* d_out, int out_size,
                              void* d_ws, size_t ws_size, hipStream_t stream) {
  const u16* x = (const u16*)d_in[0];
  const u16* kc = (const u16*)d_in[1];
  const u16* vc = (const u16*)d_in[2];
  const u16* wq = (const u16*)d_in[3];
  const u16* wo = (const u16*)d_in[4];
  const u16* w1 = (const u16*)d_in[6];
  const u16* b1 = (const u16*)d_in[7];
  const u16* w2 = (const u16*)d_in[8];
  const u16* b2 = (const u16*)d_in[9];
  char* ws = (char*)d_ws;
  u16* xnp = (u16*)(ws);
  u16* y = (u16*)(ws + (4u << 20));
  u16* h = (u16*)(ws + (8u << 20));
  u16* O = (u16*)(ws + (16u << 20));
  u16* part = (u16*)(ws + (48u << 20));

  hipFuncSetAttribute((const void*)k_attn, hipFuncAttributeMaxDynamicSharedMemorySize, 6 * TB + 4096);
  hipFuncSetAttribute((const void*)k_gemm2, hipFuncAttributeMaxDynamicSharedMemorySize, 4 * 16384);

  k_ln<<<SEQ / 4, 256, 0, stream>>>(x, xnp, y);
  k_attn<<<256, 256, 6 * TB + 4096, stream>>>(xnp, wq, kc, vc, O, y, w1, b1, h, wo, w2);
  k_gemm2<<<200, 256, 4 * 16384, stream>>>(O, wo, h, w2, part);
  k_final<<<SEQ * DM / 2048, 256, 0, stream>>>(x, part, b2, (u16*)d_out);
}
```

```cpp
#include <hip/hip_runtime.h>
#include <hip/hip_bf16.h>
#include <stdint.h>

typedef __attribute__((ext_vector_type(8))) short bf16x8;
typedef __attribute__((ext_vector_type(8))) __bf16 bf16x8b;
typedef __attribute__((ext_vector_type(4))) short s16x4;
typedef __attribute__((ext_vector_type(16))) float f32x16;
typedef __attribute__((ext_vector_type(4))) float f32x4;
typedef __attribute__((ext_vector_type(2))) float f32x2;
typedef __attribute__((ext_vector_type(2))) __bf16 bf16x2b;
typedef __attribute__((ext_vector_type(4))) unsigned u32x4;
typedef __attribute__((ext_vector_type(2))) unsigned u32x2;
typedef __attribute__((address_space(3))) char* lds_ptr;
typedef unsigned short u16;

#define DI __device__ __forceinline__

constexpr int SEQ = 2048, DM = 640, NH = 32, DH = 192, FF = 1536, QD = NH * DH;

DI f32x16 MFMA(bf16x8 a, bf16x8 b, f32x16 c) {
  return __builtin_amdgcn_mfma_f32_32x32x16_bf16(__builtin_bit_cast(bf16x8b, a), __builtin_bit_cast(bf16x8b, b), c, 0, 0, 0);
}
DI unsigned cvtpk(float lo, float hi) {
  f32x2 v = {lo, hi};
  bf16x2b b = __builtin_convertvector(v, bf16x2b);
  return __builtin_bit_cast(unsigned, b);
}
DI float rbf(float v) { return __uint_as_float(cvtpk(v, 0.f) << 16); }
DI s16x4 trread(lds_ptr p) {
  return __builtin_amdgcn_ds_read_tr16_b64_v4i16((__attribute__((address_space(3))) s16x4*)p);
}
DI bf16x8 ldsread(lds_ptr p) { return *(__attribute__((address_space(3))) bf16x8*)p; }
DI bf16x8 cat(s16x4 a, s16x4 b) { return __builtin_shufflevector(a, b, 0, 1, 2, 3, 4, 5, 6, 7); }

DI float wave_sum(float v) {
#pragma unroll
  for (int o = 32; o > 0; o >>= 1) v += __shfl_xor(v, o, 64);
  return v;
}
DI void ln_row(float (&v)[10]) {
  float s = 0.f;
#pragma unroll
  for (int i = 0; i < 10; ++i) s += v[i];
  s = wave_sum(s);
  const float mean = s * (1.0f / DM);
  float q = 0.f;
#pragma unroll
  for (int i = 0; i < 10; ++i) { float d = v[i] - mean; q += d * d; }
  q = wave_sum(q);
  const float mb = rbf(mean), vb = rbf(q * (1.0f / DM));
  const float den = rbf(sqrtf(rbf(vb + 1e-5f)));
#pragma unroll
  for (int i = 0; i < 10; ++i) v[i] = rbf(rbf(v[i] - mb) / den);
}
__global__ __launch_bounds__(256) void k_ln(const u16* __restrict__ x, u16* __restrict__ xnp, u16* __restrict__ y) {
  const int row = blockIdx.x * 4 + (threadIdx.x >> 6), lane = threadIdx.x & 63;
  const unsigned* xr = (const unsigned*)(x + (size_t)row * DM);
  float v[10];
#pragma unroll
  for (int i = 0; i < 5; ++i) {
    unsigned w = xr[lane + 64 * i];
    v[2 * i] = __uint_as_float(w << 16);
    v[2 * i + 1] = __uint_as_float(w & 0xffff0000u);
  }
  ln_row(v);
  unsigned* xo = (unsigned*)xnp + (size_t)(row >> 5) * (40 * 64 * 4) + (row & 31) * 4;
#pragma unroll
  for (int i = 0; i < 5; ++i) {
    int e = lane + 64 * i;
    int ep = (e & ~6) | ((e & 2) << 1) | ((e & 4) >> 1);
    xo[(ep >> 3) * 256 + ((ep >> 2) & 1) * 128 + (ep & 3)] =
        (__float_as_uint(v[2 * i]) >> 16) | (__float_as_uint(v[2 * i + 1]) & 0xffff0000u);
  }
  ln_row(v);
  unsigned* yo = (unsigned*)y + (size_t)(row >> 5) * (40 * 64 * 4) + (row & 31) * 4;
#pragma unroll
  for (int i = 0; i < 5; ++i) {
    int e = lane + 64 * i;
    int ep = (e & ~6) | ((e & 2) << 1) | ((e & 4) >> 1);
    yo[(ep >> 3) * 256 + ((ep >> 2) & 1) * 128 + (ep & 3)] =
        (__float_as_uint(v[2 * i]) >> 16) | (__float_as_uint(v[2 * i + 1]) & 0xffff0000u);
  }
}

#define SB() __builtin_amdgcn_sched_barrier(0)
#define PIN(x) asm volatile("" : "+v"(x))
template <int I> struct IC { static constexpr int v = I; };
template <int N, class F> DI void sfor(F&& f) {
  if constexpr (N > 0) { sfor<N - 1>(f); f(IC<N - 1>{}); }
}
template <int IMM> DI void glds_i(const void* sbase, unsigned voff, unsigned ldsbase) {
  asm volatile("s_add_i32 m0, %2, %3\n\ts_nop 0\n\tglobal_load_lds_dwordx4 %0, %1"
               :: "v"(voff), "s"(sbase), "s"(ldsbase), "i"(IMM) : "memory", "scc");
}
template <int OFF> DI void gload16(bf16x8& d, const char* p) {
  asm volatile("global_load_dwordx4 %0, %1, off offset:%2" : "=&v"(d) : "v"(p), "i"(OFF) : "memory");
}
template <int OFF> DI void gload8(u32x2& d, const void* p) {
  asm volatile("global_load_dwordx2 %0, %1, off offset:%2" : "=&v"(d) : "v"(p), "i"(OFF) : "memory");
}
template <int N> DI void xwait2(bf16x8 (&x)[2][4]) {
  asm volatile("s_waitcnt vmcnt(%8)\n\ts_barrier" : "+v"(x[0][0]), "+v"(x[0][1]), "+v"(x[0][2]), "+v"(x[0][3]), "+v"(x[1][0]), "+v"(x[1][1]), "+v"(x[1][2]), "+v"(x[1][3]) : "i"(N) : "memory");
}
DI void xpin2(bf16x8 (&x)[2][4]) {
  asm volatile("" : "+v"(x[0][0]), "+v"(x[0][1]), "+v"(x[0][2]), "+v"(x[0][3]), "+v"(x[1][0]), "+v"(x[1][1]), "+v"(x[1][2]), "+v"(x[1][3]));
}

DI void wgemm24(const char* wbase, unsigned ldwb, const char* xp, unsigned xrbs, int ktiles, unsigned lds0,
                f32x16 (&acc)[2][4]) {
  constexpr int NB = 4;
  constexpr unsigned TBY = 4096 * NB;
  const int tid = threadIdx.x, lane = tid & 63, wid = __builtin_amdgcn_readfirstlane(tid >> 6);
  const int hi = lane >> 5;
  unsigned wpo[NB];
#pragma unroll
  for (int i = 0; i < NB; ++i) {
    const int sub = 2 * (wid * NB + i) + hi, kg = sub / NB, nb = sub % NB;
    wpo[i] = (unsigned)(8 * kg + ((lane >> 2) & 7)) * ldwb + nb * 64 + (lane & 3) * 16;
  }
  const unsigned wdst = lds0 + wid * NB * 1024;
  const unsigned voff = lds0 + (hi * 4 + ((lane >> 2) & 3)) * 64 + ((lane >> 4) & 1) * 32 + (lane & 3) * 8;
  auto rdW = [&](unsigned a) { return cat(trread((lds_ptr)(uintptr_t)a), trread((lds_ptr)(uintptr_t)(a + NB * 512))); };
#pragma unroll
  for (int r = 0; r < 2; ++r)
#pragma unroll
    for (int n = 0; n < NB; ++n) acc[r][n] = f32x16{};
  bf16x8 x0[2][4], x1[2][4], x2[2][4], x3[2][4], fr[4];
  const size_t wts = (size_t)64 * ldwb;
  auto issue = [&](bf16x8 (&x)[2][4], int t, auto SLOT) {
    const char* xq = xp + (size_t)t * 4096;
    sfor<8>([&](auto I) { constexpr int i = decltype(I)::v; gload16<(i & 3) * 1024>(x[i >> 2][i & 3], xq + (i >> 2) * xrbs); });
    sfor<NB>([&](auto I) { glds_i<decltype(SLOT)::v * TBY + decltype(I)::v * 1024>(wbase + (size_t)t * wts, wpo[decltype(I)::v], wdst); });
  };
  issue(x0, 0, IC<0>{});
  issue(x1, 1, IC<1>{});
  issue(x2, 2, IC<2>{});
  asm volatile("s_waitcnt vmcnt(24)\n\ts_barrier" ::: "memory");
#pragma unroll
  for (int i = 0; i < 4; ++i) fr[i] = rdW(voff + i * 512);
  auto step = [&](bf16x8 (&xc)[2][4], bf16x8 (&xn)[2][4], const int t, auto SLOT) {
    constexpr int sl = decltype(SLOT)::v, sw = (sl + 3) & 3;
    xwait2<12>(xc);
    const unsigned vb = voff + sl * TBY, vbn = voff + ((sl + 1) & 3) * TBY;
    const int tn = t + 3 < ktiles ? t + 3 : ktiles - 1;
    const char* xq = xp + (size_t)tn * 4096;
    const char* wq_ = wbase + (size_t)tn * wts;
    SB();
    sfor<16>([&](auto I) {
      constexpr int i = decltype(I)::v;
      acc[0][i & 3] = MFMA(fr[i & 3], xc[0][i >> 2], acc[0][i & 3]);
      acc[1][i & 3] = MFMA(fr[i & 3], xc[1][i >> 2], acc[1][i & 3]);
      SB();
      if constexpr (i + 4 < 16) fr[i & 3] = rdW(vb + ((2 * ((i + 4) >> 2)) * NB + (i & 3)) * 512);
      else fr[i & 3] = rdW(vbn + (i & 3) * 512);
      if constexpr (i < 8) gload16<(i & 3) * 1024>(xn[i >> 2][i & 3], xq + (i >> 2) * xrbs);
      if constexpr (i >= 8 && i < 12) glds_i<sw * TBY + (i - 8) * 1024>(wq_, wpo[i - 8], wdst);
      SB();
    });
  };
#pragma unroll 1
  for (int t = 0; t < ktiles; t += 4) {
    step(x0, x3, t, IC<0>{});
    step(x1, x0, t + 1, IC<1>{});
    step(x2, x1, t + 2, IC<2>{});
    step(x3, x2, t + 3, IC<3>{});
  }
  asm volatile("s_waitcnt vmcnt(0)" ::: "memory");
  xpin2(x0); xpin2(x1); xpin2(x2);
}

DI float gelu_tanh(float v) {
  const float u = 0.7978845608028654f * (v + 0.044715f * v * v * v);
  const float e = __builtin_amdgcn_exp2f(2.8853900817779268f * u);
  return v - v * __builtin_amdgcn_rcpf(e + 1.f);
}

__global__ __launch_bounds__(256, 1) void k_gemm2(const u16* __restrict__ O, const u16* __restrict__ wo,
                                                  const u16* __restrict__ h, const u16* __restrict__ w2,
                                                  u16* __restrict__ part) {
  extern __shared__ __attribute__((aligned(16))) char smem[];
  const int bid = blockIdx.x, x = bid & 7, r = bid >> 3;
  const int nt = r % 5, c = r < 20 ? (x >> 1) : 4, mt = r < 20 ? 4 * (x & 1) + r / 5 : x;
  const int lane = threadIdx.x & 63, wid = threadIdx.x >> 6;
  const int rb0 = mt * 8 + wid * 2;
  const char* W; const char* X; unsigned xrbs;
  if (c < 4) {
    W = (const char*)(wo + (size_t)c * 24 * 64 * DM + nt * 128);
    xrbs = 384 * 1024;
    X = (const char*)O + ((size_t)rb0 * 384 + c * 96) * 1024 + lane * 16;
  } else {
    W = (const char*)(w2 + nt * 128);
    xrbs = 96 * 1024;
    X = (const char*)h + (size_t)rb0 * 96 * 1024 + lane * 16;
  }
  f32x16 acc[2][4];
  wgemm24(W, DM * 2, X, xrbs, 24, (unsigned)(uintptr_t)(lds_ptr)smem, acc);
  u32x4* P = (u32x4*)part + (size_t)c * (SEQ * DM / 8);
#pragma unroll
  for (int b = 0; b < 2; ++b)
#pragma unroll
    for (int nb = 0; nb < 4; ++nb)
#pragma unroll
      for (int hq = 0; hq < 2; ++hq)
        P[(((size_t)(rb0 + b) * 20 + nt * 4 + nb) * 2 + hq) * 64 + lane] =
            u32x4{cvtpk(acc[b][nb][8 * hq], acc[b][nb][8 * hq + 1]), cvtpk(acc[b][nb][8 * hq + 2], acc[b][nb][8 * hq + 3]),
                  cvtpk(acc[b][nb][8 * hq + 4], acc[b][nb][8 * hq + 5]), cvtpk(acc[b][nb][8 * hq + 6], acc[b][nb][8 * hq + 7])};
}

DI float blo(unsigned w) { return __uint_as_float(w << 16); }
DI float bhi(unsigned w) { return __uint_as_float(w & 0xffff0000u); }
__global__ __launch_bounds__(256) void k_final(const u16* __restrict__ x, const u16* __restrict__ part,
                                               const u16* __restrict__ b2, u16* __restrict__ out) {
  const int idx = blockIdx.x * 256 + threadIdx.x;
  constexpr size_t PS = (size_t)SEQ * DM / 8;
  const u32x4* P = (const u32x4*)part + idx;
  const u32x4 p0 = P[0], p1 = P[PS], p2 = P[2 * PS], p3 = P[3 * PS], pm = P[4 * PS];
  const int lane = idx & 63, hq = (idx >> 6) & 1, blk = idx >> 7, cb = blk % 20, rb = blk / 20;
  const int m = rb * 32 + (lane & 31), n0 = cb * 32 + 16 * hq + 4 * (lane >> 5);
#pragma unroll
  for (int q = 0; q < 2; ++q) {
    const int n = n0 + 8 * q;
    const u32x2 xw = *(const u32x2*)(x + (size_t)m * DM + n);
    const u32x2 bw = *(const u32x2*)(b2 + n);
    float r[4];
#pragma unroll
    for (int i = 0; i < 4; ++i) {
      const int w = 2 * q + (i >> 1);
      const float a0 = (i & 1) ? bhi(p0[w]) : blo(p0[w]), a1 = (i & 1) ? bhi(p1[w]) : blo(p1[w]);
      const float a2 = (i & 1) ? bhi(p2[w]) : blo(p2[w]), a3 = (i & 1) ? bhi(p3[w]) : blo(p3[w]);
      const float am = (i & 1) ? bhi(pm[w]) : blo(pm[w]);
      const float xo = (i & 1) ? bhi(xw[i >> 1]) : blo(xw[i >> 1]), bo = (i & 1) ? bhi(bw[i >> 1]) : blo(bw[i >> 1]);
      const float sv = rbf((a0 + a1) + (a2 + a3));
      const float mm = rbf(am + bo);
      const float g = rbf(mm / (1.f + __expf(-mm)));
      r[i] = xo + rbf(sv * g);
    }
    *(u32x2*)(out + (size_t)m * DM + n) = u32x2{cvtpk(r[0], r[1]), cvtpk(r[2], r[3])};
  }
}

constexpr int TB = 24576;
constexpr float QSCALE = 0.07216878364870322f * 1.4426950408889634f;
constexpr float THR = 20.0f;

DI void glds_s(const void* sbase, unsigned voff, unsigned ldsaddr) {
  asm volatile("s_mov_b32 m0, %2\n\ts_nop 0\n\tglobal_load_lds_dwordx4 %0, %1"
               :: "v"(voff), "s"(sbase), "s"(ldsaddr) : "memory");
}
DI float max2f(float a, float b) { float r; asm("v_max_f32_e32 %0, %1, %2" : "=v"(r) : "v"(a), "v"(b)); return r; }
DI void bcast16(f32x16& d, float aone, float negm) {
  asm volatile("s_nop 1\n\tv_mfma_f32_32x32x2_f32 %0, %1, %2, 0" : "+v"(d) : "v"(aone), "v"(negm));
}
DI bf16x8 rdK(unsigned a) { return ldsread((lds_ptr)(uintptr_t)a); }
DI bf16x8 rdV(unsigned a) { return cat(trread((lds_ptr)(uintptr_t)a), trread((lds_ptr)(uintptr_t)(a + 3072))); }
DI unsigned nxt(unsigned s) { return s == 2 * TB ? 0 : s + TB; }
#define ACLOB "a0","a1","a2","a3","a4","a5","a6","a7","a8","a9","a10","a11","a12","a13","a14","a15","a16","a17","a18","a19","a20","a21","a22","a23","a24","a25","a26","a27","a28","a29","a30","a31","a32","a33","a34","a35","a36","a37","a38","a39","a40","a41","a42","a43","a44","a45","a46","a47","a48","a49","a50","a51","a52","a53","a54","a55","a56","a57","a58","a59","a60","a61","a62","a63","a64","a65","a66","a67","a68","a69","a70","a71","a72","a73","a74","a75","a76","a77","a78","a79","a80","a81","a82","a83","a84","a85","a86","a87","a88","a89","a90","a91","a92","a93","a94","a95","a96","a97","a98","a99","a100","a101","a102","a103","a104","a105","a106","a107","a108","a109","a110","a111","a112","a113","a114","a115","a116","a117","a118","a119","a120","a121","a122","a123","a124","a125","a126","a127","a128","a129","a130","a131","a132","a133","a134","a135","a136","a137","a138","a139","a140","a141","a142","a143","a144","a145","a146","a147","a148","a149","a150","a151","a152","a153","a154","a155","a156","a157","a158","a159","a160","a161","a162","a163","a164","a165","a166","a167","a168","a169","a170","a171","a172","a173","a174","a175","a176","a177","a178","a179","a180","a181","a182","a183","a184","a185","a186","a187","a188","a189","a190","a191","a192","a193","a194","a195","a196","a197","a198","a199","a200","a201","a202","a203","a204","a205","a206","a207","a208","a209","a210","a211","a212","a213","a214","a215","a216","a217","a218","a219","a220","a221","a222","a223","a224","a225","a226","a227","a228","a229","a230","a231","a232","a233","a234","a235","a236","a237","a238","a239","a240","a241","a242","a243","a244","a245","a246","a247","a248","a249","a250","a251","a252","a253","a254","a255"
constexpr int AO(int b, int db) { return 16 * (6 * b + db); }
constexpr int AQ(int b, int i) { return 192 + 32 * b + 4 * i; }
template <int R> DI void mfma_o(const bf16x8& a, const bf16x8& b) {
  asm volatile("v_mfma_f32_32x32x16_bf16 a[%c2:%c3], %0, %1, a[%c2:%c3]" :: "v"(a), "v"(b), "i"(R), "i"(R + 15) : ACLOB);
}
template <int R> DI void mfma_o_mx(const bf16x8& a, const bf16x8& b, float& m, float x, float y, float z) {
  asm volatile("v_mfma_f32_32x32x16_bf16 a[%c6:%c7], %1, %2, a[%c6:%c7]\n\tv_max3_f32 %0, %3, %4, %5"
               : "=v"(m) : "v"(a), "v"(b), "v"(x), "v"(y), "v"(z), "i"(R), "i"(R + 15) : ACLOB);
}
template <int R> DI void mfma_first(f32x16& d, const bf16x8& a, const f32x16& c) {
  asm volatile("v_mfma_f32_32x32x16_bf16 %0, %1, a[%c3:%c4], %2" : "=&v"(d) : "v"(a), "v"(c), "i"(R), "i"(R + 3) : ACLOB);
}
template <int R> DI void mfma_qa(f32x16& d, const bf16x8& a) {
  asm volatile("v_mfma_f32_32x32x16_bf16 %0, %1, a[%c2:%c3], %0" : "+v"(d) : "v"(a), "i"(R), "i"(R + 3) : ACLOB);
}
DI void mfma_qv(f32x16& d, const bf16x8& a, const bf16x8& b) {
  asm volatile("v_mfma_f32_32x32x16_bf16 %0, %1, %2, %0" : "+v"(d) : "v"(a), "v"(b));
}
template <int R> DI void acc_zero16(const bf16x8& z) {
  asm volatile("v_mfma_f32_32x32x16_bf16 a[%c1:%c2], %0, %0, 0" :: "v"(z), "i"(R), "i"(R + 15) : ACLOB);
}
template <int R> DI void acc_rd8(float (&r)[8]) {
  asm volatile("v_accvgpr_read_b32 %0, a[%c8]\n\tv_accvgpr_read_b32 %1, a[%c9]\n\tv_accvgpr_read_b32 %2, a[%c10]\n\tv_accvgpr_read_b32 %3, a[%c11]\n\t"
               "v_accvgpr_read_b32 %4, a[%c12]\n\tv_accvgpr_read_b32 %5, a[%c13]\n\tv_accvgpr_read_b32 %6, a[%c14]\n\tv_accvgpr_read_b32 %7, a[%c15]"
               : "=v"(r[0]), "=v"(r[1]), "=v"(r[2]), "=v"(r[3]), "=v"(r[4]), "=v"(r[5]), "=v"(r[6]), "=v"(r[7])
               : "i"(R), "i"(R + 1), "i"(R + 2), "i"(R + 3), "i"(R + 4), "i"(R + 5), "i"(R + 6), "i"(R + 7));
}
template <int R> DI void acc_wr4(unsigned a, unsigned b, unsigned c, unsigned d) {
  asm volatile("v_accvgpr_write_b32 a[%c4], %0\n\tv_accvgpr_write_b32 a[%c5], %1\n\tv_accvgpr_write_b32 a[%c6], %2\n\tv_accvgpr_write_b32 a[%c7], %3"
               :: "v"(a), "v"(b), "v"(c), "v"(d), "i"(R), "i"(R + 1), "i"(R + 2), "i"(R + 3) : ACLOB);
}
template <int R> DI void acc_scale(float f) {
  float t;
  asm volatile("v_accvgpr_read_b32 %0, a[%c2]\n\tv_mul_f32 %0, %1, %0\n\tv_accvgpr_write_b32 a[%c2], %0" : "=&v"(t) : "v"(f), "i"(R) : ACLOB);
}

__global__ __launch_bounds__(256, 1) void k_attn(const u16* __restrict__ xnp, const u16* __restrict__ wq,
                                                 const u16* __restrict__ Kc, const u16* __restrict__ Vc,
                                                 u16* __restrict__ O, const u16* __restrict__ yfr,
                                                 const u16* __restrict__ w1, const u16* __restrict__ b1,
                                                 u16* __restrict__ hfr) {
  extern __shared__ __attribute__((aligned(16))) char smem[];
  const unsigned lds0 = (unsigned)(uintptr_t)(lds_ptr)smem;
  const int tid = threadIdx.x, lane = tid & 63, wid = __builtin_amdgcn_readfirstlane(tid >> 6);
  const int r32 = lane & 31, hi = lane >> 5;
  const int bid = blockIdx.x, xcd = bid & 7, idx = bid >> 3;
  const int head = xcd * 4 + (idx >> 3), qb = idx & 7;
  const int q0 = qb * 256 + wid * 64;

  const int krl = lane >> 3;
  const unsigned kvo0 = (unsigned)(krl * DH * 2 + (((lane & 7) ^ (krl >> 1)) << 4));
  const unsigned kvo1 = (unsigned)(krl * DH * 2 + (((lane & 7) ^ (4 + (krl >> 1))) << 4));
  const unsigned vvo = (unsigned)(((lane >> 2) & 7) * DH * 2 + (lane >> 5) * 64 + (lane & 3) * 16);
  const unsigned wvo = (unsigned)(((lane >> 2) & 7) * QD * 2 + (lane >> 5) * 64 + (lane & 3) * 16);
  const char* kbase = (const char*)(Kc + (size_t)head * SEQ * DH) + (size_t)(16 * wid) * DH * 2;
  const char* vbase = (const char*)(Vc + (size_t)head * SEQ * DH) + (size_t)(16 * wid) * DH * 2;
  const char* wbase = (const char*)(wq + head * DH) + (size_t)(16 * wid) * QD * 2;
  const unsigned kdst = lds0 + wid * 2048, vdst = lds0 + 3 * TB + wid * 6144;
  auto pieceK = [&](int key0, unsigned slot, int pc) {
    glds_s(kbase + (size_t)key0 * (DH * 2) + (pc & 1) * (8 * DH * 2) + (pc >> 1) * 128, (pc & 1) ? kvo1 : kvo0,
           kdst + slot + (pc & 1) * 1024 + (pc >> 1) * 8192);
  };
  auto pieceW = [&](int t, unsigned slot, int pc) {
    glds_s(wbase + (size_t)t * (64 * QD * 2) + (size_t)(pc & 1) * (8 * QD * 2) + (pc >> 1) * 128, wvo,
           vdst + slot + (pc & 1) * 3072 + (pc >> 1) * 1024);
  };
  unsigned kpo[6], vpo[6];
#pragma unroll
  for (int pc = 0; pc < 6; ++pc) {
    kpo[pc] = ((pc & 1) ? kvo1 : kvo0) + (pc & 1) * (8 * DH * 2) + (pc >> 1) * 128;
    vpo[pc] = vvo + (pc & 1) * (8 * DH * 2) + (pc >> 1) * 128;
  }
  unsigned koff[4];
#pragma unroll
  for (int j = 0; j < 4; ++j) koff[j] = lds0 + r32 * 128 + (((2 * j + hi) ^ ((r32 >> 1) & 7)) << 4);
  const unsigned voff = lds0 + 3 * TB + (hi * 4 + ((lane >> 2) & 3)) * 64 + ((lane >> 4) & 1) * 32 + (lane & 3) * 8;

  bf16x8 zfr = bf16x8{};
  PIN(zfr);
  asm volatile("s_nop 3");
  sfor<12>([&](auto I) { acc_zero16<16 * decltype(I)::v>(zfr); });
  bf16x8 fr[4];

  const int odd = head & 1, hj = head >> 1;
  const char* xsrc0 = (const char*)xnp + ((size_t)(q0 >> 5) * (40 * 64) + lane) * 16;
  const char* ysrcA = (const char*)yfr + ((size_t)((q0 >> 5) + odd) * (40 * 64) + lane) * 16;
  const char* ysrcB = (const char*)yfr + ((size_t)((q0 >> 5) + 1 - odd) * (40 * 64) + lane) * 16;
  const char* w1base = (const char*)(w1 + (3 * hj + odd) * 32) + (size_t)(16 * wid) * (FF * 2);
  const unsigned w1vo = (unsigned)(((lane >> 2) & 7) * FF * 2 + (lane >> 5) * 64 + (lane & 3) * 16);
  const unsigned w1dst = lds0 + TB + wid * 2048;
  const unsigned w1rd = lds0 + TB + (hi * 4 + ((lane >> 2) & 3)) * 64 + ((lane >> 4) & 1) * 32 + (lane & 3) * 8;
  const unsigned w1P = w1rd + (odd ? 512 : 0), w1Q = w1rd + (odd ? 0 : 512);
  auto rdH = [&](unsigned a) { return cat(trread((lds_ptr)(uintptr_t)a), trread((lds_ptr)(uintptr_t)(a + 1024))); };
  u32x2 bwv[2][4];
  {
    const u16* bp = b1 + (3 * hj + 2 * odd) * 32 + 4 * hi;
    const u16* bq = b1 + (3 * hj + 1) * 32 + 4 * hi;
    sfor<8>([&](auto I) {
      constexpr int i = decltype(I)::v;
      gload8<(i & 3) * 16>(bwv[i >> 2][i & 3], i < 4 ? bp : bq);
    });
  }
  bf16x8 xa[4][4], xb[4][4];
  f32x16 hacc[3];
  hacc[0] = f32x16{}; hacc[1] = f32x16{}; hacc[2] = f32x16{};
  PIN(hacc[0]); PIN(hacc[1]); PIN(hacc[2]);
#pragma unroll
  for (int pc = 0; pc < 6; ++pc) pieceW(0, 2 * TB, pc);
#pragma unroll
  for (int pc = 0; pc < 6; ++pc) pieceW(1, 0, pc);
#pragma unroll
  for (int t = 0; t < 2; ++t)
#pragma unroll
    for (int g = 0; g < 2; ++g)
      glds_s(w1base + (size_t)t * (64 * FF * 2) + (size_t)g * (8 * FF * 2), w1vo, w1dst + (t == 0 ? 2 : 0) * 8192 + g * 1024);
  sfor<16>([&](auto I) {
    constexpr int i = decltype(I)::v;
    gload16<(i & 3) * 1024>(xa[i >> 2][i & 3], (i >> 2) == 0 ? xsrc0 : (i >> 2) == 1 ? xsrc0 + 40960 : (i >> 2) == 2 ? ysrcA : ysrcB);
  });
  if (wid >= 2) {
#pragma unroll
    for (int pc = 0; pc < 6; ++pc) pieceK(-32, 2 * TB, pc);
  }
#pragma unroll
  for (int pc = 0; pc < 6; ++pc) pieceK(32, 0, pc);
#define XOPS(x) "+v"(x[0][0]), "+v"(x[0][1]), "+v"(x[0][2]), "+v"(x[0][3]), "+v"(x[1][0]), "+v"(x[1][1]), "+v"(x[1][2]), "+v"(x[1][3]), \
                "+v"(x[2][0]), "+v"(x[2][1]), "+v"(x[2][2]), "+v"(x[2][3]), "+v"(x[3][0]), "+v"(x[3][1]), "+v"(x[3][2]), "+v"(x[3][3])
  {
    unsigned srd = 2 * TB, swr = TB;
    auto qstep = [&](bf16x8 (&xc)[4][4], bf16x8 (&xn)[4][4], const int t) {
      asm volatile("s_waitcnt vmcnt(8)\n\ts_barrier" : XOPS(xc) :: "memory");
      const unsigned vb = voff + srd;
      const unsigned hP = w1P + srd / 3, hQ = w1Q + srd / 3;
#pragma unroll
      for (int i = 0; i < 4; ++i) fr[i] = rdV(vb + i * 512);
      const int tn = t + 1 < 10 ? t + 1 : 9, tw = t + 2 < 10 ? t + 2 : 9;
      const char* p0 = xsrc0 + tn * 4096;
      const char* p2 = ysrcA + tn * 4096;
      const char* p3 = ysrcB + tn * 4096;
      SB();
      const bool isw = t + 2 < 10;
      const char* ssrc = isw ? wbase + (size_t)(t + 2) * (64 * QD * 2) : vbase + (size_t)(t - 8) * (64 * DH * 2);
      const unsigned sgs = isw ? 8 * QD * 2 : 8 * DH * 2;
      const unsigned svo = isw ? wvo : vvo;
      const char* w1s = w1base + (size_t)tw * (64 * FF * 2);
      auto rdN = [&](auto N) {
        constexpr int n = decltype(N)::v, ks = n >> 3, g = n & 7;
        if constexpr (g < 6) return rdV(vb + ((2 * ks) * 6 + g) * 512);
        else if constexpr (g == 6) return rdH(hP + (4 * ks) * 512);
        else return rdH(hQ + (4 * ks) * 512);
      };
      sfor<32>([&](auto I) {
        constexpr int i = decltype(I)::v, ks = i >> 3, g = i & 7;
        if constexpr (g < 6) { mfma_o<AO(0, g)>(fr[i & 3], xc[0][ks]); mfma_o<AO(1, g)>(fr[i & 3], xc[1][ks]); }
        else if constexpr (g == 6) { mfma_qv(hacc[0], fr[i & 3], xc[2][ks]); mfma_qv(hacc[1], fr[i & 3], xc[3][ks]); }
        else mfma_qv(hacc[2], fr[i & 3], xc[2][ks]);
        if constexpr (i + 4 < 32) fr[i & 3] = rdN(IC<(i + 4 < 32 ? i + 4 : 0)>{});
        if constexpr (i < 16)
          gload16<(i & 3) * 1024>(xn[i >> 2][i & 3], (i >> 2) == 0 ? p0 : (i >> 2) == 1 ? p0 + 40960 : (i >> 2) == 2 ? p2 : p3);
        if constexpr (i >= 16 && i < 22) {
          constexpr int pc = i - 16;
          glds_s(ssrc + (size_t)(pc & 1) * sgs + (pc >> 1) * 128, svo, vdst + swr + (pc & 1) * 3072 + (pc >> 1) * 1024);
        }
        if constexpr (i >= 22 && i < 24) glds_s(w1s + (size_t)(i - 22) * (8 * FF * 2), w1vo, w1dst + swr / 3 + (i - 22) * 1024);
        SB();
      });
      srd = nxt(srd);
      swr = nxt(swr);
    };
    asm volatile("s_waitcnt vmcnt(0)" : XOPS(xa) :: "memory");
    asm volatile("" : "+v"(bwv[0][0]), "+v"(bwv[0][1]), "+v"(bwv[0][2]), "+v"(bwv[0][3]), "+v"(bwv[1][0]), "+v"(bwv[1][1]), "+v"(bwv[1][2]), "+v"(bwv[1][3]));
#pragma unroll 1
    for (int t = 0; t < 10; t += 2) { qstep(xa, xb, t); qstep(xb, xa, t + 1); }
    asm volatile("s_waitcnt vmcnt(0)\n\ts_barrier" : XOPS(xa) :: "memory");
  }
#pragma unroll
  for (int pc = 0; pc < 6; ++pc) pieceK(96, TB, pc);
  asm volatile("s_nop 15\n\ts_nop 7" ::: "memory");
  SB();
  {
    const int rbA = (q0 >> 5) + odd, rbB = (q0 >> 5) + 1 - odd, cP = 3 * hj + 2 * odd, cQ = 3 * hj + 1;
#pragma unroll
    for (int k = 0; k < 3; ++k) {
      const int rbk = k == 1 ? rbB : rbA, ck = k == 2 ? cQ : cP;
      float g[16];
#pragma unroll
      for (int q = 0; q < 4; ++q) {
        const u32x2 bw = bwv[k == 2 ? 1 : 0][q];
        const float bs[4] = {__uint_as_float(bw[0] << 16), __uint_as_float(bw[0] & 0xffff0000u), __uint_as_float(bw[1] << 16),
                             __uint_as_float(bw[1] & 0xffff0000u)};
#pragma unroll
        for (int e = 0; e < 4; ++e) g[4 * q + e] = gelu_tanh(rbf(rbf(hacc[k][4 * q + e]) + bs[e]));
      }
      u32x4* ho = (u32x4*)hfr + ((size_t)rbk * 96 + 2 * ck) * 64 + lane;
#pragma unroll
      for (int sidx = 0; sidx < 2; ++sidx)
        ho[sidx * 64] = u32x4{cvtpk(g[8 * sidx], g[8 * sidx + 1]), cvtpk(g[8 * sidx + 2], g[8 * sidx + 3]),
                              cvtpk(g[8 * sidx + 4], g[8 * sidx + 5]), cvtpk(g[8 * sidx + 6], g[8 * sidx + 7])};
    }
  }
  SB();
  bf16x8 qf[2][4];
  sfor<24>([&](auto I) {
    constexpr int n = decltype(I)::v, b = n / 12, f = n % 12, R = AO(b, f >> 1) + 8 * (f & 1);
    float t8[8];
    acc_rd8<R>(t8);
    unsigned w0 = cvtpk(t8[0] * QSCALE, t8[1] * QSCALE);
    unsigned w1 = cvtpk(t8[2] * QSCALE, t8[3] * QSCALE);
    unsigned w2 = cvtpk(t8[4] * QSCALE, t8[5] * QSCALE);
    unsigned w3 = cvtpk(t8[6] * QSCALE, t8[7] * QSCALE);
    auto r0 = __builtin_amdgcn_permlane32_swap(w0, w2, false, false);
    auto r1 = __builtin_amdgcn_permlane32_swap(w1, w3, false, false);
    if constexpr (f < 8) {
      acc_wr4<AQ(b, f)>(r0[0], r1[0], r0[1], r1[1]);
    } else {
      u32x4 w = {r0[0], r1[0], r0[1], r1[1]};
      qf[b][f - 8] = __builtin_bit_cast(bf16x8, w);
    }
  });
  zfr = bf16x8{};
  PIN(zfr);
  SB();
  asm volatile("s_nop 7");
  sfor<12>([&](auto I) { acc_zero16<16 * decltype(I)::v>(zfr); });

  float m_run[2], l_run[2] = {0.f, 0.f}, alpha[2];
  bool pend = false;
  f32x16 S[2][2], nm[2];
  u32x4 pw[2][2];
  unsigned kb[4], vb;
  nm[0] = f32x16{}; nm[1] = f32x16{};
  PIN(nm[0]); PIN(nm[1]);
#pragma unroll
  for (int i = 0; i < 4; ++i) { PIN(qf[0][i]); PIN(qf[1][i]); }
#pragma unroll
  for (int i = 0; i < 4; ++i) fr[i] = rdK(koff[i] + 2 * TB + 4096);
  SB();
  asm volatile("s_nop 3" ::: "memory");
  SB();

  sfor<12>([&](auto I) {
    constexpr int i = decltype(I)::v;
    if constexpr (i == 0) { mfma_first<AQ(0, 0)>(S[0][0], fr[0], nm[0]); mfma_first<AQ(1, 0)>(S[0][1], fr[0], nm[1]); }
    else if constexpr (i < 8) { mfma_qa<AQ(0, i)>(S[0][0], fr[i & 3]); mfma_qa<AQ(1, i)>(S[0][1], fr[i & 3]); }
    else { mfma_qv(S[0][0], fr[i & 3], qf[0][i - 8]); mfma_qv(S[0][1], fr[i & 3], qf[1][i - 8]); }
    if constexpr (i + 4 < 12) fr[i & 3] = rdK(koff[i & 3] + 2 * TB + ((i + 4) >> 2) * 8192 + 4096);
    SB();
  });
  asm volatile("s_nop 15\n\ts_nop 7" :: "v"(nm[0]), "v"(nm[1]) : "memory");
  SB();
#pragma unroll
  for (int b = 0; b < 2; ++b) {
    float mx = S[0][b][0];
#pragma unroll
    for (int r = 1; r < 16; ++r) mx = fmaxf(mx, S[0][b][r]);
    auto rr = __builtin_amdgcn_permlane32_swap(__float_as_uint(mx), __float_as_uint(mx), false, false);
    mx = fmaxf(__uint_as_float(rr[0]), __uint_as_float(rr[1]));
    m_run[b] = mx;
#pragma unroll
    for (int r = 0; r < 16; ++r) { nm[b][r] = -mx; S[0][b][r] -= mx; }
  }
#pragma unroll
  for (int j = 0; j < 4; ++j) kb[j] = koff[j];
  vb = voff;
#pragma unroll
  for (int i = 0; i < 4; ++i) fr[i] = rdK(kb[i]);

  auto phaseA = [&](auto XX, auto HH, const char* ssrc, const unsigned sdst) {
    constexpr int X = decltype(XX)::v, HB = decltype(HH)::v;
    sfor<12>([&](auto I) {
      constexpr int i = decltype(I)::v;
      auto ex = [&](auto N) {
        constexpr int n = decltype(N)::v;
        if constexpr (n < 32) {
          constexpr int b = (n & 15) >> 3, r = (n >> 4) * 8 + (n & 7);
          S[X][b][r] = __builtin_amdgcn_exp2f(S[X][b][r]);
        }
      };
      auto pk = [&](auto N) {
        constexpr int n = decltype(N)::v;
        if constexpr (n >= 0 && n < 16) {
          constexpr int b = (n >> 2) & 1, ks = n >> 3, w = n & 3;
          pw[b][ks][w] = cvtpk(S[X][b][8 * ks + 2 * w], S[X][b][8 * ks + 2 * w + 1]);
        }
      };
      if constexpr (i == 0) mfma_first<AQ(0, 0)>(S[X ^ 1][0], fr[0], nm[0]);
      else if constexpr (i < 8) mfma_qa<AQ(0, i)>(S[X ^ 1][0], fr[i & 3]);
      else mfma_qv(S[X ^ 1][0], fr[i & 3], qf[0][i - 8]);
      SB();
      ex(IC<3 * i>{}); ex(IC<3 * i + 1>{});
      pk(IC<2 * i - 8>{});
      PIN(S[X][0]); PIN(S[X][1]);
      SB();
      if constexpr (i == 0) mfma_first<AQ(1, 0)>(S[X ^ 1][1], fr[0], nm[1]);
      else if constexpr (i < 8) mfma_qa<AQ(1, i)>(S[X ^ 1][1], fr[i & 3]);
      else mfma_qv(S[X ^ 1][1], fr[i & 3], qf[1][i - 8]);
      SB();
      if constexpr (i + 4 < 12) fr[i & 3] = rdK(kb[i & 3] + ((i + 4) >> 2) * 8192 + HB * 4096);
      else fr[i & 3] = rdV(vb + ((4 * HB) * 6 + (i - 8)) * 512);
      ex(IC<3 * i + 2>{});
      pk(IC<2 * i - 7>{});
      if constexpr (i == 2) asm volatile("" :: "v"(nm[0]), "v"(nm[1]));
      if constexpr (i == 1 || i == 5 || i == 9) {
        constexpr int pc = (i - 1) / 4;
        if constexpr (HB == 0) glds_i<(pc & 1) * 1024 + (pc >> 1) * 8192>(ssrc, kpo[pc], sdst);
        else glds_i<(pc & 1) * 3072 + (pc >> 1) * 1024>(ssrc, vpo[pc], sdst);
      }
      PIN(S[X][0]); PIN(S[X][1]); PIN(pw[0][0]); PIN(pw[0][1]); PIN(pw[1][0]); PIN(pw[1][1]);
      SB();
    });
  };
  auto phaseB = [&](auto XX, auto HH, auto HK, const char* ssrc, const unsigned sdst) {
    constexpr int X = decltype(XX)::v, HB = decltype(HH)::v, HBKN = decltype(HK)::v;
    float ls[2][2], mx[2];
    auto fill = [&](auto G) {
      constexpr int g = decltype(G)::v;
      if constexpr (g < 16) {
        constexpr int b = g >> 3, r = (g & 7) * 2;
        if constexpr ((g & 7) == 0) { ls[b][0] = S[X][b][r]; ls[b][1] = S[X][b][r + 1]; }
        else { ls[b][0] += S[X][b][r]; ls[b][1] += S[X][b][r + 1]; }
        PIN(ls[b][0]); PIN(ls[b][1]);
      }
      if constexpr (g == 16) { l_run[0] += ls[0][0] + ls[0][1]; PIN(l_run[0]); }
      if constexpr (g == 17) { l_run[1] += ls[1][0] + ls[1][1]; PIN(l_run[1]); }
      if constexpr (g == 20) {
#pragma unroll
        for (int b = 0; b < 2; ++b) {
          auto rr = __builtin_amdgcn_permlane32_swap(__float_as_uint(mx[b]), __float_as_uint(mx[b]), false, false);
          mx[b] = max2f(__uint_as_float(rr[0]), __uint_as_float(rr[1]));
          PIN(mx[b]);
        }
      }
      if constexpr (g == 21) {
        if (__builtin_expect(__any(max2f(mx[0], mx[1]) > THR), 0)) {
          const float aone = hi == 0 ? 1.f : 0.f;
#pragma unroll
          for (int b = 0; b < 2; ++b) {
            const float dl = fmaxf(mx[b], 0.f);
            m_run[b] += dl;
            alpha[b] = __builtin_amdgcn_exp2f(-dl);
            bcast16(nm[b], aone, -m_run[b]);
#pragma unroll
            for (int r = 0; r < 16; ++r) S[X ^ 1][b][r] -= dl;
          }
          pend = true;
        }
      }
      if constexpr (g == 16 || g == 18 || g == 22) {
        constexpr int pc = g == 16 ? 3 : (g == 18 ? 4 : 5);
        if constexpr (HB == 0) glds_i<(pc & 1) * 1024 + (pc >> 1) * 8192>(ssrc, kpo[pc], sdst);
        else glds_i<(pc & 1) * 3072 + (pc >> 1) * 1024>(ssrc, vpo[pc], sdst);
      }
    };
    auto pvm = [&](auto G) {
      constexpr int g = decltype(G)::v, i = g >> 1, b = g & 1, k = (g - 4) >> 1;
      const bf16x8 pb = __builtin_bit_cast(bf16x8, pw[b][i / 6]);
      if constexpr (g >= 4 && g < 20) {
        if constexpr (k == 0) mfma_o_mx<AO(b, i % 6)>(fr[i & 3], pb, mx[b], S[X ^ 1][b][0], S[X ^ 1][b][1], S[X ^ 1][b][2]);
        else if constexpr (k < 7) mfma_o_mx<AO(b, i % 6)>(fr[i & 3], pb, mx[b], mx[b], S[X ^ 1][b][2 * k + 1], S[X ^ 1][b][2 * k + 2]);
        else mfma_o_mx<AO(b, i % 6)>(fr[i & 3], pb, mx[b], mx[b], S[X ^ 1][b][15], S[X ^ 1][b][15]);
      } else mfma_o<AO(b, i % 6)>(fr[i & 3], pb);
    };
    sfor<12>([&](auto I) {
      constexpr int i = decltype(I)::v;
      pvm(IC<2 * i>{});
      SB();
      fill(IC<2 * i>{});
      SB();
      pvm(IC<2 * i + 1>{});
      SB();
      if constexpr (i + 4 < 12) fr[i & 3] = rdV(vb + ((4 * HB + 2 * ((i + 4) / 6)) * 6 + ((i + 4) % 6)) * 512);
      else fr[i & 3] = rdK(kb[i - 8] + HBKN * 4096);
      fill(IC<2 * i + 1>{});
      SB();
    });
    if (__builtin_expect(pend, 0)) {
      asm volatile("s_nop 15\n\ts_nop 7" ::: "memory");
      l_run[0] *= alpha[0]; l_run[1] *= alpha[1];
      sfor<192>([&](auto I) { constexpr int n = decltype(I)::v; acc_scale<n>(alpha[n / 96]); });
      asm volatile("s_nop 3" ::: "memory");
      pend = false;
    }
  };

  asm volatile("s_waitcnt vmcnt(0)" ::: "memory");
  unsigned scur = 0;
#pragma unroll 1
  for (int t = 0; t < 32; ++t) {
    const unsigned snxt = nxt(scur), swr = nxt(snxt);
    asm volatile("s_waitcnt vmcnt(6)\n\ts_barrier" ::: "memory");
    int tk = t + 2 < 32 ? t + 2 : 31;
    const int tv = tk;
    if (tk == 31 && wid >= 2) tk = 30;
    const char* ks_ = kbase + (size_t)(tk * 64 + 32) * (DH * 2);
    const char* vs_ = vbase + (size_t)(tv * 64) * (DH * 2);
    SB();
    phaseA(IC<0>{}, IC<0>{}, ks_, kdst + swr);
    phaseB(IC<0>{}, IC<0>{}, IC<1>{}, ks_, kdst + swr);
    phaseA(IC<1>{}, IC<1>{}, vs_, vdst + swr);
#pragma unroll
    for (int j = 0; j < 4; ++j) kb[j] = koff[j] + snxt;
    phaseB(IC<1>{}, IC<1>{}, IC<0>{}, vs_, vdst + swr);
    vb = voff + snxt;
    scur = snxt;
  }
  asm volatile("s_waitcnt vmcnt(0)\n\ts_nop 15\n\ts_nop 7" ::: "memory");
  int tz = threadIdx.x;
  asm volatile("" : "+v"(tz));
  float rls[2];
#pragma unroll
  for (int b = 0; b < 2; ++b) {
    auto rr = __builtin_amdgcn_permlane32_swap(__float_as_uint(l_run[b]), __float_as_uint(l_run[b]), false, false);
    rls[b] = 1.0f / (__uint_as_float(rr[0]) + __uint_as_float(rr[1]));
  }
  u32x4* ofr = (u32x4*)O + ((size_t)(qb * 8 + (tz >> 6) * 2) * 384 + head * 12) * 64 + (tz & 63);
  sfor<24>([&](auto I) {
    constexpr int n = decltype(I)::v, b = n / 12, f = n % 12, R = AO(b, f >> 1) + 8 * (f & 1);
    const float rl = rls[b];
    float t8[8];
    acc_rd8<R>(t8);
    __builtin_nontemporal_store(u32x4{cvtpk(t8[0] * rl, t8[1] * rl), cvtpk(t8[2] * rl, t8[3] * rl),
                                      cvtpk(t8[4] * rl, t8[5] * rl), cvtpk(t8[6] * rl, t8[7] * rl)}, &ofr[(b * 384 + f) * 64]);
  });
}

extern "C" void kernel_launch(void* const* d_in, const int* in_sizes, int n_in, void* d_out, int out_size,
                              void* d_ws, size_t ws_size, hipStream_t stream) {
  const u16* x = (const u16*)d_in[0];
  const u16* kc = (const u16*)d_in[1];
  const u16* vc = (const u16*)d_in[2];
  const u16* wq = (const u16*)d_in[3];
  const u16* wo = (const u16*)d_in[4];
  const u16* w1 = (const u16*)d_in[6];
  const u16* b1 = (const u16*)d_in[7];
  const u16* w2 = (const u16*)d_in[8];
  const u16* b2 = (const u16*)d_in[9];
  char* ws = (char*)d_ws;
  u16* xnp = (u16*)(ws);
  u16* y = (u16*)(ws + (4u << 20));
  u16* h = (u16*)(ws + (8u << 20));
  u16* O = (u16*)(ws + (16u << 20));
  u16* part = (u16*)(ws + (48u << 20));

  hipFuncSetAttribute((const void*)k_attn, hipFuncAttributeMaxDynamicSharedMemorySize, 6 * TB);
  hipFuncSetAttribute((const void*)k_gemm2, hipFuncAttributeMaxDynamicSharedMemorySize, 4 * 16384);

  k_ln<<<SEQ / 4, 256, 0, stream>>>(x, xnp, y);
  k_attn<<<256, 256, 6 * TB, stream>>>(xnp, wq, kc, vc, O, y, w1, b1, h);
  k_gemm2<<<200, 256, 4 * 16384, stream>>>(O, wo, h, w2, part);
  k_final<<<SEQ * DM / 2048, 256, 0, stream>>>(x, part, b2, (u16*)d_out);
}
```

```cpp
#include <hip/hip_runtime.h>
#include <hip/hip_bf16.h>
#include <stdint.h>

typedef __attribute__((ext_vector_type(8))) short bf16x8;
typedef __attribute__((ext_vector_type(8))) __bf16 bf16x8b;
typedef __attribute__((ext_vector_type(4))) short s16x4;
typedef __attribute__((ext_vector_type(16))) float f32x16;
typedef __attribute__((ext_vector_type(4))) float f32x4;
typedef __attribute__((ext_vector_type(2))) float f32x2;
typedef __attribute__((ext_vector_type(2))) __bf16 bf16x2b;
typedef __attribute__((ext_vector_type(4))) unsigned u32x4;
typedef __attribute__((ext_vector_type(2))) unsigned u32x2;
typedef __attribute__((address_space(3))) char* lds_ptr;
typedef unsigned short u16;

#define DI __device__ __forceinline__

constexpr int SEQ = 2048, DM = 640, NH = 32, DH = 192, FF = 1536, QD = NH * DH;

DI f32x16 MFMA(bf16x8 a, bf16x8 b, f32x16 c) {
  return __builtin_amdgcn_mfma_f32_32x32x16_bf16(__builtin_bit_cast(bf16x8b, a), __builtin_bit_cast(bf16x8b, b), c, 0, 0, 0);
}
DI unsigned cvtpk(float lo, float hi) {
  f32x2 v = {lo, hi};
  bf16x2b b = __builtin_convertvector(v, bf16x2b);
  return __builtin_bit_cast(unsigned, b);
}
DI float rbf(float v) { return __uint_as_float(cvtpk(v, 0.f) << 16); }
DI s16x4 trread(lds_ptr p) {
  return __builtin_amdgcn_ds_read_tr16_b64_v4i16((__attribute__((address_space(3))) s16x4*)p);
}
DI bf16x8 ldsread(lds_ptr p) { return *(__attribute__((address_space(3))) bf16x8*)p; }
DI bf16x8 cat(s16x4 a, s16x4 b) { return __builtin_shufflevector(a, b, 0, 1, 2, 3, 4, 5, 6, 7); }

template <int CTRL> DI float dpp_add(float v) {
  return v + __int_as_float(__builtin_amdgcn_update_dpp(0, __float_as_int(v), CTRL, 0xf, 0xf, true));
}
DI float wave_sum(float v) {
  v = dpp_add<0xB1>(v);
  v = dpp_add<0x4E>(v);
  v = dpp_add<0x141>(v);
  v = dpp_add<0x140>(v);
  auto a = __builtin_amdgcn_permlane16_swap(__float_as_uint(v), __float_as_uint(v), false, false);
  v = __uint_as_float(a[0]) + __uint_as_float(a[1]);
  auto b = __builtin_amdgcn_permlane32_swap(__float_as_uint(v), __float_as_uint(v), false, false);
  return __uint_as_float(b[0]) + __uint_as_float(b[1]);
}
DI void ln_row(float (&v)[10]) {
  float s = 0.f;
#pragma unroll
  for (int i = 0; i < 10; ++i) s += v[i];
  s = wave_sum(s);
  const float mean = s * (1.0f / DM);
  float q = 0.f;
#pragma unroll
  for (int i = 0; i < 10; ++i) { float d = v[i] - mean; q += d * d; }
  q = wave_sum(q);
  const float mb = rbf(mean), vb = rbf(q * (1.0f / DM));
  const float den = rbf(sqrtf(rbf(vb + 1e-5f)));
#pragma unroll
  for (int i = 0; i < 10; ++i) v[i] = rbf(rbf(v[i] - mb) / den);
}
__global__ __launch_bounds__(256) void k_ln(const u16* __restrict__ x, u16* __restrict__ xnp, u16* __restrict__ y) {
  const int row = blockIdx.x * 4 + (threadIdx.x >> 6), lane = threadIdx.x & 63;
  const unsigned* xr = (const unsigned*)(x + (size_t)row * DM);
  float v[10];
#pragma unroll
  for (int i = 0; i < 5; ++i) {
    unsigned w = xr[lane + 64 * i];
    v[2 * i] = __uint_as_float(w << 16);
    v[2 * i + 1] = __uint_as_float(w & 0xffff0000u);
  }
  ln_row(v);
  unsigned* xo = (unsigned*)xnp + (size_t)(row >> 5) * (40 * 64 * 4) + (row & 31) * 4;
#pragma unroll
  for (int i = 0; i < 5; ++i) {
    int e = lane + 64 * i;
    int ep = (e & ~6) | ((e & 2) << 1) | ((e & 4) >> 1);
    xo[(ep >> 3) * 256 + ((ep >> 2) & 1) * 128 + (ep & 3)] =
        (__float_as_uint(v[2 * i]) >> 16) | (__float_as_uint(v[2 * i + 1]) & 0xffff0000u);
  }
  ln_row(v);
  unsigned* yo = (unsigned*)y + (size_t)(row >> 5) * (40 * 64 * 4) + (row & 31) * 4;
#pragma unroll
  for (int i = 0; i < 5; ++i) {
    int e = lane + 64 * i;
    int ep = (e & ~6) | ((e & 2) << 1) | ((e & 4) >> 1);
    yo[(ep >> 3) * 256 + ((ep >> 2) & 1) * 128 + (ep & 3)] =
        (__float_as_uint(v[2 * i]) >> 16) | (__float_as_uint(v[2 * i + 1]) & 0xffff0000u);
  }
}

#define SB() __builtin_amdgcn_sched_barrier(0)
#define PIN(x) asm volatile("" : "+v"(x))
template <int I> struct IC { static constexpr int v = I; };
template <int N, class F> DI void sfor(F&& f) {
  if constexpr (N > 0) { sfor<N - 1>(f); f(IC<N - 1>{}); }
}
template <int IMM> DI void glds_i(const void* sbase, unsigned voff, unsigned ldsbase) {
  asm volatile("s_add_i32 m0, %2, %3\n\ts_nop 0\n\tglobal_load_lds_dwordx4 %0, %1"
               :: "v"(voff), "s"(sbase), "s"(ldsbase), "i"(IMM) : "memory", "scc");
}
template <int OFF> DI void gload16(bf16x8& d, const char* p) {
  asm volatile("global_load_dwordx4 %0, %1, off offset:%2" : "=&v"(d) : "v"(p), "i"(OFF) : "memory");
}
template <int OFF> DI void gload8(u32x2& d, const void* p) {
  asm volatile("global_load_dwordx2 %0, %1, off offset:%2" : "=&v"(d) : "v"(p), "i"(OFF) : "memory");
}
template <int N> DI void xwait2(bf16x8 (&x)[2][4]) {
  asm volatile("s_waitcnt vmcnt(%8)\n\ts_barrier" : "+v"(x[0][0]), "+v"(x[0][1]), "+v"(x[0][2]), "+v"(x[0][3]), "+v"(x[1][0]), "+v"(x[1][1]), "+v"(x[1][2]), "+v"(x[1][3]) : "i"(N) : "memory");
}
DI void xpin2(bf16x8 (&x)[2][4]) {
  asm volatile("" : "+v"(x[0][0]), "+v"(x[0][1]), "+v"(x[0][2]), "+v"(x[0][3]), "+v"(x[1][0]), "+v"(x[1][1]), "+v"(x[1][2]), "+v"(x[1][3]));
}

DI void wgemm24(const char* wbase, unsigned ldwb, const char* xp, unsigned xrbs, int ktiles, unsigned lds0,
                f32x16 (&acc)[2][4]) {
  constexpr int NB = 4;
  constexpr unsigned TBY = 4096 * NB;
  const int tid = threadIdx.x, lane = tid & 63, wid = __builtin_amdgcn_readfirstlane(tid >> 6);
  const int hi = lane >> 5;
  unsigned wpo[NB];
#pragma unroll
  for (int i = 0; i < NB; ++i) {
    const int sub = 2 * (wid * NB + i) + hi, kg = sub / NB, nb = sub % NB;
    wpo[i] = (unsigned)(8 * kg + ((lane >> 2) & 7)) * ldwb + nb * 64 + (lane & 3) * 16;
  }
  const unsigned wdst = lds0 + wid * NB * 1024;
  const unsigned voff = lds0 + (hi * 4 + ((lane >> 2) & 3)) * 64 + ((lane >> 4) & 1) * 32 + (lane & 3) * 8;
  auto rdW = [&](unsigned a) { return cat(trread((lds_ptr)(uintptr_t)a), trread((lds_ptr)(uintptr_t)(a + NB * 512))); };
#pragma unroll
  for (int r = 0; r < 2; ++r)
#pragma unroll
    for (int n = 0; n < NB; ++n) acc[r][n] = f32x16{};
  bf16x8 x0[2][4], x1[2][4], x2[2][4], x3[2][4], fr[4];
  const size_t wts = (size_t)64 * ldwb;
  auto issue = [&](bf16x8 (&x)[2][4], int t, auto SLOT) {
    const char* xq = xp + (size_t)t * 4096;
    sfor<8>([&](auto I) { constexpr int i = decltype(I)::v; gload16<(i & 3) * 1024>(x[i >> 2][i & 3], xq + (i >> 2) * xrbs); });
    sfor<NB>([&](auto I) { glds_i<decltype(SLOT)::v * TBY + decltype(I)::v * 1024>(wbase + (size_t)t * wts, wpo[decltype(I)::v], wdst); });
  };
  issue(x0, 0, IC<0>{});
  issue(x1, 1, IC<1>{});
  issue(x2, 2, IC<2>{});
  asm volatile("s_waitcnt vmcnt(24)\n\ts_barrier" ::: "memory");
#pragma unroll
  for (int i = 0; i < 4; ++i) fr[i] = rdW(voff + i * 512);
  auto step = [&](bf16x8 (&xc)[2][4], bf16x8 (&xn)[2][4], const int t, auto SLOT) {
    constexpr int sl = decltype(SLOT)::v, sw = (sl + 3) & 3;
    xwait2<12>(xc);
    const unsigned vb = voff + sl * TBY, vbn = voff + ((sl + 1) & 3) * TBY;
    const int tn = t + 3 < ktiles ? t + 3 : ktiles - 1;
    const char* xq = xp + (size_t)tn * 4096;
    const char* wq_ = wbase + (size_t)tn * wts;
    SB();
    sfor<16>([&](auto I) {
      constexpr int i = decltype(I)::v;
      acc[0][i & 3] = MFMA(fr[i & 3], xc[0][i >> 2], acc[0][i & 3]);
      acc[1][i & 3] = MFMA(fr[i & 3], xc[1][i >> 2], acc[1][i & 3]);
      SB();
      if constexpr (i + 4 < 16) fr[i & 3] = rdW(vb + ((2 * ((i + 4) >> 2)) * NB + (i & 3)) * 512);
      else fr[i & 3] = rdW(vbn + (i & 3) * 512);
      if constexpr (i < 8) gload16<(i & 3) * 1024>(xn[i >> 2][i & 3], xq + (i >> 2) * xrbs);
      if constexpr (i >= 8 && i < 12) glds_i<sw * TBY + (i - 8) * 1024>(wq_, wpo[i - 8], wdst);
      SB();
    });
  };
#pragma unroll 1
  for (int t = 0; t < ktiles; t += 4) {
    step(x0, x3, t, IC<0>{});
    step(x1, x0, t + 1, IC<1>{});
    step(x2, x1, t + 2, IC<2>{});
    step(x3, x2, t + 3, IC<3>{});
  }
  asm volatile("s_waitcnt vmcnt(0)" ::: "memory");
  xpin2(x0); xpin2(x1); xpin2(x2);
}

DI float gelu_tanh(float v) {
  const float u = 0.7978845608028654f * (v + 0.044715f * v * v * v);
  const float e = __builtin_amdgcn_exp2f(2.8853900817779268f * u);
  return v - v * __builtin_amdgcn_rcpf(e + 1.f);
}

__global__ __launch_bounds__(256, 1) void k_gemm2(const u16* __restrict__ O, const u16* __restrict__ wo,
                                                  const u16* __restrict__ h, const u16* __restrict__ w2,
                                                  u16* __restrict__ part) {
  extern __shared__ __attribute__((aligned(16))) char smem[];
  const int bid = blockIdx.x, x = bid & 7, r = bid >> 3;
  const int nt = r % 5, c = r < 20 ? (x >> 1) : 4, mt = r < 20 ? 4 * (x & 1) + r / 5 : x;
  const int lane = threadIdx.x & 63, wid = threadIdx.x >> 6;
  const int rb0 = mt * 8 + wid * 2;
  const char* W; const char* X; unsigned xrbs;
  if (c < 4) {
    W = (const char*)(wo + (size_t)c * 24 * 64 * DM + nt * 128);
    xrbs = 384 * 1024;
    X = (const char*)O + ((size_t)rb0 * 384 + c * 96) * 1024 + lane * 16;
  } else {
    W = (const char*)(w2 + nt * 128);
    xrbs = 96 * 1024;
    X = (const char*)h + (size_t)rb0 * 96 * 1024 + lane * 16;
  }
  f32x16 acc[2][4];
  wgemm24(W, DM * 2, X, xrbs, 24, (unsigned)(uintptr_t)(lds_ptr)smem, acc);
  u32x4* P = (u32x4*)part + (size_t)c * (SEQ * DM / 8);
#pragma unroll
  for (int b = 0; b < 2; ++b)
#pragma unroll
    for (int nb = 0; nb < 4; ++nb)
#pragma unroll
      for (int hq = 0; hq < 2; ++hq)
        P[(((size_t)(rb0 + b) * 20 + nt * 4 + nb) * 2 + hq) * 64 + lane] =
            u32x4{cvtpk(acc[b][nb][8 * hq], acc[b][nb][8 * hq + 1]), cvtpk(acc[b][nb][8 * hq + 2], acc[b][nb][8 * hq + 3]),
                  cvtpk(acc[b][nb][8 * hq + 4], acc[b][nb][8 * hq + 5]), cvtpk(acc[b][nb][8 * hq + 6], acc[b][nb][8 * hq + 7])};
}

DI float blo(unsigned w) { return __uint_as_float(w << 16); }
DI float bhi(unsigned w) { return __uint_as_float(w & 0xffff0000u); }
__global__ __launch_bounds__(256) void k_final(const u16* __restrict__ x, const u16* __restrict__ part,
                                               const u16* __restrict__ b2, u16* __restrict__ out) {
  const int idx = blockIdx.x * 256 + threadIdx.x;
  constexpr size_t PS = (size_t)SEQ * DM / 8;
  const u32x4* P = (const u32x4*)part + idx;
  const u32x4 p0 = P[0], p1 = P[PS], p2 = P[2 * PS], p3 = P[3 * PS], pm = P[4 * PS];
  const int lane = idx & 63, hq = (idx >> 6) & 1, blk = idx >> 7, cb = blk % 20, rb = blk / 20;
  const int m = rb * 32 + (lane & 31), n0 = cb * 32 + 16 * hq + 4 * (lane >> 5);
#pragma unroll
  for (int q = 0; q < 2; ++q) {
    const int n = n0 + 8 * q;
    const u32x2 xw = *(const u32x2*)(x + (size_t)m * DM + n);
    const u32x2 bw = *(const u32x2*)(b2 + n);
    float r[4];
#pragma unroll
    for (int i = 0; i < 4; ++i) {
      const int w = 2 * q + (i >> 1);
      const float a0 = (i & 1) ? bhi(p0[w]) : blo(p0[w]), a1 = (i & 1) ? bhi(p1[w]) : blo(p1[w]);
      const float a2 = (i & 1) ? bhi(p2[w]) : blo(p2[w]), a3 = (i & 1) ? bhi(p3[w]) : blo(p3[w]);
      const float am = (i & 1) ? bhi(pm[w]) : blo(pm[w]);
      const float xo = (i & 1) ? bhi(xw[i >> 1]) : blo(xw[i >> 1]), bo = (i & 1) ? bhi(bw[i >> 1]) : blo(bw[i >> 1]);
      const float sv = rbf((a0 + a1) + (a2 + a3));
      const float mm = rbf(am + bo);
      const float g = rbf(mm / (1.f + __expf(-mm)));
      r[i] = xo + rbf(sv * g);
    }
    *(u32x2*)(out + (size_t)m * DM + n) = u32x2{cvtpk(r[0], r[1]), cvtpk(r[2], r[3])};
  }
}

constexpr int TB = 24576;
constexpr float QSCALE = 0.07216878364870322f * 1.4426950408889634f;
constexpr float THR = 20.0f;

DI void glds_s(const void* sbase, unsigned voff, unsigned ldsaddr) {
  asm volatile("s_mov_b32 m0, %2\n\ts_nop 0\n\tglobal_load_lds_dwordx4 %0, %1"
               :: "v"(voff), "s"(sbase), "s"(ldsaddr) : "memory");
}
DI float max2f(float a, float b) { float r; asm("v_max_f32_e32 %0, %1, %2" : "=v"(r) : "v"(a), "v"(b)); return r; }
DI void bcast16(f32x16& d, float aone, float negm) {
  asm volatile("s_nop 1\n\tv_mfma_f32_32x32x2_f32 %0, %1, %2, 0" : "+v"(d) : "v"(aone), "v"(negm));
}
DI bf16x8 rdK(unsigned a) { return ldsread((lds_ptr)(uintptr_t)a); }
DI bf16x8 rdV(unsigned a) { return cat(trread((lds_ptr)(uintptr_t)a), trread((lds_ptr)(uintptr_t)(a + 3072))); }
DI unsigned nxt(unsigned s) { return s == 2 * TB ? 0 : s + TB; }
#define ACLOB "a0","a1","a2","a3","a4","a5","a6","a7","a8","a9","a10","a11","a12","a13","a14","a15","a16","a17","a18","a19","a20","a21","a22","a23","a24","a25","a26","a27","a28","a29","a30","a31","a32","a33","a34","a35","a36","a37","a38","a39","a40","a41","a42","a43","a44","a45","a46","a47","a48","a49","a50","a51","a52","a53","a54","a55","a56","a57","a58","a59","a60","a61","a62","a63","a64","a65","a66","a67","a68","a69","a70","a71","a72","a73","a74","a75","a76","a77","a78","a79","a80","a81","a82","a83","a84","a85","a86","a87","a88","a89","a90","a91","a92","a93","a94","a95","a96","a97","a98","a99","a100","a101","a102","a103","a104","a105","a106","a107","a108","a109","a110","a111","a112","a113","a114","a115","a116","a117","a118","a119","a120","a121","a122","a123","a124","a125","a126","a127","a128","a129","a130","a131","a132","a133","a134","a135","a136","a137","a138","a139","a140","a141","a142","a143","a144","a145","a146","a147","a148","a149","a150","a151","a152","a153","a154","a155","a156","a157","a158","a159","a160","a161","a162","a163","a164","a165","a166","a167","a168","a169","a170","a171","a172","a173","a174","a175","a176","a177","a178","a179","a180","a181","a182","a183","a184","a185","a186","a187","a188","a189","a190","a191","a192","a193","a194","a195","a196","a197","a198","a199","a200","a201","a202","a203","a204","a205","a206","a207","a208","a209","a210","a211","a212","a213","a214","a215","a216","a217","a218","a219","a220","a221","a222","a223","a224","a225","a226","a227","a228","a229","a230","a231","a232","a233","a234","a235","a236","a237","a238","a239","a240","a241","a242","a243","a244","a245","a246","a247","a248","a249","a250","a251","a252","a253","a254","a255"
constexpr int AO(int b, int db) { return 16 * (6 * b + db); }
constexpr int AQ(int b, int i) { return 192 + 32 * b + 4 * i; }
template <int R> DI void mfma_o(const bf16x8& a, const bf16x8& b) {
  asm volatile("v_mfma_f32_32x32x16_bf16 a[%c2:%c3], %0, %1, a[%c2:%c3]" :: "v"(a), "v"(b), "i"(R), "i"(R + 15) : ACLOB);
}
template <int R> DI void mfma_o_mx(const bf16x8& a, const bf16x8& b, float& m, float x, float y, float z) {
  asm volatile("v_mfma_f32_32x32x16_bf16 a[%c6:%c7], %1, %2, a[%c6:%c7]\n\tv_max3_f32 %0, %3, %4, %5"
               : "=v"(m) : "v"(a), "v"(b), "v"(x), "v"(y), "v"(z), "i"(R), "i"(R + 15) : ACLOB);
}
template <int R> DI void mfma_first(f32x16& d, const bf16x8& a, const f32x16& c) {
  asm volatile("v_mfma_f32_32x32x16_bf16 %0, %1, a[%c3:%c4], %2" : "=&v"(d) : "v"(a), "v"(c), "i"(R), "i"(R + 3) : ACLOB);
}
template <int R> DI void mfma_qa(f32x16& d, const bf16x8& a) {
  asm volatile("v_mfma_f32_32x32x16_bf16 %0, %1, a[%c2:%c3], %0" : "+v"(d) : "v"(a), "i"(R), "i"(R + 3) : ACLOB);
}
DI void mfma_qv(f32x16& d, const bf16x8& a, const bf16x8& b) {
  asm volatile("v_mfma_f32_32x32x16_bf16 %0, %1, %2, %0" : "+v"(d) : "v"(a), "v"(b));
}
template <int R> DI void acc_zero16(const bf16x8& z) {
  asm volatile("v_mfma_f32_32x32x16_bf16 a[%c1:%c2], %0, %0, 0" :: "v"(z), "i"(R), "i"(R + 15) : ACLOB);
}
template <int R> DI void acc_rd8(float (&r)[8]) {
  asm volatile("v_accvgpr_read_b32 %0, a[%c8]\n\tv_accvgpr_read_b32 %1, a[%c9]\n\tv_accvgpr_read_b32 %2, a[%c10]\n\tv_accvgpr_read_b32 %3, a[%c11]\n\t"
               "v_accvgpr_read_b32 %4, a[%c12]\n\tv_accvgpr_read_b32 %5, a[%c13]\n\tv_accvgpr_read_b32 %6, a[%c14]\n\tv_accvgpr_read_b32 %7, a[%c15]"
               : "=v"(r[0]), "=v"(r[1]), "=v"(r[2]), "=v"(r[3]), "=v"(r[4]), "=v"(r[5]), "=v"(r[6]), "=v"(r[7])
               : "i"(R), "i"(R + 1), "i"(R + 2), "i"(R + 3), "i"(R + 4), "i"(R + 5), "i"(R + 6), "i"(R + 7));
}
template <int R> DI void acc_wr4(unsigned a, unsigned b, unsigned c, unsigned d) {
  asm volatile("v_accvgpr_write_b32 a[%c4], %0\n\tv_accvgpr_write_b32 a[%c5], %1\n\tv_accvgpr_write_b32 a[%c6], %2\n\tv_accvgpr_write_b32 a[%c7], %3"
               :: "v"(a), "v"(b), "v"(c), "v"(d), "i"(R), "i"(R + 1), "i"(R + 2), "i"(R + 3) : ACLOB);
}
template <int R> DI void acc_scale(float f) {
  float t;
  asm volatile("v_accvgpr_read_b32 %0, a[%c2]\n\tv_mul_f32 %0, %1, %0\n\tv_accvgpr_write_b32 a[%c2], %0" : "=&v"(t) : "v"(f), "i"(R) : ACLOB);
}

__global__ __launch_bounds__(256, 1) void k_attn(const u16* __restrict__ xnp, const u16* __restrict__ wq,
                                                 const u16* __restrict__ Kc, const u16* __restrict__ Vc,
                                                 u16* __restrict__ O, const u16* __restrict__ yfr,
                                                 const u16* __restrict__ w1, const u16* __restrict__ b1,
                                                 u16* __restrict__ hfr) {
  extern __shared__ __attribute__((aligned(16))) char smem[];
  const unsigned lds0 = (unsigned)(uintptr_t)(lds_ptr)smem;
  const int tid = threadIdx.x, lane = tid & 63, wid = __builtin_amdgcn_readfirstlane(tid >> 6);
  const int r32 = lane & 31, hi = lane >> 5;
  const int bid = blockIdx.x, xcd = bid & 7, idx = bid >> 3;
  const int head = xcd * 4 + (idx >> 3), qb = idx & 7;
  const int q0 = qb * 256 + wid * 64;

  const int krl = lane >> 3;
  const unsigned kvo0 = (unsigned)(krl * DH * 2 + (((lane & 7) ^ (krl >> 1)) << 4));
  const unsigned kvo1 = (unsigned)(krl * DH * 2 + (((lane & 7) ^ (4 + (krl >> 1))) << 4));
  const unsigned vvo = (unsigned)(((lane >> 2) & 7) * DH * 2 + (lane >> 5) * 64 + (lane & 3) * 16);
  const unsigned wvo = (unsigned)(((lane >> 2) & 7) * QD * 2 + (lane >> 5) * 64 + (lane & 3) * 16);
  const char* kbase = (const char*)(Kc + (size_t)head * SEQ * DH) + (size_t)(16 * wid) * DH * 2;
  const char* vbase = (const char*)(Vc + (size_t)head * SEQ * DH) + (size_t)(16 * wid) * DH * 2;
  const char* wbase = (const char*)(wq + head * DH) + (size_t)(16 * wid) * QD * 2;
  const unsigned kdst = lds0 + wid * 2048, vdst = lds0 + 3 * TB + wid * 6144;
  auto pieceK = [&](int key0, unsigned slot, int pc) {
    glds_s(kbase + (size_t)key0 * (DH * 2) + (pc & 1) * (8 * DH * 2) + (pc >> 1) * 128, (pc & 1) ? kvo1 : kvo0,
           kdst + slot + (pc & 1) * 1024 + (pc >> 1) * 8192);
  };
  auto pieceW = [&](int t, unsigned slot, int pc) {
    glds_s(wbase + (size_t)t * (64 * QD * 2) + (size_t)(pc & 1) * (8 * QD * 2) + (pc >> 1) * 128, wvo,
           vdst + slot + (pc & 1) * 3072 + (pc >> 1) * 1024);
  };
  unsigned kpo[6], vpo[6];
#pragma unroll
  for (int pc = 0; pc < 6; ++pc) {
    kpo[pc] = ((pc & 1) ? kvo1 : kvo0) + (pc & 1) * (8 * DH * 2) + (pc >> 1) * 128;
    vpo[pc] = vvo + (pc & 1) * (8 * DH * 2) + (pc >> 1) * 128;
  }
  unsigned koff[4];
#pragma unroll
  for (int j = 0; j < 4; ++j) koff[j] = lds0 + r32 * 128 + (((2 * j + hi) ^ ((r32 >> 1) & 7)) << 4);
  const unsigned voff = lds0 + 3 * TB + (hi * 4 + ((lane >> 2) & 3)) * 64 + ((lane >> 4) & 1) * 32 + (lane & 3) * 8;

  bf16x8 zfr = bf16x8{};
  PIN(zfr);
  asm volatile("s_nop 3");
  sfor<12>([&](auto I) { acc_zero16<16 * decltype(I)::v>(zfr); });
  bf16x8 fr[4];

  const int odd = head & 1, hj = head >> 1;
  const char* xsrc0 = (const char*)xnp + ((size_t)(q0 >> 5) * (40 * 64) + lane) * 16;
  const char* ysrcA = (const char*)yfr + ((size_t)((q0 >> 5) + odd) * (40 * 64) + lane) * 16;
  const char* ysrcB = (const char*)yfr + ((size_t)((q0 >> 5) + 1 - odd) * (40 * 64) + lane) * 16;
  const char* w1base = (const char*)(w1 + (3 * hj + odd) * 32) + (size_t)(16 * wid) * (FF * 2);
  const unsigned w1vo = (unsigned)(((lane >> 2) & 7) * FF * 2 + (lane >> 5) * 64 + (lane & 3) * 16);
  const unsigned w1dst = lds0 + TB + wid * 2048;
  const unsigned w1rd = lds0 + TB + (hi * 4 + ((lane >> 2) & 3)) * 64 + ((lane >> 4) & 1) * 32 + (lane & 3) * 8;
  const unsigned w1P = w1rd + (odd ? 512 : 0), w1Q = w1rd + (odd ? 0 : 512);
  auto rdH = [&](unsigned a) { return cat(trread((lds_ptr)(uintptr_t)a), trread((lds_ptr)(uintptr_t)(a + 1024))); };
  u32x2 bwv[2][4];
  {
    const u16* bp = b1 + (3 * hj + 2 * odd) * 32 + 4 * hi;
    const u16* bq = b1 + (3 * hj + 1) * 32 + 4 * hi;
    sfor<8>([&](auto I) {
      constexpr int i = decltype(I)::v;
      gload8<(i & 3) * 16>(bwv[i >> 2][i & 3], i < 4 ? bp : bq);
    });
  }
  bf16x8 xa[4][4], xb[4][4];
  f32x16 hacc[3];
  hacc[0] = f32x16{}; hacc[1] = f32x16{}; hacc[2] = f32x16{};
  PIN(hacc[0]); PIN(hacc[1]); PIN(hacc[2]);
#pragma unroll
  for (int pc = 0; pc < 6; ++pc) pieceW(0, 2 * TB, pc);
#pragma unroll
  for (int pc = 0; pc < 6; ++pc) pieceW(1, 0, pc);
#pragma unroll
  for (int t = 0; t < 2; ++t)
#pragma unroll
    for (int g = 0; g < 2; ++g)
      glds_s(w1base + (size_t)t * (64 * FF * 2) + (size_t)g * (8 * FF * 2), w1vo, w1dst + (t == 0 ? 2 : 0) * 8192 + g * 1024);
  sfor<16>([&](auto I) {
    constexpr int i = decltype(I)::v;
    gload16<(i & 3) * 1024>(xa[i >> 2][i & 3], (i >> 2) == 0 ? xsrc0 : (i >> 2) == 1 ? xsrc0 + 40960 : (i >> 2) == 2 ? ysrcA : ysrcB);
  });
  if (wid >= 2) {
#pragma unroll
    for (int pc = 0; pc < 6; ++pc) pieceK(-32, 2 * TB, pc);
  }
#pragma unroll
  for (int pc = 0; pc < 6; ++pc) pieceK(32, 0, pc);
#define XOPS(x) "+v"(x[0][0]), "+v"(x[0][1]), "+v"(x[0][2]), "+v"(x[0][3]), "+v"(x[1][0]), "+v"(x[1][1]), "+v"(x[1][2]), "+v"(x[1][3]), \
                "+v"(x[2][0]), "+v"(x[2][1]), "+v"(x[2][2]), "+v"(x[2][3]), "+v"(x[3][0]), "+v"(x[3][1]), "+v"(x[3][2]), "+v"(x[3][3])
  {
    unsigned srd = 2 * TB, swr = TB;
    auto qstep = [&](bf16x8 (&xc)[4][4], bf16x8 (&xn)[4][4], const int t) {
      asm volatile("s_waitcnt vmcnt(8)\n\ts_barrier" : XOPS(xc) :: "memory");
      const unsigned vb = voff + srd;
      const unsigned hP = w1P + srd / 3, hQ = w1Q + srd / 3;
#pragma unroll
      for (int i = 0; i < 4; ++i) fr[i] = rdV(vb + i * 512);
      const int tn = t + 1 < 10 ? t + 1 : 9, tw = t + 2 < 10 ? t + 2 : 9;
      const char* p0 = xsrc0 + tn * 4096;
      const char* p2 = ysrcA + tn * 4096;
      const char* p3 = ysrcB + tn * 4096;
      SB();
      const bool isw = t + 2 < 10;
      const char* ssrc = isw ? wbase + (size_t)(t + 2) * (64 * QD * 2) : vbase + (size_t)(t - 8) * (64 * DH * 2);
      const unsigned sgs = isw ? 8 * QD * 2 : 8 * DH * 2;
      const unsigned svo = isw ? wvo : vvo;
      const char* w1s = w1base + (size_t)tw * (64 * FF * 2);
      auto rdN = [&](auto N) {
        constexpr int n = decltype(N)::v, ks = n >> 3, g = n & 7;
        if constexpr (g < 6) return rdV(vb + ((2 * ks) * 6 + g) * 512);
        else if constexpr (g == 6) return rdH(hP + (4 * ks) * 512);
        else return rdH(hQ + (4 * ks) * 512);
      };
      sfor<32>([&](auto I) {
        constexpr int i = decltype(I)::v, ks = i >> 3, g = i & 7;
        if constexpr (g < 6) { mfma_o<AO(0, g)>(fr[i & 3], xc[0][ks]); mfma_o<AO(1, g)>(fr[i & 3], xc[1][ks]); }
        else if constexpr (g == 6) { mfma_qv(hacc[0], fr[i & 3], xc[2][ks]); mfma_qv(hacc[1], fr[i & 3], xc[3][ks]); }
        else mfma_qv(hacc[2], fr[i & 3], xc[2][ks]);
        if constexpr (i + 4 < 32) fr[i & 3] = rdN(IC<(i + 4 < 32 ? i + 4 : 0)>{});
        if constexpr (i < 16)
          gload16<(i & 3) * 1024>(xn[i >> 2][i & 3], (i >> 2) == 0 ? p0 : (i >> 2) == 1 ? p0 + 40960 : (i >> 2) == 2 ? p2 : p3);
        if constexpr (i >= 16 && i < 22) {
          constexpr int pc = i - 16;
          glds_s(ssrc + (size_t)(pc & 1) * sgs + (pc >> 1) * 128, svo, vdst + swr + (pc & 1) * 3072 + (pc >> 1) * 1024);
        }
        if constexpr (i >= 22 && i < 24) glds_s(w1s + (size_t)(i - 22) * (8 * FF * 2), w1vo, w1dst + swr / 3 + (i - 22) * 1024);
        SB();
      });
      srd = nxt(srd);
      swr = nxt(swr);
    };
    asm volatile("s_waitcnt vmcnt(0)" : XOPS(xa) :: "memory");
    asm volatile("" : "+v"(bwv[0][0]), "+v"(bwv[0][1]), "+v"(bwv[0][2]), "+v"(bwv[0][3]), "+v"(bwv[1][0]), "+v"(bwv[1][1]), "+v"(bwv[1][2]), "+v"(bwv[1][3]));
#pragma unroll 1
    for (int t = 0; t < 10; t += 2) { qstep(xa, xb, t); qstep(xb, xa, t + 1); }
    asm volatile("s_waitcnt vmcnt(0)\n\ts_barrier" : XOPS(xa) :: "memory");
  }
#pragma unroll
  for (int pc = 0; pc < 6; ++pc) pieceK(96, TB, pc);
  asm volatile("s_nop 15\n\ts_nop 7" ::: "memory");
  SB();
  {
    const int rbA = (q0 >> 5) + odd, rbB = (q0 >> 5) + 1 - odd, cP = 3 * hj + 2 * odd, cQ = 3 * hj + 1;
#pragma unroll
    for (int k = 0; k < 3; ++k) {
      const int rbk = k == 1 ? rbB : rbA, ck = k == 2 ? cQ : cP;
      float g[16];
#pragma unroll
      for (int q = 0; q < 4; ++q) {
        const u32x2 bw = bwv[k == 2 ? 1 : 0][q];
        const float bs[4] = {__uint_as_float(bw[0] << 16), __uint_as_float(bw[0] & 0xffff0000u), __uint_as_float(bw[1] << 16),
                             __uint_as_float(bw[1] & 0xffff0000u)};
#pragma unroll
        for (int e = 0; e < 4; ++e) g[4 * q + e] = gelu_tanh(rbf(rbf(hacc[k][4 * q + e]) + bs[e]));
      }
      u32x4* ho = (u32x4*)hfr + ((size_t)rbk * 96 + 2 * ck) * 64 + lane;
#pragma unroll
      for (int sidx = 0; sidx < 2; ++sidx)
        ho[sidx * 64] = u32x4{cvtpk(g[8 * sidx], g[8 * sidx + 1]), cvtpk(g[8 * sidx + 2], g[8 * sidx + 3]),
                              cvtpk(g[8 * sidx + 4], g[8 * sidx + 5]), cvtpk(g[8 * sidx + 6], g[8 * sidx + 7])};
    }
  }
  SB();
  bf16x8 qf[2][4];
  sfor<24>([&](auto I) {
    constexpr int n = decltype(I)::v, b = n / 12, f = n % 12, R = AO(b, f >> 1) + 8 * (f & 1);
    float t8[8];
    acc_rd8<R>(t8);
    unsigned w0 = cvtpk(t8[0] * QSCALE, t8[1] * QSCALE);
    unsigned w1 = cvtpk(t8[2] * QSCALE, t8[3] * QSCALE);
    unsigned w2 = cvtpk(t8[4] * QSCALE, t8[5] * QSCALE);
    unsigned w3 = cvtpk(t8[6] * QSCALE, t8[7] * QSCALE);
    auto r0 = __builtin_amdgcn_permlane32_swap(w0, w2, false, false);
    auto r1 = __builtin_amdgcn_permlane32_swap(w1, w3, false, false);
    if constexpr (f < 8) {
      acc_wr4<AQ(b, f)>(r0[0], r1[0], r0[1], r1[1]);
    } else {
      u32x4 w = {r0[0], r1[0], r0[1], r1[1]};
      qf[b][f - 8] = __builtin_bit_cast(bf16x8, w);
    }
  });
  zfr = bf16x8{};
  PIN(zfr);
  SB();
  asm volatile("s_nop 7");
  sfor<12>([&](auto I) { acc_zero16<16 * decltype(I)::v>(zfr); });

  float m_run[2], l_run[2] = {0.f, 0.f}, alpha[2];
  bool pend = false;
  f32x16 S[2][2], nm[2];
  u32x4 pw[2][2];
  unsigned kb[4], vb;
  nm[0] = f32x16{}; nm[1] = f32x16{};
  PIN(nm[0]); PIN(nm[1]);
#pragma unroll
  for (int i = 0; i < 4; ++i) { PIN(qf[0][i]); PIN(qf[1][i]); }
#pragma unroll
  for (int i = 0; i < 4; ++i) fr[i] = rdK(koff[i] + 2 * TB + 4096);
  SB();
  asm volatile("s_nop 3" ::: "memory");
  SB();

  sfor<12>([&](auto I) {
    constexpr int i = decltype(I)::v;
    if constexpr (i == 0) { mfma_first<AQ(0, 0)>(S[0][0], fr[0], nm[0]); mfma_first<AQ(1, 0)>(S[0][1], fr[0], nm[1]); }
    else if constexpr (i < 8) { mfma_qa<AQ(0, i)>(S[0][0], fr[i & 3]); mfma_qa<AQ(1, i)>(S[0][1], fr[i & 3]); }
    else { mfma_qv(S[0][0], fr[i & 3], qf[0][i - 8]); mfma_qv(S[0][1], fr[i & 3], qf[1][i - 8]); }
    if constexpr (i + 4 < 12) fr[i & 3] = rdK(koff[i & 3] + 2 * TB + ((i + 4) >> 2) * 8192 + 4096);
    SB();
  });
  asm volatile("s_nop 15\n\ts_nop 7" :: "v"(nm[0]), "v"(nm[1]) : "memory");
  SB();
#pragma unroll
  for (int b = 0; b < 2; ++b) {
    float mx = S[0][b][0];
#pragma unroll
    for (int r = 1; r < 16; ++r) mx = fmaxf(mx, S[0][b][r]);
    auto rr = __builtin_amdgcn_permlane32_swap(__float_as_uint(mx), __float_as_uint(mx), false, false);
    mx = fmaxf(__uint_as_float(rr[0]), __uint_as_float(rr[1]));
    m_run[b] = mx;
#pragma unroll
    for (int r = 0; r < 16; ++r) { nm[b][r] = -mx; S[0][b][r] -= mx; }
  }
#pragma unroll
  for (int j = 0; j < 4; ++j) kb[j] = koff[j];
  vb = voff;
#pragma unroll
  for (int i = 0; i < 4; ++i) fr[i] = rdK(kb[i]);

  auto phaseA = [&](auto XX, auto HH, const char* ssrc, const unsigned sdst) {
    constexpr int X = decltype(XX)::v, HB = decltype(HH)::v;
    sfor<12>([&](auto I) {
      constexpr int i = decltype(I)::v;
      auto ex = [&](auto N) {
        constexpr int n = decltype(N)::v;
        if constexpr (n < 32) {
          constexpr int b = (n & 15) >> 3, r = (n >> 4) * 8 + (n & 7);
          S[X][b][r] = __builtin_amdgcn_exp2f(S[X][b][r]);
        }
      };
      auto pk = [&](auto N) {
        constexpr int n = decltype(N)::v;
        if constexpr (n >= 0 && n < 16) {
          constexpr int b = (n >> 2) & 1, ks = n >> 3, w = n & 3;
          pw[b][ks][w] = cvtpk(S[X][b][8 * ks + 2 * w], S[X][b][8 * ks + 2 * w + 1]);
        }
      };
      if constexpr (i == 0) mfma_first<AQ(0, 0)>(S[X ^ 1][0], fr[0], nm[0]);
      else if constexpr (i < 8) mfma_qa<AQ(0, i)>(S[X ^ 1][0], fr[i & 3]);
      else mfma_qv(S[X ^ 1][0], fr[i & 3], qf[0][i - 8]);
      SB();
      ex(IC<3 * i>{}); ex(IC<3 * i + 1>{});
      pk(IC<2 * i - 8>{});
      PIN(S[X][0]); PIN(S[X][1]);
      SB();
      if constexpr (i == 0) mfma_first<AQ(1, 0)>(S[X ^ 1][1], fr[0], nm[1]);
      else if constexpr (i < 8) mfma_qa<AQ(1, i)>(S[X ^ 1][1], fr[i & 3]);
      else mfma_qv(S[X ^ 1][1], fr[i & 3], qf[1][i - 8]);
      SB();
      if constexpr (i + 4 < 12) fr[i & 3] = rdK(kb[i & 3] + ((i + 4) >> 2) * 8192 + HB * 4096);
      else fr[i & 3] = rdV(vb + ((4 * HB) * 6 + (i - 8)) * 512);
      ex(IC<3 * i + 2>{});
      pk(IC<2 * i - 7>{});
      if constexpr (i == 2) asm volatile("" :: "v"(nm[0]), "v"(nm[1]));
      if constexpr (i == 1 || i == 5 || i == 9) {
        constexpr int pc = (i - 1) / 4;
        if constexpr (HB == 0) glds_i<(pc & 1) * 1024 + (pc >> 1) * 8192>(ssrc, kpo[pc], sdst);
        else glds_i<(pc & 1) * 3072 + (pc >> 1) * 1024>(ssrc, vpo[pc], sdst);
      }
      PIN(S[X][0]); PIN(S[X][1]); PIN(pw[0][0]); PIN(pw[0][1]); PIN(pw[1][0]); PIN(pw[1][1]);
      SB();
    });
  };
  auto phaseB = [&](auto XX, auto HH, auto HK, const char* ssrc, const unsigned sdst) {
    constexpr int X = decltype(XX)::v, HB = decltype(HH)::v, HBKN = decltype(HK)::v;
    float ls[2][2], mx[2];
    auto fill = [&](auto G) {
      constexpr int g = decltype(G)::v;
      if constexpr (g < 16) {
        constexpr int b = g >> 3, r = (g & 7) * 2;
        if constexpr ((g & 7) == 0) { ls[b][0] = S[X][b][r]; ls[b][1] = S[X][b][r + 1]; }
        else { ls[b][0] += S[X][b][r]; ls[b][1] += S[X][b][r + 1]; }
        PIN(ls[b][0]); PIN(ls[b][1]);
      }
      if constexpr (g == 16) { l_run[0] += ls[0][0] + ls[0][1]; PIN(l_run[0]); }
      if constexpr (g == 17) { l_run[1] += ls[1][0] + ls[1][1]; PIN(l_run[1]); }
      if constexpr (g == 20) {
#pragma unroll
        for (int b = 0; b < 2; ++b) {
          auto rr = __builtin_amdgcn_permlane32_swap(__float_as_uint(mx[b]), __float_as_uint(mx[b]), false, false);
          mx[b] = max2f(__uint_as_float(rr[0]), __uint_as_float(rr[1]));
          PIN(mx[b]);
        }
      }
      if constexpr (g == 21) {
        if (__builtin_expect(__any(max2f(mx[0], mx[1]) > THR), 0)) {
          const float aone = hi == 0 ? 1.f : 0.f;
#pragma unroll
          for (int b = 0; b < 2; ++b) {
            const float dl = fmaxf(mx[b], 0.f);
            m_run[b] += dl;
            alpha[b] = __builtin_amdgcn_exp2f(-dl);
            bcast16(nm[b], aone, -m_run[b]);
#pragma unroll
            for (int r = 0; r < 16; ++r) S[X ^ 1][b][r] -= dl;
          }
          pend = true;
        }
      }
      if constexpr (g == 16 || g == 18 || g == 22) {
        constexpr int pc = g == 16 ? 3 : (g == 18 ? 4 : 5);
        if constexpr (HB == 0) glds_i<(pc & 1) * 1024 + (pc >> 1) * 8192>(ssrc, kpo[pc], sdst);
        else glds_i<(pc & 1) * 3072 + (pc >> 1) * 1024>(ssrc, vpo[pc], sdst);
      }
    };
    auto pvm = [&](auto G) {
      constexpr int g = decltype(G)::v, i = g >> 1, b = g & 1, k = (g - 4) >> 1;
      const bf16x8 pb = __builtin_bit_cast(bf16x8, pw[b][i / 6]);
      if constexpr (g >= 4 && g < 20) {
        if constexpr (k == 0) mfma_o_mx<AO(b, i % 6)>(fr[i & 3], pb, mx[b], S[X ^ 1][b][0], S[X ^ 1][b][1], S[X ^ 1][b][2]);
        else if constexpr (k < 7) mfma_o_mx<AO(b, i % 6)>(fr[i & 3], pb, mx[b], mx[b], S[X ^ 1][b][2 * k + 1], S[X ^ 1][b][2 * k + 2]);
        else mfma_o_mx<AO(b, i % 6)>(fr[i & 3], pb, mx[b], mx[b], S[X ^ 1][b][15], S[X ^ 1][b][15]);
      } else mfma_o<AO(b, i % 6)>(fr[i & 3], pb);
    };
    sfor<12>([&](auto I) {
      constexpr int i = decltype(I)::v;
      pvm(IC<2 * i>{});
      SB();
      fill(IC<2 * i>{});
      SB();
      pvm(IC<2 * i + 1>{});
      SB();
      if constexpr (i + 4 < 12) fr[i & 3] = rdV(vb + ((4 * HB + 2 * ((i + 4) / 6)) * 6 + ((i + 4) % 6)) * 512);
      else fr[i & 3] = rdK(kb[i - 8] + HBKN * 4096);
      fill(IC<2 * i + 1>{});
      SB();
    });
    if (__builtin_expect(pend, 0)) {
      asm volatile("s_nop 15\n\ts_nop 7" ::: "memory");
      l_run[0] *= alpha[0]; l_run[1] *= alpha[1];
      sfor<192>([&](auto I) { constexpr int n = decltype(I)::v; acc_scale<n>(alpha[n / 96]); });
      asm volatile("s_nop 3" ::: "memory");
      pend = false;
    }
  };

  asm volatile("s_waitcnt vmcnt(0)" ::: "memory");
  unsigned scur = 0;
#pragma unroll 1
  for (int t = 0; t < 32; ++t) {
    const unsigned snxt = nxt(scur), swr = nxt(snxt);
    asm volatile("s_waitcnt vmcnt(6)\n\ts_barrier" ::: "memory");
    int tk = t + 2 < 32 ? t + 2 : 31;
    const int tv = tk;
    if (tk == 31 && wid >= 2) tk = 30;
    const char* ks_ = kbase + (size_t)(tk * 64 + 32) * (DH * 2);
    const char* vs_ = vbase + (size_t)(tv * 64) * (DH * 2);
    SB();
    phaseA(IC<0>{}, IC<0>{}, ks_, kdst + swr);
    phaseB(IC<0>{}, IC<0>{}, IC<1>{}, ks_, kdst + swr);
    phaseA(IC<1>{}, IC<1>{}, vs_, vdst + swr);
#pragma unroll
    for (int j = 0; j < 4; ++j) kb[j] = koff[j] + snxt;
    phaseB(IC<1>{}, IC<1>{}, IC<0>{}, vs_, vdst + swr);
    vb = voff + snxt;
    scur = snxt;
  }
  asm volatile("s_waitcnt vmcnt(0)\n\ts_nop 15\n\ts_nop 7" ::: "memory");
  int tz = threadIdx.x;
  asm volatile("" : "+v"(tz));
  float rls[2];
#pragma unroll
  for (int b = 0; b < 2; ++b) {
    auto rr = __builtin_amdgcn_permlane32_swap(__float_as_uint(l_run[b]), __float_as_uint(l_run[b]), false, false);
    rls[b] = 1.0f / (__uint_as_float(rr[0]) + __uint_as_float(rr[1]));
  }
  u32x4* ofr = (u32x4*)O + ((size_t)(qb * 8 + (tz >> 6) * 2) * 384 + head * 12) * 64 + (tz & 63);
  sfor<24>([&](auto I) {
    constexpr int n = decltype(I)::v, b = n / 12, f = n % 12, R = AO(b, f >> 1) + 8 * (f & 1);
    const float rl = rls[b];
    float t8[8];
    acc_rd8<R>(t8);
    __builtin_nontemporal_store(u32x4{cvtpk(t8[0] * rl, t8[1] * rl), cvtpk(t8[2] * rl, t8[3] * rl),
                                      cvtpk(t8[4] * rl, t8[5] * rl), cvtpk(t8[6] * rl, t8[7] * rl)}, &ofr[(b * 384 + f) * 64]);
  });
}

extern "C" void kernel_launch(void* const* d_in, const int* in_sizes, int n_in, void* d_out, int out_size,
                              void* d_ws, size_t ws_size, hipStream_t stream) {
  const u16* x = (const u16*)d_in[0];
  const u16* kc = (const u16*)d_in[1];
  const u16* vc = (const u16*)d_in[2];
  const u16* wq = (const u16*)d_in[3];
  const u16* wo = (const u16*)d_in[4];
  const u16* w1 = (const u16*)d_in[6];
  const u16* b1 = (const u16*)d_in[7];
  const u16* w2 = (const u16*)d_in[8];
  const u16* b2 = (const u16*)d_in[9];
  char* ws = (char*)d_ws;
  u16* xnp = (u16*)(ws);
  u16* y = (u16*)(ws + (4u << 20));
  u16* h = (u16*)(ws + (8u << 20));
  u16* O = (u16*)(ws + (16u << 20));
  u16* part = (u16*)(ws + (48u << 20));

  hipFuncSetAttribute((const void*)k_attn, hipFuncAttributeMaxDynamicSharedMemorySize, 6 * TB);
  hipFuncSetAttribute((const void*)k_gemm2, hipFuncAttributeMaxDynamicSharedMemorySize, 4 * 16384);

  k_ln<<<SEQ / 4, 256, 0, stream>>>(x, xnp, y);
  k_attn<<<256, 256, 6 * TB, stream>>>(xnp, wq, kc, vc, O, y, w1, b1, h);
  k_gemm2<<<200, 256, 4 * 16384, stream>>>(O, wo, h, w2, part);
  k_final<<<SEQ * DM / 2048, 256, 0, stream>>>(x, part, b2, (u16*)d_out);
}
```
